```python
import torch

def _softmax(x, axis=-1):
    return torch.softmax(x, dim=axis)
def _gelu(x):
    return torch.nn.functional.gelu(x, approximate="tanh")
def _silu(x):
    return torch.nn.functional.silu(x)
def _sigmoid(x):
    return torch.sigmoid(x)
def _layernorm(x, axis=-1, eps=1e-5):
    m = x.mean(dim=axis, keepdim=True)
    v = x.var(dim=axis, keepdim=True, unbiased=False)
    return (x - m) / torch.sqrt(v + eps)
def _scatter_add(tgt, idx, src, axis=0):
    return tgt.clone().index_add_(axis, idx, src)
def _segment_sum(src, seg, n):
    out = torch.zeros((n,) + src.shape[1:], dtype=src.dtype)
    return out.index_add_(0, seg, src)
def _pad(x, pw):
    flat = tuple(v for pair in reversed(pw) for v in pair)
    return torch.nn.functional.pad(x, flat)

kernel_name = 'gated_mix__linear_attn_block__sliding_local_dilated__prompt'

def setup_inputs():

    _w = (lambda a: (lambda t: t.to(torch.bfloat16) if t.is_floating_point() else t)(torch.as_tensor(a)))
    def _A_setup():
        B, S, D = 1, 2048, 512
        return {
            "x": _w((lambda *s: torch.randn(*s, dtype=torch.bfloat16) * 0.05)(B, S, D)),
            "wqkv": _w((lambda *s: torch.randn(*s, dtype=torch.bfloat16) * 1.6)(D, 3 * D)),
            "wo": _w((lambda *s: torch.randn(*s, dtype=torch.bfloat16) * 0.4)(D, D)),
        }
    _A_in = _A_setup()

    def _B_setup():
        B, S, D = 1, 2048, 512
        return {
            "x": _w((lambda *s: torch.randn(*s, dtype=torch.bfloat16) * 0.05)(B, S, D)),
            "wqkv": _w((lambda *s: torch.randn(*s, dtype=torch.bfloat16) * 0.8)(D, 3 * D)),
            "wo": _w((lambda *s: torch.randn(*s, dtype=torch.bfloat16) * 0.05)(D, D)),
        }
    _B_in = _B_setup()

    return {
        "A_x": _A_in["x"],
        "A_wqkv": _A_in["wqkv"],
        "A_wo": _A_in["wo"],
        "B_x": _B_in["x"],
        "B_wqkv": _B_in["wqkv"],
        "B_wo": _B_in["wo"],
        "gate_w": _w((lambda *s: torch.randn(*s, dtype=torch.bfloat16) * 0.05)(512) * 16.0),
    }

def reference(A_x, A_wqkv, A_wo, B_x, B_wqkv, B_wo, gate_w):
    def _A_ref(x, wqkv, wo):
        B, S, D = x.shape
        H = 32; Dh = D // H
        qkv = (lambda x, *d: x.permute(*d))((x @ wqkv).reshape(B, S, 3, H, Dh), 2, 0, 3, 1, 4)
        q, k, v = qkv[0], qkv[1], qkv[2]
        phi = lambda t: torch.where(t > 0, t + 1, torch.exp(t)) + 1e-6
        pq, pk = phi(q), phi(k)
        kv = torch.einsum("bhsd,bhse->bhde", pk, v)
        z = torch.einsum("bhsd->bhd", pk)
        num = torch.einsum("bhqd,bhde->bhqe", pq, kv)
        den = torch.einsum("bhqd,bhd->bhq", pq, z)[..., None] + 1e-6
        o = num / den
        return (lambda x, *d: x.permute(*d))(o, 0, 2, 1, 3).reshape(B, S, D) @ wo

    def _B_ref(x, wqkv, wo):
        B, S, D = x.shape
        H = 32; Dh = D // H
        W, dil = 8, 2
        qkv = (lambda x, *d: x.permute(*d))((x @ wqkv).reshape(B, S, 3, H, Dh), 2, 0, 3, 1, 4)
        q, k, v = qkv[0], qkv[1], qkv[2]
        scale = Dh ** -0.5
        pos = torch.arange(S)
        off = torch.arange(W) * dil
        idx = pos[:, None] - off[None, :]
        mask = idx >= 0
        idx_c = torch.where(mask, idx, 0).reshape(-1)
        kg = k[:, :, idx_c, :].reshape(B, H, S, W, Dh)
        vg = v[:, :, idx_c, :].reshape(B, H, S, W, Dh)
        logits = torch.einsum("bhsd,bhswd->bhsw", q, kg) * scale
        logits = torch.where(mask[None, None, :, :], logits, -1e9)
        attn = _softmax(logits, axis=-1)
        o = torch.einsum("bhsw,bhswd->bhsd", attn, vg)
        return (lambda x, *d: x.permute(*d))(o, 0, 2, 1, 3).reshape(B, S, D) @ wo

    x = A_x
    g = _sigmoid((x * gate_w).sum(-1, keepdims=True))
    a = _A_ref(x, A_wqkv, A_wo)
    b = _B_ref(x, B_wqkv, B_wo)
    return g * a + (1 - g) * b
```

```cpp
#include <hip/hip_runtime.h>
#include <hip/hip_bf16.h>
typedef __hip_bfloat16 bf16;
constexpr int S=2048, D=512, H=32, DH=16;
__device__ __forceinline__ float b2f(bf16 v){return __bfloat162float(v);}
__device__ __forceinline__ float rb(float v){return __bfloat162float(__float2bfloat16(v));}

__global__ void k_qkv(const bf16* x, const bf16* wa, const bf16* wb, float* qkv){
  int n = blockIdx.x*256+threadIdx.x;
  int s0 = blockIdx.y*8;
  const bf16* w = n < 1536 ? wa : wb; int nn = n < 1536 ? n : n-1536;
  float acc[8]={0,0,0,0,0,0,0,0};
  for(int k=0;k<D;k++){ float wv=b2f(w[k*1536+nn]);
    #pragma unroll
    for(int i=0;i<8;i++) acc[i]+=b2f(x[(s0+i)*D+k])*wv; }
  #pragma unroll
  for(int i=0;i<8;i++) qkv[(size_t)(s0+i)*3072+n]=rb(acc[i]);
}
__device__ __forceinline__ float phi(float t){ return (t>0.f? t+1.f : __expf(t)) + 1e-6f; }
__global__ void k_kv(const float* qkv, float* kv){
  int h=blockIdx.x, t=threadIdx.x; if(t>=272) return;
  float acc=0;
  if(t<256){int d=t>>4,e=t&15; for(int s=0;s<S;s++) acc+=phi(qkv[(size_t)s*3072+512+h*16+d])*qkv[(size_t)s*3072+1024+h*16+e];}
  else {int d=t-256; for(int s=0;s<S;s++) acc+=phi(qkv[(size_t)s*3072+512+h*16+d]);}
  kv[h*272+t]=acc;
}
__global__ void k_o(const float* qkv, const float* kv, float* o){
  int s=blockIdx.x; int c=threadIdx.x;
  int h=c>>4,e=c&15;
  float num=0,den=0;
  for(int d=0;d<16;d++){ float p=phi(qkv[(size_t)s*3072+h*16+d]); num+=p*kv[h*272+d*16+e]; den+=p*kv[h*272+256+d]; }
  o[s*1024+c]=rb(num/(den+1e-6f));
  float lg[8]; float m=-1e30f;
  for(int w=0;w<8;w++){ int j=s-2*w; if(j>=0){ float a=0; for(int d=0;d<16;d++) a+=qkv[(size_t)s*3072+1536+h*16+d]*qkv[(size_t)j*3072+2048+h*16+d]; lg[w]=a*0.25f;} else lg[w]=-1e9f; m=fmaxf(m,lg[w]); }
  float sum=0,acc=0;
  for(int w=0;w<8;w++){ int j=s-2*w; float p=__expf(lg[w]-m); sum+=p; if(j>=0) acc+=p*qkv[(size_t)j*3072+2560+h*16+e]; }
  o[s*1024+512+c]=rb(acc/sum);
}
__global__ void k_out(const bf16* x,const bf16* gw,const float* o,const bf16* woa,const bf16* wob,bf16* out){
  int s=blockIdx.x,n=threadIdx.x; __shared__ float red[512];
  red[n]=b2f(x[s*D+n])*b2f(gw[n]); __syncthreads();
  for(int st=256;st>0;st>>=1){ if(n<st) red[n]+=red[n+st]; __syncthreads(); }
  float g=1.f/(1.f+__expf(-red[0]));
  float a=0,b=0;
  for(int k=0;k<D;k++){ a+=o[s*1024+k]*b2f(woa[k*D+n]); b+=o[s*1024+512+k]*b2f(wob[k*D+n]); }
  out[s*D+n]=__float2bfloat16(g*a+(1.f-g)*b);
}
extern "C" void kernel_launch(void* const* d_in, const int* in_sizes, int n_in, void* d_out, int out_size, void* d_ws, size_t ws_size, hipStream_t stream){
  const bf16* x=(const bf16*)d_in[0]; const bf16* wa=(const bf16*)d_in[1]; const bf16* woa=(const bf16*)d_in[2];
  const bf16* wb=(const bf16*)d_in[4]; const bf16* wob=(const bf16*)d_in[5]; const bf16* gw=(const bf16*)d_in[6];
  float* qkv=(float*)d_ws; float* kv=qkv+(size_t)S*3072; float* o=kv+32*272;
  k_qkv<<<dim3(12,S/8),256,0,stream>>>(x,wa,wb,qkv);
  k_kv<<<32,320,0,stream>>>(qkv,kv);
  k_o<<<S,512,0,stream>>>(qkv,kv,o);
  k_out<<<S,512,0,stream>>>(x,gw,o,woa,wob,(bf16*)d_out);
}
```

```cpp
#include <hip/hip_runtime.h>
#include <hip/hip_bf16.h>
#include <stdint.h>

typedef __bf16 bf16x2 __attribute__((ext_vector_type(2)));
typedef __bf16 bf16x8 __attribute__((ext_vector_type(8)));
typedef short s16x4 __attribute__((ext_vector_type(4)));
typedef float f32x2 __attribute__((ext_vector_type(2)));
typedef float f32x4 __attribute__((ext_vector_type(4)));
typedef float f32x16 __attribute__((ext_vector_type(16)));
typedef unsigned u32x2 __attribute__((ext_vector_type(2)));
typedef unsigned u32x4 __attribute__((ext_vector_type(4)));
typedef unsigned long long u64;
#define LAS __attribute__((address_space(3)))
#define GAS __attribute__((address_space(1)))
#define DI __device__ __forceinline__
typedef LAS char* lptr;
typedef GAS u64 gu64;
typedef __amdgpu_buffer_rsrc_t rsrc_t;

constexpr unsigned WS_EO = 0;
constexpr unsigned WS_EKV = 2048;
constexpr unsigned WS_EH = 4096;
constexpr unsigned WS_FKV = 32768;
constexpr unsigned WS_FH = 65536;
constexpr unsigned WS_FO = 131072;
constexpr unsigned WS_KVP = 262144 + 65536;
constexpr unsigned KVP_BYTES = 4352;
constexpr unsigned WS_HALO = 1048576;
constexpr unsigned WS_O = 2097152;
constexpr unsigned WS_STAMP = 8u << 20;
constexpr unsigned WS_USED = 16u << 20;

constexpr int SLOT = 32768;
constexpr int L_GW = 131072;
constexpr int L_G = L_GW + 1024;
constexpr int L_PART = L_GW + 2048;
constexpr int L_TOT = L_PART + 8 * 2176;
constexpr int LDS_BYTES = 163840;
constexpr int PROW = 272;

constexpr float LOG2E = 1.4426950408889634f;
constexpr unsigned SPIN_CAP = 1u << 16;

#define MFMA(a, b, c) __builtin_amdgcn_mfma_f32_32x32x16_bf16((a), (b), (c), 0, 0, 0)
#define WAIT_BAR(N) asm volatile("s_waitcnt vmcnt(" #N ") lgkmcnt(0)\n\ts_barrier" ::: "memory")
#define WAIT_VM(N) asm volatile("s_waitcnt vmcnt(" #N ")" ::: "memory")
#define LBAR() asm volatile("s_waitcnt lgkmcnt(0)\n\ts_barrier" ::: "memory")

#define STAMP(i) do {} while (0)

DI unsigned pk(float a, float b) { return __builtin_bit_cast(unsigned, __builtin_convertvector((f32x2){a, b}, bf16x2)); }
DI bf16x8 pack8(const f32x16& a, int o) {
  u32x4 r = {pk(a[o], a[o + 1]), pk(a[o + 2], a[o + 3]), pk(a[o + 4], a[o + 5]), pk(a[o + 6], a[o + 7])};
  return __builtin_bit_cast(bf16x8, r);
}
DI float phi(float t) {
  const float e = __builtin_amdgcn_fmed3f(__builtin_amdgcn_exp2f(t * LOG2E), 0.f, 1.f);
  return __builtin_fmaxf(t + 1.f, e) + 1e-6f;
}
DI void dma16(unsigned lds, unsigned voff, rsrc_t rs, unsigned soff) {
  asm volatile("s_mov_b32 m0, %0\n\ts_nop 0\n\tbuffer_load_dwordx4 %1, %2, %3 offen lds" ::"s"(lds), "v"(voff), "s"(rs), "s"(soff) : "memory");
}
DI bf16x8 lds_frag(lptr p) { return *(const LAS bf16x8*)p; }
DI bf16x8 tr_frag(lptr p, int o0, int o1) {
  const s16x4 lo = __builtin_bit_cast(s16x4, __builtin_amdgcn_ds_read_tr16_b64_v4i16((LAS s16x4*)(p + o0)));
  const s16x4 hi = __builtin_bit_cast(s16x4, __builtin_amdgcn_ds_read_tr16_b64_v4i16((LAS s16x4*)(p + o1)));
  return __builtin_bit_cast(bf16x8, __builtin_shufflevector(lo, hi, 0, 1, 2, 3, 4, 5, 6, 7));
}
DI u64 sload64(const char* p) {
  u64 r;
  asm volatile("s_load_dwordx2 %0, %1, 0x0\n\ts_waitcnt lgkmcnt(0)" : "=s"(r) : "s"(p) : "memory");
  return r;
}
DI void flag_store(char* ws, unsigned off, u64 v) { __hip_atomic_store((gu64*)(ws + off), v, __ATOMIC_RELAXED, __HIP_MEMORY_SCOPE_AGENT); }
DI void poll_flags(char* ws, unsigned off0, unsigned stride, int n, u64 tag, int lane) {
  gu64* p = (gu64*)(ws + off0 + (unsigned)lane * stride);
  for (unsigned spins = 0; spins < SPIN_CAP; ++spins) {
    u64 v = tag;
    if (lane < n) v = __hip_atomic_load(p, __ATOMIC_RELAXED, __HIP_MEMORY_SCOPE_AGENT);
    if (__all(v == tag)) break;
    __builtin_amdgcn_s_sleep(1);
  }
  __builtin_amdgcn_fence(__ATOMIC_ACQUIRE, "wavefront");
  asm volatile("" ::: "memory");
}
DI float swap_max(float m) {
  auto r = __builtin_amdgcn_permlane32_swap(__float_as_uint(m), __float_as_uint(m), false, false);
  return __builtin_fmaxf(__uint_as_float(r[0]), __uint_as_float(r[1]));
}

template <int BR>
DI void phase1(char* smem_generic, const rsrc_t rsX, const rsrc_t rsW, const rsrc_t rsWoA, const rsrc_t rsWoB, const rsrc_t rsWS, char* ws,
               const int rt, const int quad, const int wid, const int lane, const unsigned vX0, const unsigned vX1, const unsigned vW,
               const unsigned vWo, const u64 tagG, const u64 tagP, const u64 tagO) {
  const lptr L = (lptr)smem_generic;
  const unsigned lds0 = (unsigned)(uintptr_t)L;
  const int c = wid >> 2, rq = wid & 3, p = rq & 1, sp = rq >> 1;
  const int i32 = lane & 31, h = lane >> 5;
  const unsigned pieceX = lds0 + wid * 2048, pieceW = lds0 + wid * 1024;

  auto fill = [&](int f) {
    const unsigned sl = (unsigned)(f & 3) * SLOT;
    if (f < 8) {
      dma16(pieceX + sl, vX0, rsX, f * 128);
      dma16(pieceX + sl + 1024, vX1, rsX, f * 128);
      dma16(pieceW + sl + 16384, vW, rsW, f * 196608 + 1024);
      dma16(pieceW + sl + 24576, vW, rsW, f * 196608 + 2048);
    } else if (f < 10) {
#pragma unroll
      for (int i = 0; i < 4; ++i) dma16(pieceW + sl + i * 8192, vW, rsW, ((f - 8) * 256 + i * 64) * 3072);
    } else {
      const int kq = f - 10;
      const unsigned so = (unsigned)(kq == 0 ? 2 : kq == 1 ? 3 : kq == 2 ? 0 : 1) * SLOT;
#pragma unroll
      for (int i = 0; i < 4; ++i) dma16(pieceW + so + i * 8192, vWo, kq < 2 ? rsWoA : rsWoB, ((kq & 1) * 256 + i * 64) * 1024);
    }
  };

  const lptr xb = L + (32 * rq + i32) * 128;
  const int xs = h ^ ((i32 >> 1) & 7);
  const int q4 = (lane & 15) >> 2, p4 = lane & 3, nsub = (lane >> 4) & 1;
  const int wchunk = (4 * c + 2 * nsub + (p4 >> 1)) ^ (((q4 >> 1) & 1) << 2);
  const lptr wb = L + (8 * h + q4) * 128 + wchunk * 16 + 8 * (p4 & 1);

  bf16x8 xf[32];
  f32x16 accK = {}, accV = {};
#pragma unroll
  for (int ch = 0; ch < 8; ++ch) {
    WAIT_BAR(8);
    fill(ch + 3);
    const int sl = (ch & 3) * SLOT;
#pragma unroll
    for (int t = 0; t < 4; ++t) {
      xf[ch * 4 + t] = lds_frag(xb + sl + ((xs ^ (2 * t)) << 4));
      const bf16x8 wk = tr_frag(wb + sl + 16384, t * 2048, t * 2048 + 512);
      const bf16x8 wv = tr_frag(wb + sl + 24576, t * 2048, t * 2048 + 512);
      accK = BR == 0 ? MFMA(xf[ch * 4 + t], wk, accK) : MFMA(wk, xf[ch * 4 + t], accK);
      accV = MFMA(xf[ch * 4 + t], wv, accV);
    }
  }
  STAMP(2);
  bf16x8 Kf[2], Vf[2];
  if (BR == 0) {
#pragma unroll
    for (int r = 0; r < 16; ++r) accK[r] = phi(accK[r]);
  }
  Kf[0] = pack8(accK, 0); Kf[1] = pack8(accK, 8);
  Vf[0] = pack8(accV, 0); Vf[1] = pack8(accV, 8);
  if (BR == 0) {
    const u32x4 one4 = {0x3F803F80u, 0x3F803F80u, 0x3F803F80u, 0x3F803F80u};
    const bf16x8 ones = __builtin_bit_cast(bf16x8, one4);
    f32x16 kv = {}, z = {};
    kv = MFMA(Kf[0], Vf[0], kv); z = MFMA(Kf[0], ones, z);
    kv = MFMA(Kf[1], Vf[1], kv); z = MFMA(Kf[1], ones, z);
    const bool lo = i32 < 16;
    f32x4 u0, u1;
#pragma unroll
    for (int j = 0; j < 4; ++j) { u0[j] = lo ? kv[j] : kv[8 + j]; u1[j] = lo ? kv[4 + j] : kv[12 + j]; }
    const lptr pp = L + L_PART + wid * 2176;
    *(LAS f32x4*)(pp + lane * 16) = u0;
    *(LAS f32x4*)(pp + 1024 + lane * 16) = u1;
    if (i32 == 0) {
#pragma unroll
      for (int g4 = 0; g4 < 4; ++g4) *(LAS f32x4*)(pp + 2048 + h * 64 + g4 * 16) = (f32x4){z[4 * g4], z[4 * g4 + 1], z[4 * g4 + 2], z[4 * g4 + 3]};
    }
  } else {
    if (sp == 0) {
      const lptr ex = L + L_PART + (c * 2 + p) * 1024;
      if (i32 >= 24) {
        *(LAS bf16x8*)(ex + h * 128 + (i32 - 24) * 16) = Kf[0];
        *(LAS bf16x8*)(ex + 256 + h * 128 + (i32 - 24) * 16) = Kf[1];
      }
      const u32x4 v1 = __builtin_bit_cast(u32x4, Vf[1]);
      *(LAS u32x2*)(ex + 512 + lane * 8) = (u32x2){v1[2], v1[3]};
    }
  }
  WAIT_BAR(4);
  STAMP(3);
  if (BR == 0) {
    if (wid == 7) {
#pragma unroll
      for (int it = 0; it < 5; ++it) {
        const int u = it * 64 + lane;
        if (u < 272) {
          const int cc = u < 256 ? (u >> 7) : ((u - 256) >> 3);
          const int o = u < 256 ? (u & 127) * 16 : 2048 + (u & 7) * 16;
          const lptr s = L + L_PART + cc * 4 * 2176 + o;
          const f32x4 a = *(LAS f32x4*)s + *(LAS f32x4*)(s + 2176) + (*(LAS f32x4*)(s + 2 * 2176) + *(LAS f32x4*)(s + 3 * 2176));
          __builtin_amdgcn_raw_buffer_store_b128(__builtin_bit_cast(u32x4, a), rsWS, WS_KVP + (quad * 16 + rt) * KVP_BYTES + u * 16, 0, 16);
        }
      }
    }
  } else {
    if (sp == 1 && rt < 15) {
      const unsigned ex = WS_HALO + ((rt * 8 + quad) * 4 + c * 2 + p) * 1024;
      if (i32 >= 24) {
        __builtin_amdgcn_raw_buffer_store_b128(__builtin_bit_cast(u32x4, Kf[0]), rsWS, ex + h * 128 + (i32 - 24) * 16, 0, 16);
        __builtin_amdgcn_raw_buffer_store_b128(__builtin_bit_cast(u32x4, Kf[1]), rsWS, ex + 256 + h * 128 + (i32 - 24) * 16, 0, 16);
      }
      const u32x4 v1 = __builtin_bit_cast(u32x4, Vf[1]);
      __builtin_amdgcn_raw_buffer_store_b64((u32x2){v1[2], v1[3]}, rsWS, ex + 512 + lane * 8, 0, 16);
    }
  }
  fill(11);
  f32x16 accQ = {};
#pragma unroll
  for (int t = 0; t < 16; ++t) accQ = MFMA(tr_frag(wb, t * 2048, t * 2048 + 512), xf[t], accQ);
  {
    const bool pub = BR == 0 ? (wid == 7) : (sp == 1 && rt < 15);
    if (pub) {
      WAIT_VM(4);
      if (lane == 0) flag_store(ws, BR == 0 ? WS_FKV + (quad * 16 + rt) * 128 : WS_FH + ((rt * 8 + quad) * 4 + c * 2 + p) * 64, tagP);
    }
  }
#pragma unroll
  for (int t = 0; t < 16; ++t) accQ = MFMA(tr_frag(wb + SLOT, t * 2048, t * 2048 + 512), xf[16 + t], accQ);
  if (BR == 0) {
#pragma unroll
    for (int r = 0; r < 16; ++r) accQ[r] = phi(accQ[r]);
  }
  bf16x8 Qf[2] = {pack8(accQ, 0), pack8(accQ, 8)};
  STAMP(4);
  if (c == 0) {
    f32x16 ga = {};
#pragma unroll
    for (int t = 0; t < 32; ++t) ga = MFMA(lds_frag(L + L_GW + t * 32 + h * 16), xf[t], ga);
    const float g = __builtin_amdgcn_rcpf(1.f + __builtin_amdgcn_exp2f(-LOG2E * ga[0]));
    if (h == 0) *(LAS float*)(L + L_G + (rq * 32 + i32) * 4) = BR == 0 ? g : 1.f - g;
  }
  const unsigned ofrag = WS_O + ((((rt * 2 + sp) * 2 + p) * 64) + BR * 32 + quad * 4 + 2 * c) * 1024 + lane * 16;
  if (BR == 0) {
    const int u = wid >= 4 ? (wid - 4) * 64 + lane : (wid == 0 && lane < 16 ? 256 + lane : -1);
    if (wid >= 4 || wid == 0) {
      poll_flags(ws, WS_FKV + quad * 16 * 128, 128, 16, tagG, lane);
      STAMP(5);
      if (u >= 0) {
        f32x4 a[16];
#pragma unroll
        for (int r = 0; r < 16; ++r)
          a[r] = __builtin_bit_cast(f32x4, __builtin_amdgcn_raw_buffer_load_b128(rsWS, WS_KVP + quad * 16 * KVP_BYTES + u * 16, r * KVP_BYTES, 16));
#pragma unroll
        for (int s = 1; s < 16; s *= 2)
#pragma unroll
          for (int r = 0; r < 16; r += 2 * s) a[r] += a[r + s];
        *(LAS f32x4*)(L + L_TOT + u * 16) = a[0];
      }
      if (rt == 0 && wid == 0 && lane == 0) flag_store(ws, WS_EKV + quad * 128, tagG);
    }
    LBAR();
    STAMP(6);
    const float gs = *(LAS float*)(L + L_G + (rq * 32 + i32) * 4);
    const f32x4 u0 = *(LAS f32x4*)(L + L_TOT + (c * 128 + lane) * 16), u1 = *(LAS f32x4*)(L + L_TOT + (c * 128 + 64 + lane) * 16);
    const lptr zp = L + L_TOT + 4096 + c * 128 + h * 64;
    const f32x4 z0 = *(LAS f32x4*)zp, z1 = *(LAS f32x4*)(zp + 16), z2 = *(LAS f32x4*)(zp + 32), z3 = *(LAS f32x4*)(zp + 48);
    const bool lo = i32 < 16;
    const u32x4 pu = {pk(u0[0], u0[1]), pk(u0[2], u0[3]), pk(u1[0], u1[1]), pk(u1[2], u1[3])};
    const u32x4 pz0 = {pk(z0[0], z0[1]), pk(z0[2], z0[3]), pk(z1[0], z1[1]), pk(z1[2], z1[3])};
    const u32x4 pz1 = {pk(z2[0], z2[1]), pk(z2[2], z2[3]), pk(z3[0], z3[1]), pk(z3[2], z3[3])};
    u32x4 a0, a1;
#pragma unroll
    for (int j = 0; j < 4; ++j) { a0[j] = lo ? pu[j] : pz0[j]; a1[j] = lo ? pz1[j] : pu[j]; }
    const f32x16 zero = {};
    const f32x16 d0 = MFMA(__builtin_bit_cast(bf16x8, a0), Qf[0], zero);
    const f32x16 d1 = MFMA(__builtin_bit_cast(bf16x8, a1), Qf[1], zero);
    const float s0 = gs * __builtin_amdgcn_rcpf(d0[8] + 1e-6f), s1 = gs * __builtin_amdgcn_rcpf(d1[0] + 1e-6f);
    const u32x4 o0 = {pk(d0[0] * s0, d0[1] * s0), pk(d0[2] * s0, d0[3] * s0), pk(d0[4] * s0, d0[5] * s0), pk(d0[6] * s0, d0[7] * s0)};
    const u32x4 o1 = {pk(d1[8] * s1, d1[9] * s1), pk(d1[10] * s1, d1[11] * s1), pk(d1[12] * s1, d1[13] * s1), pk(d1[14] * s1, d1[15] * s1)};
    __builtin_amdgcn_raw_buffer_store_b128(o0, rsWS, ofrag, 0, 16);
    __builtin_amdgcn_raw_buffer_store_b128(o1, rsWS, ofrag + 1024, 0, 16);
  } else {
    LBAR();
    STAMP(5);
    bf16x8 Kp[2];
    u32x2 vp;
    if (sp == 1) {
      const lptr ex = L + L_PART + (c * 2 + p) * 1024;
      Kp[0] = lds_frag(ex + h * 128 + (i32 & 7) * 16);
      Kp[1] = lds_frag(ex + 256 + h * 128 + (i32 & 7) * 16);
      vp = *(LAS u32x2*)(ex + 512 + lane * 8);
    } else if (rt > 0) {
      poll_flags(ws, WS_FH + (((rt - 1) * 8 + quad) * 4 + c * 2 + p) * 64, 64, 1, tagG, lane);
      const unsigned ex = WS_HALO + (((rt - 1) * 8 + quad) * 4 + c * 2 + p) * 1024;
      Kp[0] = __builtin_bit_cast(bf16x8, __builtin_amdgcn_raw_buffer_load_b128(rsWS, ex + h * 128 + (i32 & 7) * 16, 0, 16));
      Kp[1] = __builtin_bit_cast(bf16x8, __builtin_amdgcn_raw_buffer_load_b128(rsWS, ex + 256 + h * 128 + (i32 & 7) * 16, 0, 16));
      vp = __builtin_amdgcn_raw_buffer_load_b64(rsWS, ex + 512 + lane * 8, 0, 16);
      if (wid == 0 && lane == 0) flag_store(ws, WS_EH + ((rt - 1) * 8 + quad) * 128, tagG);
    } else {
      Kp[0] = Kf[0]; Kp[1] = Kf[1]; vp = (u32x2){0u, 0u};
    }
    STAMP(6);
    const bool noprev = (rt == 0 && sp == 0);
    f32x16 bias;
#pragma unroll
    for (int r = 0; r < 16; ++r) bias[r] = ((unsigned)(i32 - ((r & 3) + 8 * (r >> 2) + 4 * h)) <= 7u) ? 0.f : -1e30f;
    float bprev[4];
#pragma unroll
    for (int j = 0; j < 4; ++j) bprev[j] = (!noprev && (24 + 4 * h + j >= i32 + 25)) ? 0.f : -1e30f;
    const float gs = *(LAS float*)(L + L_G + (rq * 32 + i32) * 4);
    constexpr float C1 = 0.25f * LOG2E;
    const unsigned ONE2 = 0x3F803F80u;
    const f32x16 zero = {};
#pragma unroll
    for (int hh = 0; hh < 2; ++hh) {
      f32x16 sc = MFMA(Kf[hh], Qf[hh], bias);
      const f32x16 sv = MFMA(Kp[hh], Qf[hh], zero);
      float sp4[4];
#pragma unroll
      for (int j = 0; j < 4; ++j) sp4[j] = sv[12 + j] + bprev[j];
      float m = __builtin_fmaxf(__builtin_fmaxf(sp4[0], sp4[1]), __builtin_fmaxf(sp4[2], sp4[3]));
#pragma unroll
      for (int r = 0; r < 16; r += 2) m = __builtin_fmaxf(__builtin_fmaxf(m, sc[r]), sc[r + 1]);
      m = swap_max(m);
      const float nm = -m * C1;
#pragma unroll
      for (int r = 0; r < 16; ++r) sc[r] = __builtin_amdgcn_exp2f(__builtin_fmaf(sc[r], C1, nm));
#pragma unroll
      for (int j = 0; j < 4; ++j) sp4[j] = __builtin_amdgcn_exp2f(__builtin_fmaf(sp4[j], C1, nm));
      const bf16x8 P0 = pack8(sc, 0), P1 = pack8(sc, 8);
      const u32x4 ppv = {0u, 0u, pk(sp4[0], sp4[1]), pk(sp4[2], sp4[3])};
      const bool mine = (i32 < 16) == (hh == 0);
      const u32x4 v0 = __builtin_bit_cast(u32x4, Vf[0]), v1 = __builtin_bit_cast(u32x4, Vf[1]);
      u32x4 a0, a1, ap;
#pragma unroll
      for (int j = 0; j < 4; ++j) { a0[j] = mine ? v0[j] : ONE2; a1[j] = mine ? v1[j] : ONE2; }
      ap = (u32x4){0u, 0u, mine ? vp[0] : ONE2, mine ? vp[1] : ONE2};
      f32x16 o = MFMA(__builtin_bit_cast(bf16x8, a0), P0, zero);
      o = MFMA(__builtin_bit_cast(bf16x8, a1), P1, o);
      o = MFMA(__builtin_bit_cast(bf16x8, ap), __builtin_bit_cast(bf16x8, ppv), o);
      const int nb = hh == 0 ? 0 : 8, db = hh == 0 ? 8 : 0;
      const float s = gs * __builtin_amdgcn_rcpf(o[db]);
      const u32x4 ov = {pk(o[nb] * s, o[nb + 1] * s), pk(o[nb + 2] * s, o[nb + 3] * s), pk(o[nb + 4] * s, o[nb + 5] * s), pk(o[nb + 6] * s, o[nb + 7] * s)};
      __builtin_amdgcn_raw_buffer_store_b128(ov, rsWS, ofrag + hh * 1024, 0, 16);
    }
  }
  STAMP(7);
  fill(12); fill(13);
  WAIT_VM(8);
  if (lane == 0) flag_store(ws, WS_FO + (((((rt * 2 + sp) * 2 + p) * 2 + BR) * 8 + quad) * 2 + c) * 64, tagO);
  STAMP(8);
}

__global__ __launch_bounds__(512, 2) void gated_mix_kernel(const __hip_bfloat16* __restrict__ x, const __hip_bfloat16* __restrict__ wA,
                                                           const __hip_bfloat16* __restrict__ woA, const __hip_bfloat16* __restrict__ wB,
                                                           const __hip_bfloat16* __restrict__ woB, const __hip_bfloat16* __restrict__ gw,
                                                           __hip_bfloat16* __restrict__ out, char* ws) {
  __shared__ __attribute__((aligned(1024))) char smem[LDS_BYTES];
  const int tid = threadIdx.x, lane = tid & 63, wid = __builtin_amdgcn_readfirstlane(tid >> 6);
  const int b = blockIdx.x, xcd = b & 7, loc = b >> 3;
  const int br = xcd & 1, rt = (xcd >> 1) * 4 + (loc >> 3), quad = loc & 7;
  STAMP(0);
  const rsrc_t rsX = __builtin_amdgcn_make_buffer_rsrc((void*)x, 0, 2048 * 512 * 2, 0x00020000);
  const rsrc_t rsW = __builtin_amdgcn_make_buffer_rsrc((void*)(br ? wB : wA), 0, 512 * 1536 * 2, 0x00020000);
  const rsrc_t rsWoA = __builtin_amdgcn_make_buffer_rsrc((void*)woA, 0, 512 * 512 * 2, 0x00020000);
  const rsrc_t rsWoB = __builtin_amdgcn_make_buffer_rsrc((void*)woB, 0, 512 * 512 * 2, 0x00020000);
  const rsrc_t rsG = __builtin_amdgcn_make_buffer_rsrc((void*)gw, 0, 1024, 0x00020000);
  const rsrc_t rsWS = __builtin_amdgcn_make_buffer_rsrc((void*)ws, 0, WS_USED, 0x00020000);
  const lptr L = (lptr)smem;
  const unsigned lds0 = (unsigned)(uintptr_t)L;

  const int l8 = lane >> 3, c8 = lane & 7;
  const int xrow = rt * 128 + 64 * (wid >> 2) + ((wid >> 1) & 1) + 2 * (16 * (wid & 1) + l8);
  const unsigned vX0 = xrow * 1024 + ((c8 ^ (lane >> 4)) << 4);
  const unsigned vX1 = (xrow + 16) * 1024 + ((c8 ^ (4 + (lane >> 4))) << 4);
  const int wsw = (c8 ^ (((lane >> 4) & 1) << 2)) << 4;
  const unsigned vW = (8 * wid + l8) * 3072 + quad * 128 + wsw;
  const unsigned vWo = (8 * wid + l8) * 1024 + quad * 128 + wsw;

  asm volatile("s_nop 4" ::: "memory");
  if (wid == 0) dma16(lds0 + L_GW, lane * 16, rsG, 0);
#pragma unroll
  for (int f = 0; f < 3; ++f) {
    const unsigned sl = lds0 + f * SLOT;
    dma16(sl + wid * 2048, vX0, rsX, f * 128);
    dma16(sl + wid * 2048 + 1024, vX1, rsX, f * 128);
    dma16(sl + 16384 + wid * 1024, vW, rsW, f * 196608 + 1024);
    dma16(sl + 24576 + wid * 1024, vW, rsW, f * 196608 + 2048);
  }
  const u64 tagO = sload64(ws + WS_EO + rt * 128) + 1;
  u64 tagG, tagP;
  if (br == 0) {
    tagG = tagP = sload64(ws + WS_EKV + quad * 128) + 1;
  } else {
    tagP = sload64(ws + WS_EH + (rt * 8 + quad) * 128) + 1;
    tagG = sload64(ws + WS_EH + ((rt > 0 ? rt - 1 : 0) * 8 + quad) * 128) + 1;
  }
  STAMP(1);
  if (br == 0) phase1<0>(smem, rsX, rsW, rsWoA, rsWoB, rsWS, ws, rt, quad, wid, lane, vX0, vX1, vW, vWo, tagG, tagP, tagO);
  else phase1<1>(smem, rsX, rsW, rsWoA, rsWoB, rsWS, ws, rt, quad, wid, lane, vX0, vX1, vW, vWo, tagG, tagP, tagO);

  const int pt = wid & 1, kq = (wid >> 2) * 2 + ((wid >> 1) & 1);
  const int i32 = lane & 31, h = lane >> 5;
  WAIT_BAR(0);
  bf16x8 wo[32];
  {
    const int q4 = (lane & 15) >> 2, p4 = lane & 3, nsub = (lane >> 4) & 1;
    const int slot = kq == 0 ? 2 : kq == 1 ? 3 : kq == 2 ? 0 : 1;
    const lptr base = L + slot * SLOT + (4 * h + q4) * 128 + 8 * (p4 & 1);
    const int sw = ((q4 >> 1) & 1) << 2;
    const lptr b0 = base + ((2 * nsub + (p4 >> 1)) ^ sw) * 16, b1 = base + ((4 + 2 * nsub + (p4 >> 1)) ^ sw) * 16;
#pragma unroll
    for (int i = 0; i < 16; ++i) {
      wo[2 * i] = tr_frag(b0, i * 2048, i * 2048 + 1024);
      wo[2 * i + 1] = tr_frag(b1, i * 2048, i * 2048 + 1024);
    }
  }
  LBAR();
  STAMP(9);
  poll_flags(ws, WS_FO + (((((rt * 2 + br) * 2 + pt) * 2 + (kq >> 1)) * 8 + (kq & 1) * 4) * 2) * 64, 64, 8, tagO, lane);
  STAMP(10);
  f32x16 a0 = {}, a1 = {};
  {
    const unsigned ob = WS_O + ((((rt * 2 + br) * 2 + pt) * 64) + kq * 16) * 1024 + lane * 16;
    bf16x8 of[16];
#pragma unroll
    for (int i = 0; i < 16; ++i) of[i] = __builtin_bit_cast(bf16x8, __builtin_amdgcn_raw_buffer_load_b128(rsWS, ob, i * 1024, 16));
#pragma unroll
    for (int i = 0; i < 16; ++i) { a0 = MFMA(wo[2 * i], of[i], a0); a1 = MFMA(wo[2 * i + 1], of[i], a1); }
  }
  STAMP(11);
  {
    const lptr pr = L + (kq * 64 + pt * 32 + i32) * PROW + h * 16;
#pragma unroll
    for (int g4 = 0; g4 < 4; ++g4) {
      *(LAS f32x4*)(pr + g4 * 32) = (f32x4){a0[4 * g4], a0[4 * g4 + 1], a0[4 * g4 + 2], a0[4 * g4 + 3]};
      *(LAS f32x4*)(pr + 128 + g4 * 32) = (f32x4){a1[4 * g4], a1[4 * g4 + 1], a1[4 * g4 + 2], a1[4 * g4 + 3]};
    }
  }
  LBAR();
  {
    const int pos = tid >> 3, cg = tid & 7;
    const lptr pr = L + pos * PROW + cg * 32;
    f32x4 s0 = *(LAS f32x4*)pr, s1 = *(LAS f32x4*)(pr + 16);
#pragma unroll
    for (int k = 1; k < 4; ++k) { s0 += *(LAS f32x4*)(pr + k * 64 * PROW); s1 += *(LAS f32x4*)(pr + k * 64 * PROW + 16); }
    const int row = rt * 128 + 64 * br + 2 * (pos & 31) + (pos >> 5);
    const u32x4 ov = {pk(s0[0], s0[1]), pk(s0[2], s0[3]), pk(s1[0], s1[1]), pk(s1[2], s1[3])};
    *(GAS u32x4*)((GAS char*)out + row * 1024 + quad * 128 + cg * 16) = ov;
  }
  if (quad == 0 && br == 0 && tid == 0) flag_store(ws, WS_EO + rt * 128, tagO);
  STAMP(12);
}

extern "C" void kernel_launch(void* const* d_in, const int* in_sizes, int n_in, void* d_out, int out_size, void* d_ws, size_t ws_size,
                              hipStream_t stream) {
  gated_mix_kernel<<<256, 512, 0, stream>>>((const __hip_bfloat16*)d_in[0], (const __hip_bfloat16*)d_in[1], (const __hip_bfloat16*)d_in[2],
                                            (const __hip_bfloat16*)d_in[4], (const __hip_bfloat16*)d_in[5], (const __hip_bfloat16*)d_in[6],
                                            (__hip_bfloat16*)d_out, (char*)d_ws);
}
```

```cpp
#define DIAG 1
#define DIAGB 0
#define DIAGTHR 2100
#include <hip/hip_runtime.h>
#include <hip/hip_bf16.h>
#include <stdint.h>

typedef __bf16 bf16x2 __attribute__((ext_vector_type(2)));
typedef __bf16 bf16x8 __attribute__((ext_vector_type(8)));
typedef short s16x4 __attribute__((ext_vector_type(4)));
typedef float f32x2 __attribute__((ext_vector_type(2)));
typedef float f32x4 __attribute__((ext_vector_type(4)));
typedef float f32x16 __attribute__((ext_vector_type(16)));
typedef unsigned u32x2 __attribute__((ext_vector_type(2)));
typedef unsigned u32x4 __attribute__((ext_vector_type(4)));
typedef unsigned long long u64;
#define LAS __attribute__((address_space(3)))
#define GAS __attribute__((address_space(1)))
#define DI __device__ __forceinline__
typedef LAS char* lptr;
typedef GAS u64 gu64;
typedef __amdgpu_buffer_rsrc_t rsrc_t;

constexpr unsigned WS_EO = 0;
constexpr unsigned WS_EKV = 2048;
constexpr unsigned WS_EH = 4096;
constexpr unsigned WS_FKV = 32768;
constexpr unsigned WS_FH = 65536;
constexpr unsigned WS_FO = 131072;
constexpr unsigned WS_KVP = 262144 + 65536;
constexpr unsigned KVP_BYTES = 4352;
constexpr unsigned WS_HALO = 1048576;
constexpr unsigned WS_O = 2097152;
constexpr unsigned WS_STAMP = 8u << 20;
constexpr unsigned WS_USED = 16u << 20;

constexpr int SLOT = 32768;
constexpr int L_GW = 131072;
constexpr int L_G = L_GW + 1024;
constexpr int L_PART = L_GW + 2048;
constexpr int L_TOT = L_PART + 8 * 2176;
constexpr int LDS_BYTES = 163840;
constexpr int PROW = 272;

constexpr float LOG2E = 1.4426950408889634f;
constexpr unsigned SPIN_CAP = 1u << 16;

#define MFMA(a, b, c) __builtin_amdgcn_mfma_f32_32x32x16_bf16((a), (b), (c), 0, 0, 0)
#define WAIT_BAR(N) asm volatile("s_waitcnt vmcnt(" #N ") lgkmcnt(0)\n\ts_barrier" ::: "memory")
#define WAIT_VM(N) asm volatile("s_waitcnt vmcnt(" #N ")" ::: "memory")
#define LBAR() asm volatile("s_waitcnt lgkmcnt(0)\n\ts_barrier" ::: "memory")

#define STAMP(i) do { if ((i) == 0) { dg0 = __builtin_amdgcn_s_memrealtime(); dc0 = __builtin_amdgcn_s_memtime(); } if ((i) == DIAG) { dg1 = __builtin_amdgcn_s_memrealtime(); dc1 = __builtin_amdgcn_s_memtime(); } } while (0)

DI unsigned pk(float a, float b) { return __builtin_bit_cast(unsigned, __builtin_convertvector((f32x2){a, b}, bf16x2)); }
DI bf16x8 pack8(const f32x16& a, int o) {
  u32x4 r = {pk(a[o], a[o + 1]), pk(a[o + 2], a[o + 3]), pk(a[o + 4], a[o + 5]), pk(a[o + 6], a[o + 7])};
  return __builtin_bit_cast(bf16x8, r);
}
DI float phi(float t) {
  const float e = __builtin_amdgcn_fmed3f(__builtin_amdgcn_exp2f(t * LOG2E), 0.f, 1.f);
  return __builtin_fmaxf(t + 1.f, e) + 1e-6f;
}
DI void dma16(unsigned lds, unsigned voff, rsrc_t rs, unsigned soff) {
  asm volatile("s_mov_b32 m0, %0\n\ts_nop 0\n\tbuffer_load_dwordx4 %1, %2, %3 offen lds" ::"s"(lds), "v"(voff), "s"(rs), "s"(soff) : "memory");
}
DI bf16x8 lds_frag(lptr p) { return *(const LAS bf16x8*)p; }
DI bf16x8 tr_frag(lptr p, int o0, int o1) {
  const s16x4 lo = __builtin_bit_cast(s16x4, __builtin_amdgcn_ds_read_tr16_b64_v4i16((LAS s16x4*)(p + o0)));
  const s16x4 hi = __builtin_bit_cast(s16x4, __builtin_amdgcn_ds_read_tr16_b64_v4i16((LAS s16x4*)(p + o1)));
  return __builtin_bit_cast(bf16x8, __builtin_shufflevector(lo, hi, 0, 1, 2, 3, 4, 5, 6, 7));
}
DI u64 sload64(const char* p) {
  u64 r;
  asm volatile("s_load_dwordx2 %0, %1, 0x0\n\ts_waitcnt lgkmcnt(0)" : "=s"(r) : "s"(p) : "memory");
  return r;
}
DI void flag_store(char* ws, unsigned off, u64 v) { __hip_atomic_store((gu64*)(ws + off), v, __ATOMIC_RELAXED, __HIP_MEMORY_SCOPE_AGENT); }
DI void poll_flags(char* ws, unsigned off0, unsigned stride, int n, u64 tag, int lane) {
  gu64* p = (gu64*)(ws + off0 + (unsigned)lane * stride);
  for (unsigned spins = 0; spins < SPIN_CAP; ++spins) {
    u64 v = tag;
    if (lane < n) v = __hip_atomic_load(p, __ATOMIC_RELAXED, __HIP_MEMORY_SCOPE_AGENT);
    if (__all(v == tag)) break;
    __builtin_amdgcn_s_sleep(1);
  }
  __builtin_amdgcn_fence(__ATOMIC_ACQUIRE, "wavefront");
  asm volatile("" ::: "memory");
}
DI float swap_max(float m) {
  auto r = __builtin_amdgcn_permlane32_swap(__float_as_uint(m), __float_as_uint(m), false, false);
  return __builtin_fmaxf(__uint_as_float(r[0]), __uint_as_float(r[1]));
}

template <int BR>
DI void phase1(char* smem_generic, const rsrc_t rsX, const rsrc_t rsW, const rsrc_t rsWoA, const rsrc_t rsWoB, const rsrc_t rsWS, char* ws,
               const int rt, const int quad, const int wid, const int lane, const unsigned vX0, const unsigned vX1, const unsigned vW,
               const unsigned vWo, const u64 tagG, const u64 tagP, const u64 tagO, u64& dg0, u64& dc0, u64& dg1, u64& dc1) {
  const lptr L = (lptr)smem_generic;
  const unsigned lds0 = (unsigned)(uintptr_t)L;
  const int c = wid >> 2, rq = wid & 3, p = rq & 1, sp = rq >> 1;
  const int i32 = lane & 31, h = lane >> 5;
  const unsigned pieceX = lds0 + wid * 2048, pieceW = lds0 + wid * 1024;

  auto fill = [&](int f) {
    const unsigned sl = (unsigned)(f & 3) * SLOT;
    if (f < 8) {
      dma16(pieceX + sl, vX0, rsX, f * 128);
      dma16(pieceX + sl + 1024, vX1, rsX, f * 128);
      dma16(pieceW + sl + 16384, vW, rsW, f * 196608 + 1024);
      dma16(pieceW + sl + 24576, vW, rsW, f * 196608 + 2048);
    } else if (f < 10) {
#pragma unroll
      for (int i = 0; i < 4; ++i) dma16(pieceW + sl + i * 8192, vW, rsW, ((f - 8) * 256 + i * 64) * 3072);
    } else {
      const int kq = f - 10;
      const unsigned so = (unsigned)(kq == 0 ? 2 : kq == 1 ? 3 : kq == 2 ? 0 : 1) * SLOT;
#pragma unroll
      for (int i = 0; i < 4; ++i) dma16(pieceW + so + i * 8192, vWo, kq < 2 ? rsWoA : rsWoB, ((kq & 1) * 256 + i * 64) * 1024);
    }
  };

  const lptr xb = L + (32 * rq + i32) * 128;
  const int xs = h ^ ((i32 >> 1) & 7);
  const int q4 = (lane & 15) >> 2, p4 = lane & 3, nsub = (lane >> 4) & 1;
  const int wchunk = (4 * c + 2 * nsub + (p4 >> 1)) ^ (((q4 >> 1) & 1) << 2);
  const lptr wb = L + (8 * h + q4) * 128 + wchunk * 16 + 8 * (p4 & 1);

  bf16x8 xf[32];
  f32x16 accK = {}, accV = {};
#pragma unroll
  for (int ch = 0; ch < 8; ++ch) {
    WAIT_BAR(8);
    fill(ch + 3);
    const int sl = (ch & 3) * SLOT;
#pragma unroll
    for (int t = 0; t < 4; ++t) {
      xf[ch * 4 + t] = lds_frag(xb + sl + ((xs ^ (2 * t)) << 4));
      const bf16x8 wk = tr_frag(wb + sl + 16384, t * 2048, t * 2048 + 512);
      const bf16x8 wv = tr_frag(wb + sl + 24576, t * 2048, t * 2048 + 512);
      accK = BR == 0 ? MFMA(xf[ch * 4 + t], wk, accK) : MFMA(wk, xf[ch * 4 + t], accK);
      accV = MFMA(xf[ch * 4 + t], wv, accV);
    }
  }
  STAMP(2);
  bf16x8 Kf[2], Vf[2];
  if (BR == 0) {
#pragma unroll
    for (int r = 0; r < 16; ++r) accK[r] = phi(accK[r]);
  }
  Kf[0] = pack8(accK, 0); Kf[1] = pack8(accK, 8);
  Vf[0] = pack8(accV, 0); Vf[1] = pack8(accV, 8);
  if (BR == 0) {
    const u32x4 one4 = {0x3F803F80u, 0x3F803F80u, 0x3F803F80u, 0x3F803F80u};
    const bf16x8 ones = __builtin_bit_cast(bf16x8, one4);
    f32x16 kv = {}, z = {};
    kv = MFMA(Kf[0], Vf[0], kv); z = MFMA(Kf[0], ones, z);
    kv = MFMA(Kf[1], Vf[1], kv); z = MFMA(Kf[1], ones, z);
    const bool lo = i32 < 16;
    f32x4 u0, u1;
#pragma unroll
    for (int j = 0; j < 4; ++j) { u0[j] = lo ? kv[j] : kv[8 + j]; u1[j] = lo ? kv[4 + j] : kv[12 + j]; }
    const lptr pp = L + L_PART + wid * 2176;
    *(LAS f32x4*)(pp + lane * 16) = u0;
    *(LAS f32x4*)(pp + 1024 + lane * 16) = u1;
    if (i32 == 0) {
#pragma unroll
      for (int g4 = 0; g4 < 4; ++g4) *(LAS f32x4*)(pp + 2048 + h * 64 + g4 * 16) = (f32x4){z[4 * g4], z[4 * g4 + 1], z[4 * g4 + 2], z[4 * g4 + 3]};
    }
  } else {
    if (sp == 0) {
      const lptr ex = L + L_PART + (c * 2 + p) * 1024;
      if (i32 >= 24) {
        *(LAS bf16x8*)(ex + h * 128 + (i32 - 24) * 16) = Kf[0];
        *(LAS bf16x8*)(ex + 256 + h * 128 + (i32 - 24) * 16) = Kf[1];
      }
      const u32x4 v1 = __builtin_bit_cast(u32x4, Vf[1]);
      *(LAS u32x2*)(ex + 512 + lane * 8) = (u32x2){v1[2], v1[3]};
    }
  }
  WAIT_BAR(4);
  STAMP(3);
  if (BR == 0) {
    if (wid == 7) {
#pragma unroll
      for (int it = 0; it < 5; ++it) {
        const int u = it * 64 + lane;
        if (u < 272) {
          const int cc = u < 256 ? (u >> 7) : ((u - 256) >> 3);
          const int o = u < 256 ? (u & 127) * 16 : 2048 + (u & 7) * 16;
          const lptr s = L + L_PART + cc * 4 * 2176 + o;
          const f32x4 a = *(LAS f32x4*)s + *(LAS f32x4*)(s + 2176) + (*(LAS f32x4*)(s + 2 * 2176) + *(LAS f32x4*)(s + 3 * 2176));
          __builtin_amdgcn_raw_buffer_store_b128(__builtin_bit_cast(u32x4, a), rsWS, WS_KVP + (quad * 16 + rt) * KVP_BYTES + u * 16, 0, 16);
        }
      }
    }
  } else {
    if (sp == 1 && rt < 15) {
      const unsigned ex = WS_HALO + ((rt * 8 + quad) * 4 + c * 2 + p) * 1024;
      if (i32 >= 24) {
        __builtin_amdgcn_raw_buffer_store_b128(__builtin_bit_cast(u32x4, Kf[0]), rsWS, ex + h * 128 + (i32 - 24) * 16, 0, 16);
        __builtin_amdgcn_raw_buffer_store_b128(__builtin_bit_cast(u32x4, Kf[1]), rsWS, ex + 256 + h * 128 + (i32 - 24) * 16, 0, 16);
      }
      const u32x4 v1 = __builtin_bit_cast(u32x4, Vf[1]);
      __builtin_amdgcn_raw_buffer_store_b64((u32x2){v1[2], v1[3]}, rsWS, ex + 512 + lane * 8, 0, 16);
    }
  }
  fill(11);
  f32x16 accQ = {};
#pragma unroll
  for (int t = 0; t < 16; ++t) accQ = MFMA(tr_frag(wb, t * 2048, t * 2048 + 512), xf[t], accQ);
  {
    const bool pub = BR == 0 ? (wid == 7) : (sp == 1 && rt < 15);
    if (pub) {
      WAIT_VM(4);
      if (lane == 0) flag_store(ws, BR == 0 ? WS_FKV + (quad * 16 + rt) * 128 : WS_FH + ((rt * 8 + quad) * 4 + c * 2 + p) * 64, tagP);
    }
  }
#pragma unroll
  for (int t = 0; t < 16; ++t) accQ = MFMA(tr_frag(wb + SLOT, t * 2048, t * 2048 + 512), xf[16 + t], accQ);
  if (BR == 0) {
#pragma unroll
    for (int r = 0; r < 16; ++r) accQ[r] = phi(accQ[r]);
  }
  bf16x8 Qf[2] = {pack8(accQ, 0), pack8(accQ, 8)};
  STAMP(4);
  if (c == 0) {
    f32x16 ga = {};
#pragma unroll
    for (int t = 0; t < 32; ++t) ga = MFMA(lds_frag(L + L_GW + t * 32 + h * 16), xf[t], ga);
    const float g = __builtin_amdgcn_rcpf(1.f + __builtin_amdgcn_exp2f(-LOG2E * ga[0]));
    if (h == 0) *(LAS float*)(L + L_G + (rq * 32 + i32) * 4) = BR == 0 ? g : 1.f - g;
  }
  const unsigned ofrag = WS_O + ((((rt * 2 + sp) * 2 + p) * 64) + BR * 32 + quad * 4 + 2 * c) * 1024 + lane * 16;
  if (BR == 0) {
    const int u = wid >= 4 ? (wid - 4) * 64 + lane : (wid == 0 && lane < 16 ? 256 + lane : -1);
    if (wid >= 4 || wid == 0) {
      poll_flags(ws, WS_FKV + quad * 16 * 128, 128, 16, tagG, lane);
      STAMP(5);
      if (u >= 0) {
        f32x4 a[16];
#pragma unroll
        for (int r = 0; r < 16; ++r)
          a[r] = __builtin_bit_cast(f32x4, __builtin_amdgcn_raw_buffer_load_b128(rsWS, WS_KVP + quad * 16 * KVP_BYTES + u * 16, r * KVP_BYTES, 16));
#pragma unroll
        for (int s = 1; s < 16; s *= 2)
#pragma unroll
          for (int r = 0; r < 16; r += 2 * s) a[r] += a[r + s];
        *(LAS f32x4*)(L + L_TOT + u * 16) = a[0];
      }
      if (rt == 0 && wid == 0 && lane == 0) flag_store(ws, WS_EKV + quad * 128, tagG);
    }
    LBAR();
    STAMP(6);
    const float gs = *(LAS float*)(L + L_G + (rq * 32 + i32) * 4);
    const f32x4 u0 = *(LAS f32x4*)(L + L_TOT + (c * 128 + lane) * 16), u1 = *(LAS f32x4*)(L + L_TOT + (c * 128 + 64 + lane) * 16);
    const lptr zp = L + L_TOT + 4096 + c * 128 + h * 64;
    const f32x4 z0 = *(LAS f32x4*)zp, z1 = *(LAS f32x4*)(zp + 16), z2 = *(LAS f32x4*)(zp + 32), z3 = *(LAS f32x4*)(zp + 48);
    const bool lo = i32 < 16;
    const u32x4 pu = {pk(u0[0], u0[1]), pk(u0[2], u0[3]), pk(u1[0], u1[1]), pk(u1[2], u1[3])};
    const u32x4 pz0 = {pk(z0[0], z0[1]), pk(z0[2], z0[3]), pk(z1[0], z1[1]), pk(z1[2], z1[3])};
    const u32x4 pz1 = {pk(z2[0], z2[1]), pk(z2[2], z2[3]), pk(z3[0], z3[1]), pk(z3[2], z3[3])};
    u32x4 a0, a1;
#pragma unroll
    for (int j = 0; j < 4; ++j) { a0[j] = lo ? pu[j] : pz0[j]; a1[j] = lo ? pz1[j] : pu[j]; }
    const f32x16 zero = {};
    const f32x16 d0 = MFMA(__builtin_bit_cast(bf16x8, a0), Qf[0], zero);
    const f32x16 d1 = MFMA(__builtin_bit_cast(bf16x8, a1), Qf[1], zero);
    const float s0 = gs * __builtin_amdgcn_rcpf(d0[8] + 1e-6f), s1 = gs * __builtin_amdgcn_rcpf(d1[0] + 1e-6f);
    const u32x4 o0 = {pk(d0[0] * s0, d0[1] * s0), pk(d0[2] * s0, d0[3] * s0), pk(d0[4] * s0, d0[5] * s0), pk(d0[6] * s0, d0[7] * s0)};
    const u32x4 o1 = {pk(d1[8] * s1, d1[9] * s1), pk(d1[10] * s1, d1[11] * s1), pk(d1[12] * s1, d1[13] * s1), pk(d1[14] * s1, d1[15] * s1)};
    __builtin_amdgcn_raw_buffer_store_b128(o0, rsWS, ofrag, 0, 16);
    __builtin_amdgcn_raw_buffer_store_b128(o1, rsWS, ofrag + 1024, 0, 16);
  } else {
    LBAR();
    STAMP(5);
    bf16x8 Kp[2];
    u32x2 vp;
    if (sp == 1) {
      const lptr ex = L + L_PART + (c * 2 + p) * 1024;
      Kp[0] = lds_frag(ex + h * 128 + (i32 & 7) * 16);
      Kp[1] = lds_frag(ex + 256 + h * 128 + (i32 & 7) * 16);
      vp = *(LAS u32x2*)(ex + 512 + lane * 8);
    } else if (rt > 0) {
      poll_flags(ws, WS_FH + (((rt - 1) * 8 + quad) * 4 + c * 2 + p) * 64, 64, 1, tagG, lane);
      const unsigned ex = WS_HALO + (((rt - 1) * 8 + quad) * 4 + c * 2 + p) * 1024;
      Kp[0] = __builtin_bit_cast(bf16x8, __builtin_amdgcn_raw_buffer_load_b128(rsWS, ex + h * 128 + (i32 & 7) * 16, 0, 16));
      Kp[1] = __builtin_bit_cast(bf16x8, __builtin_amdgcn_raw_buffer_load_b128(rsWS, ex + 256 + h * 128 + (i32 & 7) * 16, 0, 16));
      vp = __builtin_amdgcn_raw_buffer_load_b64(rsWS, ex + 512 + lane * 8, 0, 16);
      if (wid == 0 && lane == 0) flag_store(ws, WS_EH + ((rt - 1) * 8 + quad) * 128, tagG);
    } else {
      Kp[0] = Kf[0]; Kp[1] = Kf[1]; vp = (u32x2){0u, 0u};
    }
    STAMP(6);
    const bool noprev = (rt == 0 && sp == 0);
    f32x16 bias;
#pragma unroll
    for (int r = 0; r < 16; ++r) bias[r] = ((unsigned)(i32 - ((r & 3) + 8 * (r >> 2) + 4 * h)) <= 7u) ? 0.f : -1e30f;
    float bprev[4];
#pragma unroll
    for (int j = 0; j < 4; ++j) bprev[j] = (!noprev && (24 + 4 * h + j >= i32 + 25)) ? 0.f : -1e30f;
    const float gs = *(LAS float*)(L + L_G + (rq * 32 + i32) * 4);
    constexpr float C1 = 0.25f * LOG2E;
    const unsigned ONE2 = 0x3F803F80u;
    const f32x16 zero = {};
#pragma unroll
    for (int hh = 0; hh < 2; ++hh) {
      f32x16 sc = MFMA(Kf[hh], Qf[hh], bias);
      const f32x16 sv = MFMA(Kp[hh], Qf[hh], zero);
      float sp4[4];
#pragma unroll
      for (int j = 0; j < 4; ++j) sp4[j] = sv[12 + j] + bprev[j];
      float m = __builtin_fmaxf(__builtin_fmaxf(sp4[0], sp4[1]), __builtin_fmaxf(sp4[2], sp4[3]));
#pragma unroll
      for (int r = 0; r < 16; r += 2) m = __builtin_fmaxf(__builtin_fmaxf(m, sc[r]), sc[r + 1]);
      m = swap_max(m);
      const float nm = -m * C1;
#pragma unroll
      for (int r = 0; r < 16; ++r) sc[r] = __builtin_amdgcn_exp2f(__builtin_fmaf(sc[r], C1, nm));
#pragma unroll
      for (int j = 0; j < 4; ++j) sp4[j] = __builtin_amdgcn_exp2f(__builtin_fmaf(sp4[j], C1, nm));
      const bf16x8 P0 = pack8(sc, 0), P1 = pack8(sc, 8);
      const u32x4 ppv = {0u, 0u, pk(sp4[0], sp4[1]), pk(sp4[2], sp4[3])};
      const bool mine = (i32 < 16) == (hh == 0);
      const u32x4 v0 = __builtin_bit_cast(u32x4, Vf[0]), v1 = __builtin_bit_cast(u32x4, Vf[1]);
      u32x4 a0, a1, ap;
#pragma unroll
      for (int j = 0; j < 4; ++j) { a0[j] = mine ? v0[j] : ONE2; a1[j] = mine ? v1[j] : ONE2; }
      ap = (u32x4){0u, 0u, mine ? vp[0] : ONE2, mine ? vp[1] : ONE2};
      f32x16 o = MFMA(__builtin_bit_cast(bf16x8, a0), P0, zero);
      o = MFMA(__builtin_bit_cast(bf16x8, a1), P1, o);
      o = MFMA(__builtin_bit_cast(bf16x8, ap), __builtin_bit_cast(bf16x8, ppv), o);
      const int nb = hh == 0 ? 0 : 8, db = hh == 0 ? 8 : 0;
      const float s = gs * __builtin_amdgcn_rcpf(o[db]);
      const u32x4 ov = {pk(o[nb] * s, o[nb + 1] * s), pk(o[nb + 2] * s, o[nb + 3] * s), pk(o[nb + 4] * s, o[nb + 5] * s), pk(o[nb + 6] * s, o[nb + 7] * s)};
      __builtin_amdgcn_raw_buffer_store_b128(ov, rsWS, ofrag + hh * 1024, 0, 16);
    }
  }
  STAMP(7);
  fill(12); fill(13);
  WAIT_VM(8);
  if (lane == 0) flag_store(ws, WS_FO + (((((rt * 2 + sp) * 2 + p) * 2 + BR) * 8 + quad) * 2 + c) * 64, tagO);
  STAMP(8);
}

__global__ __launch_bounds__(512, 2) void gated_mix_kernel(const __hip_bfloat16* __restrict__ x, const __hip_bfloat16* __restrict__ wA,
                                                           const __hip_bfloat16* __restrict__ woA, const __hip_bfloat16* __restrict__ wB,
                                                           const __hip_bfloat16* __restrict__ woB, const __hip_bfloat16* __restrict__ gw,
                                                           __hip_bfloat16* __restrict__ out, char* ws) {
  __shared__ __attribute__((aligned(1024))) char smem[LDS_BYTES];
  const int tid = threadIdx.x, lane = tid & 63, wid = __builtin_amdgcn_readfirstlane(tid >> 6);
  const int b = blockIdx.x, xcd = b & 7, loc = b >> 3;
  const int br = xcd & 1, rt = (xcd >> 1) * 4 + (loc >> 3), quad = loc & 7;
  u64 dg0 = 0, dc0 = 0, dg1 = 0, dc1 = 0;
  STAMP(0);
  const rsrc_t rsX = __builtin_amdgcn_make_buffer_rsrc((void*)x, 0, 2048 * 512 * 2, 0x00020000);
  const rsrc_t rsW = __builtin_amdgcn_make_buffer_rsrc((void*)(br ? wB : wA), 0, 512 * 1536 * 2, 0x00020000);
  const rsrc_t rsWoA = __builtin_amdgcn_make_buffer_rsrc((void*)woA, 0, 512 * 512 * 2, 0x00020000);
  const rsrc_t rsWoB = __builtin_amdgcn_make_buffer_rsrc((void*)woB, 0, 512 * 512 * 2, 0x00020000);
  const rsrc_t rsG = __builtin_amdgcn_make_buffer_rsrc((void*)gw, 0, 1024, 0x00020000);
  const rsrc_t rsWS = __builtin_amdgcn_make_buffer_rsrc((void*)ws, 0, WS_USED, 0x00020000);
  const lptr L = (lptr)smem;
  const unsigned lds0 = (unsigned)(uintptr_t)L;

  const int l8 = lane >> 3, c8 = lane & 7;
  const int xrow = rt * 128 + 64 * (wid >> 2) + ((wid >> 1) & 1) + 2 * (16 * (wid & 1) + l8);
  const unsigned vX0 = xrow * 1024 + ((c8 ^ (lane >> 4)) << 4);
  const unsigned vX1 = (xrow + 16) * 1024 + ((c8 ^ (4 + (lane >> 4))) << 4);
  const int wsw = (c8 ^ (((lane >> 4) & 1) << 2)) << 4;
  const unsigned vW = (8 * wid + l8) * 3072 + quad * 128 + wsw;
  const unsigned vWo = (8 * wid + l8) * 1024 + quad * 128 + wsw;

  asm volatile("s_nop 4" ::: "memory");
  if (wid == 0) dma16(lds0 + L_GW, lane * 16, rsG, 0);
#pragma unroll
  for (int f = 0; f < 3; ++f) {
    const unsigned sl = lds0 + f * SLOT;
    dma16(sl + wid * 2048, vX0, rsX, f * 128);
    dma16(sl + wid * 2048 + 1024, vX1, rsX, f * 128);
    dma16(sl + 16384 + wid * 1024, vW, rsW, f * 196608 + 1024);
    dma16(sl + 24576 + wid * 1024, vW, rsW, f * 196608 + 2048);
  }
  const u64 tagO = sload64(ws + WS_EO + rt * 128) + 1;
  u64 tagG, tagP;
  if (br == 0) {
    tagG = tagP = sload64(ws + WS_EKV + quad * 128) + 1;
  } else {
    tagP = sload64(ws + WS_EH + (rt * 8 + quad) * 128) + 1;
    tagG = sload64(ws + WS_EH + ((rt > 0 ? rt - 1 : 0) * 8 + quad) * 128) + 1;
  }
  STAMP(1);
  if (br == 0) phase1<0>(smem, rsX, rsW, rsWoA, rsWoB, rsWS, ws, rt, quad, wid, lane, vX0, vX1, vW, vWo, tagG, tagP, tagO, dg0, dc0, dg1, dc1);
  else phase1<1>(smem, rsX, rsW, rsWoA, rsWoB, rsWS, ws, rt, quad, wid, lane, vX0, vX1, vW, vWo, tagG, tagP, tagO, dg0, dc0, dg1, dc1);

  const int pt = wid & 1, kq = (wid >> 2) * 2 + ((wid >> 1) & 1);
  const int i32 = lane & 31, h = lane >> 5;
  WAIT_BAR(0);
  bf16x8 wo[32];
  {
    const int q4 = (lane & 15) >> 2, p4 = lane & 3, nsub = (lane >> 4) & 1;
    const int slot = kq == 0 ? 2 : kq == 1 ? 3 : kq == 2 ? 0 : 1;
    const lptr base = L + slot * SLOT + (4 * h + q4) * 128 + 8 * (p4 & 1);
    const int sw = ((q4 >> 1) & 1) << 2;
    const lptr b0 = base + ((2 * nsub + (p4 >> 1)) ^ sw) * 16, b1 = base + ((4 + 2 * nsub + (p4 >> 1)) ^ sw) * 16;
#pragma unroll
    for (int i = 0; i < 16; ++i) {
      wo[2 * i] = tr_frag(b0, i * 2048, i * 2048 + 1024);
      wo[2 * i + 1] = tr_frag(b1, i * 2048, i * 2048 + 1024);
    }
  }
  LBAR();
  STAMP(9);
  poll_flags(ws, WS_FO + (((((rt * 2 + br) * 2 + pt) * 2 + (kq >> 1)) * 8 + (kq & 1) * 4) * 2) * 64, 64, 8, tagO, lane);
  STAMP(10);
  f32x16 a0 = {}, a1 = {};
  {
    const unsigned ob = WS_O + ((((rt * 2 + br) * 2 + pt) * 64) + kq * 16) * 1024 + lane * 16;
    bf16x8 of[16];
#pragma unroll
    for (int i = 0; i < 16; ++i) of[i] = __builtin_bit_cast(bf16x8, __builtin_amdgcn_raw_buffer_load_b128(rsWS, ob, i * 1024, 16));
#pragma unroll
    for (int i = 0; i < 16; ++i) { a0 = MFMA(wo[2 * i], of[i], a0); a1 = MFMA(wo[2 * i + 1], of[i], a1); }
  }
  STAMP(11);
  {
    const lptr pr = L + (kq * 64 + pt * 32 + i32) * PROW + h * 16;
#pragma unroll
    for (int g4 = 0; g4 < 4; ++g4) {
      *(LAS f32x4*)(pr + g4 * 32) = (f32x4){a0[4 * g4], a0[4 * g4 + 1], a0[4 * g4 + 2], a0[4 * g4 + 3]};
      *(LAS f32x4*)(pr + 128 + g4 * 32) = (f32x4){a1[4 * g4], a1[4 * g4 + 1], a1[4 * g4 + 2], a1[4 * g4 + 3]};
    }
  }
  LBAR();
  {
    const int pos = tid >> 3, cg = tid & 7;
    const lptr pr = L + pos * PROW + cg * 32;
    f32x4 s0 = *(LAS f32x4*)pr, s1 = *(LAS f32x4*)(pr + 16);
#pragma unroll
    for (int k = 1; k < 4; ++k) { s0 += *(LAS f32x4*)(pr + k * 64 * PROW); s1 += *(LAS f32x4*)(pr + k * 64 * PROW + 16); }
    const int row = rt * 128 + 64 * br + 2 * (pos & 31) + (pos >> 5);
    const u32x4 ov = {pk(s0[0], s0[1]), pk(s0[2], s0[3]), pk(s1[0], s1[1]), pk(s1[2], s1[3])};
    *(GAS u32x4*)((GAS char*)out + row * 1024 + quad * 128 + cg * 16) = ov;
  }
  if (quad == 0 && br == 0 && tid == 0) flag_store(ws, WS_EO + rt * 128, tagO);
  STAMP(12);
  asm volatile("s_waitcnt vmcnt(0)" ::: "memory");
  __syncthreads();
  const u64 dend = __builtin_amdgcn_s_memrealtime();
  if (b == DIAGB && tid == 0 && (dend - dg0) > DIAGTHR) {
    const float v = (float)(dg1 - dg0) * 0.01f;
    out[(rt * 128 + 64 * br) * 512 + quad * 64] = __float2bfloat16(100.f + 0.f * v);
    out[(rt * 128 + 64 * br) * 512 + quad * 64] = __float2bfloat16(v);
  }
}

extern "C" void kernel_launch(void* const* d_in, const int* in_sizes, int n_in, void* d_out, int out_size, void* d_ws, size_t ws_size,
                              hipStream_t stream) {
  gated_mix_kernel<<<256, 512, 0, stream>>>((const __hip_bfloat16*)d_in[0], (const __hip_bfloat16*)d_in[1], (const __hip_bfloat16*)d_in[2],
                                            (const __hip_bfloat16*)d_in[4], (const __hip_bfloat16*)d_in[5], (const __hip_bfloat16*)d_in[6],
                                            (__hip_bfloat16*)d_out, (char*)d_ws);
}
```

```cpp
#define DIAGQ 0
#define DIAGTHR 23.f
#include <hip/hip_runtime.h>
#include <hip/hip_bf16.h>
#include <stdint.h>

typedef __bf16 bf16x2 __attribute__((ext_vector_type(2)));
typedef __bf16 bf16x8 __attribute__((ext_vector_type(8)));
typedef short s16x4 __attribute__((ext_vector_type(4)));
typedef float f32x2 __attribute__((ext_vector_type(2)));
typedef float f32x4 __attribute__((ext_vector_type(4)));
typedef float f32x16 __attribute__((ext_vector_type(16)));
typedef unsigned u32x2 __attribute__((ext_vector_type(2)));
typedef unsigned u32x4 __attribute__((ext_vector_type(4)));
typedef unsigned long long u64;
#define LAS __attribute__((address_space(3)))
#define GAS __attribute__((address_space(1)))
#define DI __device__ __forceinline__
typedef LAS char* lptr;
typedef GAS u64 gu64;
typedef __amdgpu_buffer_rsrc_t rsrc_t;

constexpr unsigned WS_EO = 0;
constexpr unsigned WS_EKV = 2048;
constexpr unsigned WS_EH = 4096;
constexpr unsigned WS_FKV = 32768;
constexpr unsigned WS_FH = 65536;
constexpr unsigned WS_FO = 131072;
constexpr unsigned WS_KVP = 262144 + 65536;
constexpr unsigned KVP_BYTES = 4352;
constexpr unsigned WS_HALO = 1048576;
constexpr unsigned WS_O = 2097152;
constexpr unsigned WS_STAMP = 8u << 20;
constexpr unsigned WS_USED = 16u << 20;

constexpr int SLOT = 32768;
constexpr int L_GW = 131072;
constexpr int L_G = L_GW + 1024;
constexpr int L_PART = L_GW + 2048;
constexpr int L_TOT = L_PART + 8 * 2176;
constexpr int LDS_BYTES = 163840;
constexpr int PROW = 272;

constexpr float LOG2E = 1.4426950408889634f;
constexpr unsigned SPIN_CAP = 1u << 16;

#define MFMA(a, b, c) __builtin_amdgcn_mfma_f32_32x32x16_bf16((a), (b), (c), 0, 0, 0)
#define WAIT_BAR(N) asm volatile("s_waitcnt vmcnt(" #N ") lgkmcnt(0)\n\ts_barrier" ::: "memory")
#define WAIT_VM(N) asm volatile("s_waitcnt vmcnt(" #N ")" ::: "memory")
#define LBAR() asm volatile("s_waitcnt lgkmcnt(0)\n\ts_barrier" ::: "memory")

#define STAMP(i) do {} while (0)

DI unsigned pk(float a, float b) { return __builtin_bit_cast(unsigned, __builtin_convertvector((f32x2){a, b}, bf16x2)); }
DI bf16x8 pack8(const f32x16& a, int o) {
  u32x4 r = {pk(a[o], a[o + 1]), pk(a[o + 2], a[o + 3]), pk(a[o + 4], a[o + 5]), pk(a[o + 6], a[o + 7])};
  return __builtin_bit_cast(bf16x8, r);
}
DI float phi(float t) {
  const float e = __builtin_amdgcn_fmed3f(__builtin_amdgcn_exp2f(t * LOG2E), 0.f, 1.f);
  return __builtin_fmaxf(t + 1.f, e) + 1e-6f;
}
DI void dma16(unsigned lds, unsigned voff, rsrc_t rs, unsigned soff) {
  asm volatile("s_mov_b32 m0, %0\n\ts_nop 0\n\tbuffer_load_dwordx4 %1, %2, %3 offen lds" ::"s"(lds), "v"(voff), "s"(rs), "s"(soff) : "memory");
}
DI bf16x8 lds_frag(lptr p) { return *(const LAS bf16x8*)p; }
DI bf16x8 tr_frag(lptr p, int o0, int o1) {
  const s16x4 lo = __builtin_bit_cast(s16x4, __builtin_amdgcn_ds_read_tr16_b64_v4i16((LAS s16x4*)(p + o0)));
  const s16x4 hi = __builtin_bit_cast(s16x4, __builtin_amdgcn_ds_read_tr16_b64_v4i16((LAS s16x4*)(p + o1)));
  return __builtin_bit_cast(bf16x8, __builtin_shufflevector(lo, hi, 0, 1, 2, 3, 4, 5, 6, 7));
}
DI u64 sload64(const char* p) {
  u64 r;
  asm volatile("s_load_dwordx2 %0, %1, 0x0\n\ts_waitcnt lgkmcnt(0)" : "=s"(r) : "s"(p) : "memory");
  return r;
}
DI void flag_store(char* ws, unsigned off, u64 v) { __hip_atomic_store((gu64*)(ws + off), v, __ATOMIC_RELAXED, __HIP_MEMORY_SCOPE_AGENT); }
DI void poll_flags(char* ws, unsigned off0, unsigned stride, int n, u64 tag, int lane) {
  gu64* p = (gu64*)(ws + off0 + (unsigned)lane * stride);
  for (unsigned spins = 0; spins < SPIN_CAP; ++spins) {
    u64 v = tag;
    if (lane < n) v = __hip_atomic_load(p, __ATOMIC_RELAXED, __HIP_MEMORY_SCOPE_AGENT);
    if (__all(v == tag)) break;
    __builtin_amdgcn_s_sleep(1);
  }
  __builtin_amdgcn_fence(__ATOMIC_ACQUIRE, "wavefront");
  asm volatile("" ::: "memory");
}
DI float swap_max(float m) {
  auto r = __builtin_amdgcn_permlane32_swap(__float_as_uint(m), __float_as_uint(m), false, false);
  return __builtin_fmaxf(__uint_as_float(r[0]), __uint_as_float(r[1]));
}

template <int BR>
DI void phase1(char* smem_generic, const rsrc_t rsX, const rsrc_t rsW, const rsrc_t rsWoA, const rsrc_t rsWoB, const rsrc_t rsWS, char* ws,
               const int rt, const int quad, const int wid, const int lane, const unsigned vX0, const unsigned vX1, const unsigned vW,
               const unsigned vWo, const u64 tagG, const u64 tagP, const u64 tagO) {
  const lptr L = (lptr)smem_generic;
  const unsigned lds0 = (unsigned)(uintptr_t)L;
  const int c = wid >> 2, rq = wid & 3, p = rq & 1, sp = rq >> 1;
  const int i32 = lane & 31, h = lane >> 5;
  const unsigned pieceX = lds0 + wid * 2048, pieceW = lds0 + wid * 1024;

  auto fill = [&](int f) {
    const unsigned sl = (unsigned)(f & 3) * SLOT;
    if (f < 8) {
      dma16(pieceX + sl, vX0, rsX, f * 128);
      dma16(pieceX + sl + 1024, vX1, rsX, f * 128);
      dma16(pieceW + sl + 16384, vW, rsW, f * 196608 + 1024);
      dma16(pieceW + sl + 24576, vW, rsW, f * 196608 + 2048);
    } else if (f < 10) {
#pragma unroll
      for (int i = 0; i < 4; ++i) dma16(pieceW + sl + i * 8192, vW, rsW, ((f - 8) * 256 + i * 64) * 3072);
    } else {
      const int kq = f - 10;
      const unsigned so = (unsigned)(kq == 0 ? 2 : kq == 1 ? 3 : kq == 2 ? 0 : 1) * SLOT;
#pragma unroll
      for (int i = 0; i < 4; ++i) dma16(pieceW + so + i * 8192, vWo, kq < 2 ? rsWoA : rsWoB, ((kq & 1) * 256 + i * 64) * 1024);
    }
  };

  const lptr xb = L + (32 * rq + i32) * 128;
  const int xs = h ^ ((i32 >> 1) & 7);
  const int q4 = (lane & 15) >> 2, p4 = lane & 3, nsub = (lane >> 4) & 1;
  const int wchunk = (4 * c + 2 * nsub + (p4 >> 1)) ^ (((q4 >> 1) & 1) << 2);
  const lptr wb = L + (8 * h + q4) * 128 + wchunk * 16 + 8 * (p4 & 1);

  bf16x8 xf[32];
  f32x16 accK = {}, accV = {};
#pragma unroll
  for (int ch = 0; ch < 8; ++ch) {
    WAIT_BAR(8);
    fill(ch + 3);
    const int sl = (ch & 3) * SLOT;
#pragma unroll
    for (int t = 0; t < 4; ++t) {
      xf[ch * 4 + t] = lds_frag(xb + sl + ((xs ^ (2 * t)) << 4));
      const bf16x8 wk = tr_frag(wb + sl + 16384, t * 2048, t * 2048 + 512);
      const bf16x8 wv = tr_frag(wb + sl + 24576, t * 2048, t * 2048 + 512);
      accK = BR == 0 ? MFMA(xf[ch * 4 + t], wk, accK) : MFMA(wk, xf[ch * 4 + t], accK);
      accV = MFMA(xf[ch * 4 + t], wv, accV);
    }
  }
  STAMP(2);
  bf16x8 Kf[2], Vf[2];
  if (BR == 0) {
#pragma unroll
    for (int r = 0; r < 16; ++r) accK[r] = phi(accK[r]);
  }
  Kf[0] = pack8(accK, 0); Kf[1] = pack8(accK, 8);
  Vf[0] = pack8(accV, 0); Vf[1] = pack8(accV, 8);
  if (BR == 0) {
    const u32x4 one4 = {0x3F803F80u, 0x3F803F80u, 0x3F803F80u, 0x3F803F80u};
    const bf16x8 ones = __builtin_bit_cast(bf16x8, one4);
    f32x16 kv = {}, z = {};
    kv = MFMA(Kf[0], Vf[0], kv); z = MFMA(Kf[0], ones, z);
    kv = MFMA(Kf[1], Vf[1], kv); z = MFMA(Kf[1], ones, z);
    const bool lo = i32 < 16;
    f32x4 u0, u1;
#pragma unroll
    for (int j = 0; j < 4; ++j) { u0[j] = lo ? kv[j] : kv[8 + j]; u1[j] = lo ? kv[4 + j] : kv[12 + j]; }
    const lptr pp = L + L_PART + wid * 2176;
    *(LAS f32x4*)(pp + lane * 16) = u0;
    *(LAS f32x4*)(pp + 1024 + lane * 16) = u1;
    if (i32 == 0) {
#pragma unroll
      for (int g4 = 0; g4 < 4; ++g4) *(LAS f32x4*)(pp + 2048 + h * 64 + g4 * 16) = (f32x4){z[4 * g4], z[4 * g4 + 1], z[4 * g4 + 2], z[4 * g4 + 3]};
    }
  } else {
    if (sp == 0) {
      const lptr ex = L + L_PART + (c * 2 + p) * 1024;
      if (i32 >= 24) {
        *(LAS bf16x8*)(ex + h * 128 + (i32 - 24) * 16) = Kf[0];
        *(LAS bf16x8*)(ex + 256 + h * 128 + (i32 - 24) * 16) = Kf[1];
      }
      const u32x4 v1 = __builtin_bit_cast(u32x4, Vf[1]);
      *(LAS u32x2*)(ex + 512 + lane * 8) = (u32x2){v1[2], v1[3]};
    }
  }
  WAIT_BAR(4);
  STAMP(3);
  if (BR == 0) {
    if (wid == 7) {
#pragma unroll
      for (int it = 0; it < 5; ++it) {
        const int u = it * 64 + lane;
        if (u < 272) {
          const int cc = u < 256 ? (u >> 7) : ((u - 256) >> 3);
          const int o = u < 256 ? (u & 127) * 16 : 2048 + (u & 7) * 16;
          const lptr s = L + L_PART + cc * 4 * 2176 + o;
          const f32x4 a = *(LAS f32x4*)s + *(LAS f32x4*)(s + 2176) + (*(LAS f32x4*)(s + 2 * 2176) + *(LAS f32x4*)(s + 3 * 2176));
          __builtin_amdgcn_raw_buffer_store_b128(__builtin_bit_cast(u32x4, a), rsWS, WS_KVP + (quad * 16 + rt) * KVP_BYTES + u * 16, 0, 16);
        }
      }
    }
  } else {
    if (sp == 1 && rt < 15) {
      const unsigned ex = WS_HALO + ((rt * 8 + quad) * 4 + c * 2 + p) * 1024;
      if (i32 >= 24) {
        __builtin_amdgcn_raw_buffer_store_b128(__builtin_bit_cast(u32x4, Kf[0]), rsWS, ex + h * 128 + (i32 - 24) * 16, 0, 16);
        __builtin_amdgcn_raw_buffer_store_b128(__builtin_bit_cast(u32x4, Kf[1]), rsWS, ex + 256 + h * 128 + (i32 - 24) * 16, 0, 16);
      }
      const u32x4 v1 = __builtin_bit_cast(u32x4, Vf[1]);
      __builtin_amdgcn_raw_buffer_store_b64((u32x2){v1[2], v1[3]}, rsWS, ex + 512 + lane * 8, 0, 16);
    }
  }
  fill(11);
  f32x16 accQ = {};
#pragma unroll
  for (int t = 0; t < 16; ++t) accQ = MFMA(tr_frag(wb, t * 2048, t * 2048 + 512), xf[t], accQ);
  {
    const bool pub = BR == 0 ? (wid == 7) : (sp == 1 && rt < 15);
    if (pub) {
      WAIT_VM(4);
      if (lane == 0) flag_store(ws, BR == 0 ? WS_FKV + (quad * 16 + rt) * 128 : WS_FH + ((rt * 8 + quad) * 4 + c * 2 + p) * 64, tagP);
    }
  }
#pragma unroll
  for (int t = 0; t < 16; ++t) accQ = MFMA(tr_frag(wb + SLOT, t * 2048, t * 2048 + 512), xf[16 + t], accQ);
  if (BR == 0) {
#pragma unroll
    for (int r = 0; r < 16; ++r) accQ[r] = phi(accQ[r]);
  }
  bf16x8 Qf[2] = {pack8(accQ, 0), pack8(accQ, 8)};
  STAMP(4);
  if (c == 0) {
    f32x16 ga = {};
#pragma unroll
    for (int t = 0; t < 32; ++t) ga = MFMA(lds_frag(L + L_GW + t * 32 + h * 16), xf[t], ga);
    const float g = __builtin_amdgcn_rcpf(1.f + __builtin_amdgcn_exp2f(-LOG2E * ga[0]));
    if (h == 0) *(LAS float*)(L + L_G + (rq * 32 + i32) * 4) = BR == 0 ? g : 1.f - g;
  }
  const unsigned ofrag = WS_O + ((((rt * 2 + sp) * 2 + p) * 64) + BR * 32 + quad * 4 + 2 * c) * 1024 + lane * 16;
  if (BR == 0) {
    const int u = wid >= 4 ? (wid - 4) * 64 + lane : (wid == 0 && lane < 16 ? 256 + lane : -1);
    if (wid >= 4 || wid == 0) {
      poll_flags(ws, WS_FKV + quad * 16 * 128, 128, 16, tagG, lane);
      STAMP(5);
      if (u >= 0) {
        f32x4 a[16];
#pragma unroll
        for (int r = 0; r < 16; ++r)
          a[r] = __builtin_bit_cast(f32x4, __builtin_amdgcn_raw_buffer_load_b128(rsWS, WS_KVP + quad * 16 * KVP_BYTES + u * 16, r * KVP_BYTES, 16));
#pragma unroll
        for (int s = 1; s < 16; s *= 2)
#pragma unroll
          for (int r = 0; r < 16; r += 2 * s) a[r] += a[r + s];
        *(LAS f32x4*)(L + L_TOT + u * 16) = a[0];
      }
      if (rt == 0 && wid == 0 && lane == 0) flag_store(ws, WS_EKV + quad * 128, tagG);
    }
    LBAR();
    STAMP(6);
    const float gs = *(LAS float*)(L + L_G + (rq * 32 + i32) * 4);
    const f32x4 u0 = *(LAS f32x4*)(L + L_TOT + (c * 128 + lane) * 16), u1 = *(LAS f32x4*)(L + L_TOT + (c * 128 + 64 + lane) * 16);
    const lptr zp = L + L_TOT + 4096 + c * 128 + h * 64;
    const f32x4 z0 = *(LAS f32x4*)zp, z1 = *(LAS f32x4*)(zp + 16), z2 = *(LAS f32x4*)(zp + 32), z3 = *(LAS f32x4*)(zp + 48);
    const bool lo = i32 < 16;
    const u32x4 pu = {pk(u0[0], u0[1]), pk(u0[2], u0[3]), pk(u1[0], u1[1]), pk(u1[2], u1[3])};
    const u32x4 pz0 = {pk(z0[0], z0[1]), pk(z0[2], z0[3]), pk(z1[0], z1[1]), pk(z1[2], z1[3])};
    const u32x4 pz1 = {pk(z2[0], z2[1]), pk(z2[2], z2[3]), pk(z3[0], z3[1]), pk(z3[2], z3[3])};
    u32x4 a0, a1;
#pragma unroll
    for (int j = 0; j < 4; ++j) { a0[j] = lo ? pu[j] : pz0[j]; a1[j] = lo ? pz1[j] : pu[j]; }
    const f32x16 zero = {};
    const f32x16 d0 = MFMA(__builtin_bit_cast(bf16x8, a0), Qf[0], zero);
    const f32x16 d1 = MFMA(__builtin_bit_cast(bf16x8, a1), Qf[1], zero);
    const float s0 = gs * __builtin_amdgcn_rcpf(d0[8] + 1e-6f), s1 = gs * __builtin_amdgcn_rcpf(d1[0] + 1e-6f);
    const u32x4 o0 = {pk(d0[0] * s0, d0[1] * s0), pk(d0[2] * s0, d0[3] * s0), pk(d0[4] * s0, d0[5] * s0), pk(d0[6] * s0, d0[7] * s0)};
    const u32x4 o1 = {pk(d1[8] * s1, d1[9] * s1), pk(d1[10] * s1, d1[11] * s1), pk(d1[12] * s1, d1[13] * s1), pk(d1[14] * s1, d1[15] * s1)};
    __builtin_amdgcn_raw_buffer_store_b128(o0, rsWS, ofrag, 0, 16);
    __builtin_amdgcn_raw_buffer_store_b128(o1, rsWS, ofrag + 1024, 0, 16);
  } else {
    LBAR();
    STAMP(5);
    bf16x8 Kp[2];
    u32x2 vp;
    if (sp == 1) {
      const lptr ex = L + L_PART + (c * 2 + p) * 1024;
      Kp[0] = lds_frag(ex + h * 128 + (i32 & 7) * 16);
      Kp[1] = lds_frag(ex + 256 + h * 128 + (i32 & 7) * 16);
      vp = *(LAS u32x2*)(ex + 512 + lane * 8);
    } else if (rt > 0) {
      poll_flags(ws, WS_FH + (((rt - 1) * 8 + quad) * 4 + c * 2 + p) * 64, 64, 1, tagG, lane);
      const unsigned ex = WS_HALO + (((rt - 1) * 8 + quad) * 4 + c * 2 + p) * 1024;
      Kp[0] = __builtin_bit_cast(bf16x8, __builtin_amdgcn_raw_buffer_load_b128(rsWS, ex + h * 128 + (i32 & 7) * 16, 0, 16));
      Kp[1] = __builtin_bit_cast(bf16x8, __builtin_amdgcn_raw_buffer_load_b128(rsWS, ex + 256 + h * 128 + (i32 & 7) * 16, 0, 16));
      vp = __builtin_amdgcn_raw_buffer_load_b64(rsWS, ex + 512 + lane * 8, 0, 16);
      if (wid == 0 && lane == 0) flag_store(ws, WS_EH + ((rt - 1) * 8 + quad) * 128, tagG);
    } else {
      Kp[0] = Kf[0]; Kp[1] = Kf[1]; vp = (u32x2){0u, 0u};
    }
    STAMP(6);
    const bool noprev = (rt == 0 && sp == 0);
    f32x16 bias;
#pragma unroll
    for (int r = 0; r < 16; ++r) bias[r] = ((unsigned)(i32 - ((r & 3) + 8 * (r >> 2) + 4 * h)) <= 7u) ? 0.f : -1e30f;
    float bprev[4];
#pragma unroll
    for (int j = 0; j < 4; ++j) bprev[j] = (!noprev && (24 + 4 * h + j >= i32 + 25)) ? 0.f : -1e30f;
    const float gs = *(LAS float*)(L + L_G + (rq * 32 + i32) * 4);
    constexpr float C1 = 0.25f * LOG2E;
    const unsigned ONE2 = 0x3F803F80u;
    const f32x16 zero = {};
#pragma unroll
    for (int hh = 0; hh < 2; ++hh) {
      f32x16 sc = MFMA(Kf[hh], Qf[hh], bias);
      const f32x16 sv = MFMA(Kp[hh], Qf[hh], zero);
      float sp4[4];
#pragma unroll
      for (int j = 0; j < 4; ++j) sp4[j] = sv[12 + j] + bprev[j];
      float m = __builtin_fmaxf(__builtin_fmaxf(sp4[0], sp4[1]), __builtin_fmaxf(sp4[2], sp4[3]));
#pragma unroll
      for (int r = 0; r < 16; r += 2) m = __builtin_fmaxf(__builtin_fmaxf(m, sc[r]), sc[r + 1]);
      m = swap_max(m);
      const float nm = -m * C1;
#pragma unroll
      for (int r = 0; r < 16; ++r) sc[r] = __builtin_amdgcn_exp2f(__builtin_fmaf(sc[r], C1, nm));
#pragma unroll
      for (int j = 0; j < 4; ++j) sp4[j] = __builtin_amdgcn_exp2f(__builtin_fmaf(sp4[j], C1, nm));
      const bf16x8 P0 = pack8(sc, 0), P1 = pack8(sc, 8);
      const u32x4 ppv = {0u, 0u, pk(sp4[0], sp4[1]), pk(sp4[2], sp4[3])};
      const bool mine = (i32 < 16) == (hh == 0);
      const u32x4 v0 = __builtin_bit_cast(u32x4, Vf[0]), v1 = __builtin_bit_cast(u32x4, Vf[1]);
      u32x4 a0, a1, ap;
#pragma unroll
      for (int j = 0; j < 4; ++j) { a0[j] = mine ? v0[j] : ONE2; a1[j] = mine ? v1[j] : ONE2; }
      ap = (u32x4){0u, 0u, mine ? vp[0] : ONE2, mine ? vp[1] : ONE2};
      f32x16 o = MFMA(__builtin_bit_cast(bf16x8, a0), P0, zero);
      o = MFMA(__builtin_bit_cast(bf16x8, a1), P1, o);
      o = MFMA(__builtin_bit_cast(bf16x8, ap), __builtin_bit_cast(bf16x8, ppv), o);
      const int nb = hh == 0 ? 0 : 8, db = hh == 0 ? 8 : 0;
      const float s = gs * __builtin_amdgcn_rcpf(o[db]);
      const u32x4 ov = {pk(o[nb] * s, o[nb + 1] * s), pk(o[nb + 2] * s, o[nb + 3] * s), pk(o[nb + 4] * s, o[nb + 5] * s), pk(o[nb + 6] * s, o[nb + 7] * s)};
      __builtin_amdgcn_raw_buffer_store_b128(ov, rsWS, ofrag + hh * 1024, 0, 16);
    }
  }
  STAMP(7);
  fill(12); fill(13);
  WAIT_VM(8);
  if (lane == 0) flag_store(ws, WS_FO + (((((rt * 2 + sp) * 2 + p) * 2 + BR) * 8 + quad) * 2 + c) * 64, tagO);
  STAMP(8);
}

__global__ __launch_bounds__(512, 2) void gated_mix_kernel(const __hip_bfloat16* __restrict__ x, const __hip_bfloat16* __restrict__ wA,
                                                           const __hip_bfloat16* __restrict__ woA, const __hip_bfloat16* __restrict__ wB,
                                                           const __hip_bfloat16* __restrict__ woB, const __hip_bfloat16* __restrict__ gw,
                                                           __hip_bfloat16* __restrict__ out, char* ws) {
  __shared__ __attribute__((aligned(1024))) char smem[LDS_BYTES];
  const int tid = threadIdx.x, lane = tid & 63, wid = __builtin_amdgcn_readfirstlane(tid >> 6);
  const int b = blockIdx.x, xcd = b & 7, loc = b >> 3;
  const int br = xcd & 1, rt = (xcd >> 1) * 4 + (loc >> 3), quad = loc & 7;
  const u64 dstart = __builtin_amdgcn_s_memrealtime();
  if (tid == 0) __hip_atomic_store((gu64*)(ws + WS_STAMP + b * 8), dstart, __ATOMIC_RELAXED, __HIP_MEMORY_SCOPE_AGENT);
  const rsrc_t rsX = __builtin_amdgcn_make_buffer_rsrc((void*)x, 0, 2048 * 512 * 2, 0x00020000);
  const rsrc_t rsW = __builtin_amdgcn_make_buffer_rsrc((void*)(br ? wB : wA), 0, 512 * 1536 * 2, 0x00020000);
  const rsrc_t rsWoA = __builtin_amdgcn_make_buffer_rsrc((void*)woA, 0, 512 * 512 * 2, 0x00020000);
  const rsrc_t rsWoB = __builtin_amdgcn_make_buffer_rsrc((void*)woB, 0, 512 * 512 * 2, 0x00020000);
  const rsrc_t rsG = __builtin_amdgcn_make_buffer_rsrc((void*)gw, 0, 1024, 0x00020000);
  const rsrc_t rsWS = __builtin_amdgcn_make_buffer_rsrc((void*)ws, 0, WS_USED, 0x00020000);
  const lptr L = (lptr)smem;
  const unsigned lds0 = (unsigned)(uintptr_t)L;

  const int l8 = lane >> 3, c8 = lane & 7;
  const int xrow = rt * 128 + 64 * (wid >> 2) + ((wid >> 1) & 1) + 2 * (16 * (wid & 1) + l8);
  const unsigned vX0 = xrow * 1024 + ((c8 ^ (lane >> 4)) << 4);
  const unsigned vX1 = (xrow + 16) * 1024 + ((c8 ^ (4 + (lane >> 4))) << 4);
  const int wsw = (c8 ^ (((lane >> 4) & 1) << 2)) << 4;
  const unsigned vW = (8 * wid + l8) * 3072 + quad * 128 + wsw;
  const unsigned vWo = (8 * wid + l8) * 1024 + quad * 128 + wsw;

  asm volatile("s_nop 4" ::: "memory");
  if (wid == 0) dma16(lds0 + L_GW, lane * 16, rsG, 0);
#pragma unroll
  for (int f = 0; f < 3; ++f) {
    const unsigned sl = lds0 + f * SLOT;
    dma16(sl + wid * 2048, vX0, rsX, f * 128);
    dma16(sl + wid * 2048 + 1024, vX1, rsX, f * 128);
    dma16(sl + 16384 + wid * 1024, vW, rsW, f * 196608 + 1024);
    dma16(sl + 24576 + wid * 1024, vW, rsW, f * 196608 + 2048);
  }
  const u64 tagO = sload64(ws + WS_EO + rt * 128) + 1;
  u64 tagG, tagP;
  if (br == 0) {
    tagG = tagP = sload64(ws + WS_EKV + quad * 128) + 1;
  } else {
    tagP = sload64(ws + WS_EH + (rt * 8 + quad) * 128) + 1;
    tagG = sload64(ws + WS_EH + ((rt > 0 ? rt - 1 : 0) * 8 + quad) * 128) + 1;
  }
  STAMP(1);
  if (br == 0) phase1<0>(smem, rsX, rsW, rsWoA, rsWoB, rsWS, ws, rt, quad, wid, lane, vX0, vX1, vW, vWo, tagG, tagP, tagO);
  else phase1<1>(smem, rsX, rsW, rsWoA, rsWoB, rsWS, ws, rt, quad, wid, lane, vX0, vX1, vW, vWo, tagG, tagP, tagO);

  const int pt = wid & 1, kq = (wid >> 2) * 2 + ((wid >> 1) & 1);
  const int i32 = lane & 31, h = lane >> 5;
  WAIT_BAR(0);
  bf16x8 wo[32];
  {
    const int q4 = (lane & 15) >> 2, p4 = lane & 3, nsub = (lane >> 4) & 1;
    const int slot = kq == 0 ? 2 : kq == 1 ? 3 : kq == 2 ? 0 : 1;
    const lptr base = L + slot * SLOT + (4 * h + q4) * 128 + 8 * (p4 & 1);
    const int sw = ((q4 >> 1) & 1) << 2;
    const lptr b0 = base + ((2 * nsub + (p4 >> 1)) ^ sw) * 16, b1 = base + ((4 + 2 * nsub + (p4 >> 1)) ^ sw) * 16;
#pragma unroll
    for (int i = 0; i < 16; ++i) {
      wo[2 * i] = tr_frag(b0, i * 2048, i * 2048 + 1024);
      wo[2 * i + 1] = tr_frag(b1, i * 2048, i * 2048 + 1024);
    }
  }
  LBAR();
  STAMP(9);
  poll_flags(ws, WS_FO + (((((rt * 2 + br) * 2 + pt) * 2 + (kq >> 1)) * 8 + (kq & 1) * 4) * 2) * 64, 64, 8, tagO, lane);
  STAMP(10);
  f32x16 a0 = {}, a1 = {};
  {
    const unsigned ob = WS_O + ((((rt * 2 + br) * 2 + pt) * 64) + kq * 16) * 1024 + lane * 16;
    bf16x8 of[16];
#pragma unroll
    for (int i = 0; i < 16; ++i) of[i] = __builtin_bit_cast(bf16x8, __builtin_amdgcn_raw_buffer_load_b128(rsWS, ob, i * 1024, 16));
#pragma unroll
    for (int i = 0; i < 16; ++i) { a0 = MFMA(wo[2 * i], of[i], a0); a1 = MFMA(wo[2 * i + 1], of[i], a1); }
  }
  STAMP(11);
  {
    const lptr pr = L + (kq * 64 + pt * 32 + i32) * PROW + h * 16;
#pragma unroll
    for (int g4 = 0; g4 < 4; ++g4) {
      *(LAS f32x4*)(pr + g4 * 32) = (f32x4){a0[4 * g4], a0[4 * g4 + 1], a0[4 * g4 + 2], a0[4 * g4 + 3]};
      *(LAS f32x4*)(pr + 128 + g4 * 32) = (f32x4){a1[4 * g4], a1[4 * g4 + 1], a1[4 * g4 + 2], a1[4 * g4 + 3]};
    }
  }
  LBAR();
  {
    const int pos = tid >> 3, cg = tid & 7;
    const lptr pr = L + pos * PROW + cg * 32;
    f32x4 s0 = *(LAS f32x4*)pr, s1 = *(LAS f32x4*)(pr + 16);
#pragma unroll
    for (int k = 1; k < 4; ++k) { s0 += *(LAS f32x4*)(pr + k * 64 * PROW); s1 += *(LAS f32x4*)(pr + k * 64 * PROW + 16); }
    const int row = rt * 128 + 64 * br + 2 * (pos & 31) + (pos >> 5);
    const u32x4 ov = {pk(s0[0], s0[1]), pk(s0[2], s0[3]), pk(s1[0], s1[1]), pk(s1[2], s1[3])};
    *(GAS u32x4*)((GAS char*)out + row * 1024 + quad * 128 + cg * 16) = ov;
  }
  if (quad == 0 && br == 0 && tid == 0) flag_store(ws, WS_EO + rt * 128, tagO);
  asm volatile("s_waitcnt vmcnt(0)" ::: "memory");
  __syncthreads();
  if (wid == 0) {
    const u64 dend = __builtin_amdgcn_s_memrealtime();
    long long mn = 1 << 30, mx = 0;
    for (int i = 0; i < 4; ++i) {
      const u64 s = __hip_atomic_load((gu64*)(ws + WS_STAMP + (i * 64 + lane) * 8), __ATOMIC_RELAXED, __HIP_MEMORY_SCOPE_AGENT);
      const long long d = (long long)(dend - s);
      if (d >= 0 && d < 100000) { mn = d < mn ? d : mn; mx = d > mx ? d : mx; }
    }
    for (int m = 32; m >= 1; m >>= 1) { long long a = __shfl_xor(mn, m), c2 = __shfl_xor(mx, m); mn = a < mn ? a : mn; mx = c2 > mx ? c2 : mx; }
    const float v = (DIAGQ == 0 ? (float)mx : (float)(mx - mn)) * 0.01f;
    if (lane == 0 && v > DIAGTHR) out[(rt * 128 + 64 * br) * 512 + quad * 64] = __float2bfloat16(v);
  }
}

extern "C" void kernel_launch(void* const* d_in, const int* in_sizes, int n_in, void* d_out, int out_size, void* d_ws, size_t ws_size,
                              hipStream_t stream) {
  gated_mix_kernel<<<256, 512, 0, stream>>>((const __hip_bfloat16*)d_in[0], (const __hip_bfloat16*)d_in[1], (const __hip_bfloat16*)d_in[2],
                                            (const __hip_bfloat16*)d_in[4], (const __hip_bfloat16*)d_in[5], (const __hip_bfloat16*)d_in[6],
                                            (__hip_bfloat16*)d_out, (char*)d_ws);
}
```

```cpp
#include <hip/hip_runtime.h>
#include <hip/hip_bf16.h>
#include <stdint.h>

typedef __bf16 bf16x2 __attribute__((ext_vector_type(2)));
typedef __bf16 bf16x8 __attribute__((ext_vector_type(8)));
typedef short s16x4 __attribute__((ext_vector_type(4)));
typedef float f32x2 __attribute__((ext_vector_type(2)));
typedef float f32x4 __attribute__((ext_vector_type(4)));
typedef float f32x16 __attribute__((ext_vector_type(16)));
typedef unsigned u32x2 __attribute__((ext_vector_type(2)));
typedef unsigned u32x4 __attribute__((ext_vector_type(4)));
typedef unsigned long long u64;
#define LAS __attribute__((address_space(3)))
#define GAS __attribute__((address_space(1)))
#define DI __device__ __forceinline__
typedef LAS char* lptr;
typedef GAS u64 gu64;
typedef __amdgpu_buffer_rsrc_t rsrc_t;

constexpr unsigned WS_EO = 0;
constexpr unsigned WS_EKV = 2048;
constexpr unsigned WS_EH = 4096;
constexpr unsigned WS_FKV = 32768;
constexpr unsigned WS_FH = 65536;
constexpr unsigned WS_FO = 131072;
constexpr unsigned WS_KVP = 262144 + 65536;
constexpr unsigned KVP_BYTES = 4352;
constexpr unsigned WS_HALO = 1048576;
constexpr unsigned WS_O = 2097152;
constexpr unsigned WS_STAMP = 8u << 20;
constexpr unsigned WS_USED = 16u << 20;

constexpr int SLOT = 32768;
constexpr int L_GW = 131072;
constexpr int L_G = L_GW + 1024;
constexpr int L_PART = L_GW + 2048;
constexpr int L_TOT = L_PART + 8 * 2176;
constexpr int LDS_BYTES = 163840;
constexpr int PROW = 272;

constexpr float LOG2E = 1.4426950408889634f;
constexpr unsigned SPIN_CAP = 1u << 16;

#define MFMA(a, b, c) __builtin_amdgcn_mfma_f32_32x32x16_bf16((a), (b), (c), 0, 0, 0)
#define WAIT_BAR(N) asm volatile("s_waitcnt vmcnt(" #N ") lgkmcnt(0)\n\ts_barrier" ::: "memory")
#define WAIT_VM(N) asm volatile("s_waitcnt vmcnt(" #N ")" ::: "memory")
#define LBAR() asm volatile("s_waitcnt lgkmcnt(0)\n\ts_barrier" ::: "memory")

#define STAMP(i) do {} while (0)

DI unsigned pk(float a, float b) { return __builtin_bit_cast(unsigned, __builtin_convertvector((f32x2){a, b}, bf16x2)); }
DI bf16x8 pack8(const f32x16& a, int o) {
  u32x4 r = {pk(a[o], a[o + 1]), pk(a[o + 2], a[o + 3]), pk(a[o + 4], a[o + 5]), pk(a[o + 6], a[o + 7])};
  return __builtin_bit_cast(bf16x8, r);
}
DI float phi(float t) {
  const float e = __builtin_amdgcn_fmed3f(__builtin_amdgcn_exp2f(t * LOG2E), 0.f, 1.f);
  return __builtin_fmaxf(t + 1.f, e) + 1e-6f;
}
DI void dma16(unsigned lds, unsigned voff, rsrc_t rs, unsigned soff) {
  asm volatile("s_mov_b32 m0, %0\n\ts_nop 0\n\tbuffer_load_dwordx4 %1, %2, %3 offen lds" ::"s"(lds), "v"(voff), "s"(rs), "s"(soff) : "memory");
}
DI bf16x8 lds_frag(lptr p) { return *(const LAS bf16x8*)p; }
DI bf16x8 tr_frag(lptr p, int o0, int o1) {
  const s16x4 lo = __builtin_bit_cast(s16x4, __builtin_amdgcn_ds_read_tr16_b64_v4i16((LAS s16x4*)(p + o0)));
  const s16x4 hi = __builtin_bit_cast(s16x4, __builtin_amdgcn_ds_read_tr16_b64_v4i16((LAS s16x4*)(p + o1)));
  return __builtin_bit_cast(bf16x8, __builtin_shufflevector(lo, hi, 0, 1, 2, 3, 4, 5, 6, 7));
}
DI u64 sload64(const char* p) {
  u64 r;
  asm volatile("s_load_dwordx2 %0, %1, 0x0\n\ts_waitcnt lgkmcnt(0)" : "=s"(r) : "s"(p) : "memory");
  return r;
}
DI void flag_store(char* ws, unsigned off, u64 v) { __hip_atomic_store((gu64*)(ws + off), v, __ATOMIC_RELAXED, __HIP_MEMORY_SCOPE_AGENT); }
DI void poll_flags(char* ws, unsigned off0, unsigned stride, int n, u64 tag, int lane) {
  gu64* p = (gu64*)(ws + off0 + (unsigned)lane * stride);
  for (unsigned spins = 0; spins < SPIN_CAP; ++spins) {
    u64 v = tag;
    if (lane < n) v = __hip_atomic_load(p, __ATOMIC_RELAXED, __HIP_MEMORY_SCOPE_AGENT);
    if (__all(v == tag)) break;
    __builtin_amdgcn_s_sleep(1);
  }
  __builtin_amdgcn_fence(__ATOMIC_ACQUIRE, "wavefront");
  asm volatile("" ::: "memory");
}
DI float swap_max(float m) {
  auto r = __builtin_amdgcn_permlane32_swap(__float_as_uint(m), __float_as_uint(m), false, false);
  return __builtin_fmaxf(__uint_as_float(r[0]), __uint_as_float(r[1]));
}

template <int BR>
DI void phase1(char* smem_generic, const rsrc_t rsX, const rsrc_t rsW, const rsrc_t rsWoA, const rsrc_t rsWoB, const rsrc_t rsWS, char* ws,
               const int rt, const int quad, const int wid, const int lane, const unsigned vX0, const unsigned vX1, const unsigned vW,
               const unsigned vWo, const u64 tagG, const u64 tagP, const u64 tagO) {
  const lptr L = (lptr)smem_generic;
  const unsigned lds0 = (unsigned)(uintptr_t)L;
  const int c = wid >> 2, rq = wid & 3, p = rq & 1, sp = rq >> 1;
  const int i32 = lane & 31, h = lane >> 5;
  const unsigned pieceX = lds0 + wid * 2048, pieceW = lds0 + wid * 1024;

  auto fill = [&](int f) {
    const unsigned sl = (unsigned)(f & 3) * SLOT;
    if (f < 8) {
      dma16(pieceX + sl, vX0, rsX, f * 128);
      dma16(pieceX + sl + 1024, vX1, rsX, f * 128);
      dma16(pieceW + sl + 16384, vW, rsW, f * 196608 + 1024);
      dma16(pieceW + sl + 24576, vW, rsW, f * 196608 + 2048);
    } else if (f < 10) {
#pragma unroll
      for (int i = 0; i < 4; ++i) dma16(pieceW + sl + i * 8192, vW, rsW, ((f - 8) * 256 + i * 64) * 3072);
    } else {
      const int kq = f - 10;
      const unsigned so = (unsigned)(kq == 0 ? 2 : kq == 1 ? 3 : kq == 2 ? 0 : 1) * SLOT;
#pragma unroll
      for (int i = 0; i < 4; ++i) dma16(pieceW + so + i * 8192, vWo, kq < 2 ? rsWoA : rsWoB, ((kq & 1) * 256 + i * 64) * 1024);
    }
  };

  const lptr xb = L + (32 * rq + i32) * 128;
  const int xs = h ^ ((i32 >> 1) & 7);
  const int q4 = (lane & 15) >> 2, p4 = lane & 3, nsub = (lane >> 4) & 1;
  const int wchunk = (4 * c + 2 * nsub + (p4 >> 1)) ^ (((q4 >> 1) & 1) << 2);
  const lptr wb = L + (8 * h + q4) * 128 + wchunk * 16 + 8 * (p4 & 1);

  bf16x8 xf[32];
  f32x16 accK = {}, accV = {};
#pragma unroll
  for (int ch = 0; ch < 8; ++ch) {
    WAIT_BAR(8);
    fill(ch + 3);
    const int sl = (ch & 3) * SLOT;
#pragma unroll
    for (int t = 0; t < 4; ++t) {
      xf[ch * 4 + t] = lds_frag(xb + sl + ((xs ^ (2 * t)) << 4));
      const bf16x8 wk = tr_frag(wb + sl + 16384, t * 2048, t * 2048 + 512);
      const bf16x8 wv = tr_frag(wb + sl + 24576, t * 2048, t * 2048 + 512);
      accK = BR == 0 ? MFMA(xf[ch * 4 + t], wk, accK) : MFMA(wk, xf[ch * 4 + t], accK);
      accV = MFMA(xf[ch * 4 + t], wv, accV);
    }
  }
  STAMP(2);
  bf16x8 Kf[2], Vf[2];
  if (BR == 0) {
#pragma unroll
    for (int r = 0; r < 16; ++r) accK[r] = phi(accK[r]);
  }
  Kf[0] = pack8(accK, 0); Kf[1] = pack8(accK, 8);
  Vf[0] = pack8(accV, 0); Vf[1] = pack8(accV, 8);
  if (BR == 0) {
    const u32x4 one4 = {0x3F803F80u, 0x3F803F80u, 0x3F803F80u, 0x3F803F80u};
    const bf16x8 ones = __builtin_bit_cast(bf16x8, one4);
    f32x16 kv = {}, z = {};
    kv = MFMA(Kf[0], Vf[0], kv); z = MFMA(Kf[0], ones, z);
    kv = MFMA(Kf[1], Vf[1], kv); z = MFMA(Kf[1], ones, z);
    const bool lo = i32 < 16;
    f32x4 u0, u1;
#pragma unroll
    for (int j = 0; j < 4; ++j) { u0[j] = lo ? kv[j] : kv[8 + j]; u1[j] = lo ? kv[4 + j] : kv[12 + j]; }
    const lptr pp = L + L_PART + wid * 2176;
    *(LAS f32x4*)(pp + lane * 16) = u0;
    *(LAS f32x4*)(pp + 1024 + lane * 16) = u1;
    if (i32 == 0) {
#pragma unroll
      for (int g4 = 0; g4 < 4; ++g4) *(LAS f32x4*)(pp + 2048 + h * 64 + g4 * 16) = (f32x4){z[4 * g4], z[4 * g4 + 1], z[4 * g4 + 2], z[4 * g4 + 3]};
    }
  } else {
    if (sp == 0) {
      const lptr ex = L + L_PART + (c * 2 + p) * 1024;
      if (i32 >= 24) {
        *(LAS bf16x8*)(ex + h * 128 + (i32 - 24) * 16) = Kf[0];
        *(LAS bf16x8*)(ex + 256 + h * 128 + (i32 - 24) * 16) = Kf[1];
      }
      const u32x4 v1 = __builtin_bit_cast(u32x4, Vf[1]);
      *(LAS u32x2*)(ex + 512 + lane * 8) = (u32x2){v1[2], v1[3]};
    }
  }
  WAIT_BAR(4);
  STAMP(3);
  if (BR == 0) {
    if (wid == 7) {
#pragma unroll
      for (int it = 0; it < 5; ++it) {
        const int u = it * 64 + lane;
        if (u < 272) {
          const int cc = u < 256 ? (u >> 7) : ((u - 256) >> 3);
          const int o = u < 256 ? (u & 127) * 16 : 2048 + (u & 7) * 16;
          const lptr s = L + L_PART + cc * 4 * 2176 + o;
          const f32x4 a = *(LAS f32x4*)s + *(LAS f32x4*)(s + 2176) + (*(LAS f32x4*)(s + 2 * 2176) + *(LAS f32x4*)(s + 3 * 2176));
          __builtin_amdgcn_raw_buffer_store_b128(__builtin_bit_cast(u32x4, a), rsWS, WS_KVP + (quad * 16 + rt) * KVP_BYTES + u * 16, 0, 16);
        }
      }
    }
  } else {
    if (sp == 1 && rt < 15) {
      const unsigned ex = WS_HALO + ((rt * 8 + quad) * 4 + c * 2 + p) * 1024;
      if (i32 >= 24) {
        __builtin_amdgcn_raw_buffer_store_b128(__builtin_bit_cast(u32x4, Kf[0]), rsWS, ex + h * 128 + (i32 - 24) * 16, 0, 16);
        __builtin_amdgcn_raw_buffer_store_b128(__builtin_bit_cast(u32x4, Kf[1]), rsWS, ex + 256 + h * 128 + (i32 - 24) * 16, 0, 16);
      }
      const u32x4 v1 = __builtin_bit_cast(u32x4, Vf[1]);
      __builtin_amdgcn_raw_buffer_store_b64((u32x2){v1[2], v1[3]}, rsWS, ex + 512 + lane * 8, 0, 16);
    }
  }
  fill(11);
  f32x16 accQ = {};
#pragma unroll
  for (int t = 0; t < 16; ++t) accQ = MFMA(tr_frag(wb, t * 2048, t * 2048 + 512), xf[t], accQ);
  {
    const bool pub = BR == 0 ? (wid == 7) : (sp == 1 && rt < 15);
    if (pub) {
      WAIT_VM(4);
      if (lane == 0) flag_store(ws, BR == 0 ? WS_FKV + (quad * 16 + rt) * 128 : WS_FH + ((rt * 8 + quad) * 4 + c * 2 + p) * 64, tagP);
    }
  }
#pragma unroll
  for (int t = 0; t < 16; ++t) accQ = MFMA(tr_frag(wb + SLOT, t * 2048, t * 2048 + 512), xf[16 + t], accQ);
  if (BR == 0) {
#pragma unroll
    for (int r = 0; r < 16; ++r) accQ[r] = phi(accQ[r]);
  }
  bf16x8 Qf[2] = {pack8(accQ, 0), pack8(accQ, 8)};
  STAMP(4);
  if (c == 0) {
    f32x16 ga = {};
#pragma unroll
    for (int t = 0; t < 32; ++t) ga = MFMA(lds_frag(L + L_GW + t * 32 + h * 16), xf[t], ga);
    const float g = __builtin_amdgcn_rcpf(1.f + __builtin_amdgcn_exp2f(-LOG2E * ga[0]));
    if (h == 0) *(LAS float*)(L + L_G + (rq * 32 + i32) * 4) = BR == 0 ? g : 1.f - g;
  }
  const unsigned ofrag = WS_O + ((((rt * 2 + sp) * 2 + p) * 64) + BR * 32 + quad * 4 + 2 * c) * 1024 + lane * 16;
  if (BR == 0) {
    const int u = wid >= 4 ? (wid - 4) * 64 + lane : (wid == 0 && lane < 16 ? 256 + lane : -1);
    if (wid >= 4 || wid == 0) {
      poll_flags(ws, WS_FKV + quad * 16 * 128, 128, 16, tagG, lane);
      STAMP(5);
      if (u >= 0) {
        f32x4 a[16];
#pragma unroll
        for (int r = 0; r < 16; ++r)
          a[r] = __builtin_bit_cast(f32x4, __builtin_amdgcn_raw_buffer_load_b128(rsWS, WS_KVP + quad * 16 * KVP_BYTES + u * 16, r * KVP_BYTES, 16));
#pragma unroll
        for (int s = 1; s < 16; s *= 2)
#pragma unroll
          for (int r = 0; r < 16; r += 2 * s) a[r] += a[r + s];
        *(LAS f32x4*)(L + L_TOT + u * 16) = a[0];
      }
      if (rt == 0 && wid == 0 && lane == 0) flag_store(ws, WS_EKV + quad * 128, tagG);
    }
    LBAR();
    STAMP(6);
    const float gs = *(LAS float*)(L + L_G + (rq * 32 + i32) * 4);
    const f32x4 u0 = *(LAS f32x4*)(L + L_TOT + (c * 128 + lane) * 16), u1 = *(LAS f32x4*)(L + L_TOT + (c * 128 + 64 + lane) * 16);
    const lptr zp = L + L_TOT + 4096 + c * 128 + h * 64;
    const f32x4 z0 = *(LAS f32x4*)zp, z1 = *(LAS f32x4*)(zp + 16), z2 = *(LAS f32x4*)(zp + 32), z3 = *(LAS f32x4*)(zp + 48);
    const bool lo = i32 < 16;
    const u32x4 pu = {pk(u0[0], u0[1]), pk(u0[2], u0[3]), pk(u1[0], u1[1]), pk(u1[2], u1[3])};
    const u32x4 pz0 = {pk(z0[0], z0[1]), pk(z0[2], z0[3]), pk(z1[0], z1[1]), pk(z1[2], z1[3])};
    const u32x4 pz1 = {pk(z2[0], z2[1]), pk(z2[2], z2[3]), pk(z3[0], z3[1]), pk(z3[2], z3[3])};
    u32x4 a0, a1;
#pragma unroll
    for (int j = 0; j < 4; ++j) { a0[j] = lo ? pu[j] : pz0[j]; a1[j] = lo ? pz1[j] : pu[j]; }
    const f32x16 zero = {};
    const f32x16 d0 = MFMA(__builtin_bit_cast(bf16x8, a0), Qf[0], zero);
    const f32x16 d1 = MFMA(__builtin_bit_cast(bf16x8, a1), Qf[1], zero);
    const float s0 = gs * __builtin_amdgcn_rcpf(d0[8] + 1e-6f), s1 = gs * __builtin_amdgcn_rcpf(d1[0] + 1e-6f);
    const u32x4 o0 = {pk(d0[0] * s0, d0[1] * s0), pk(d0[2] * s0, d0[3] * s0), pk(d0[4] * s0, d0[5] * s0), pk(d0[6] * s0, d0[7] * s0)};
    const u32x4 o1 = {pk(d1[8] * s1, d1[9] * s1), pk(d1[10] * s1, d1[11] * s1), pk(d1[12] * s1, d1[13] * s1), pk(d1[14] * s1, d1[15] * s1)};
    __builtin_amdgcn_raw_buffer_store_b128(o0, rsWS, ofrag, 0, 16);
    __builtin_amdgcn_raw_buffer_store_b128(o1, rsWS, ofrag + 1024, 0, 16);
  } else {
    LBAR();
    STAMP(5);
    bf16x8 Kp[2];
    u32x2 vp;
    if (sp == 1) {
      const lptr ex = L + L_PART + (c * 2 + p) * 1024;
      Kp[0] = lds_frag(ex + h * 128 + (i32 & 7) * 16);
      Kp[1] = lds_frag(ex + 256 + h * 128 + (i32 & 7) * 16);
      vp = *(LAS u32x2*)(ex + 512 + lane * 8);
    } else if (rt > 0) {
      poll_flags(ws, WS_FH + (((rt - 1) * 8 + quad) * 4 + c * 2 + p) * 64, 64, 1, tagG, lane);
      const unsigned ex = WS_HALO + (((rt - 1) * 8 + quad) * 4 + c * 2 + p) * 1024;
      Kp[0] = __builtin_bit_cast(bf16x8, __builtin_amdgcn_raw_buffer_load_b128(rsWS, ex + h * 128 + (i32 & 7) * 16, 0, 16));
      Kp[1] = __builtin_bit_cast(bf16x8, __builtin_amdgcn_raw_buffer_load_b128(rsWS, ex + 256 + h * 128 + (i32 & 7) * 16, 0, 16));
      vp = __builtin_amdgcn_raw_buffer_load_b64(rsWS, ex + 512 + lane * 8, 0, 16);
      if (wid == 0 && lane == 0) flag_store(ws, WS_EH + ((rt - 1) * 8 + quad) * 128, tagG);
    } else {
      Kp[0] = Kf[0]; Kp[1] = Kf[1]; vp = (u32x2){0u, 0u};
    }
    STAMP(6);
    const bool noprev = (rt == 0 && sp == 0);
    f32x16 bias;
#pragma unroll
    for (int r = 0; r < 16; ++r) bias[r] = ((unsigned)(i32 - ((r & 3) + 8 * (r >> 2) + 4 * h)) <= 7u) ? 0.f : -1e30f;
    float bprev[4];
#pragma unroll
    for (int j = 0; j < 4; ++j) bprev[j] = (!noprev && (24 + 4 * h + j >= i32 + 25)) ? 0.f : -1e30f;
    const float gs = *(LAS float*)(L + L_G + (rq * 32 + i32) * 4);
    constexpr float C1 = 0.25f * LOG2E;
    const unsigned ONE2 = 0x3F803F80u;
    const f32x16 zero = {};
#pragma unroll
    for (int hh = 0; hh < 2; ++hh) {
      f32x16 sc = MFMA(Kf[hh], Qf[hh], bias);
      const f32x16 sv = MFMA(Kp[hh], Qf[hh], zero);
      float sp4[4];
#pragma unroll
      for (int j = 0; j < 4; ++j) sp4[j] = sv[12 + j] + bprev[j];
      float m = __builtin_fmaxf(__builtin_fmaxf(sp4[0], sp4[1]), __builtin_fmaxf(sp4[2], sp4[3]));
#pragma unroll
      for (int r = 0; r < 16; r += 2) m = __builtin_fmaxf(__builtin_fmaxf(m, sc[r]), sc[r + 1]);
      m = swap_max(m);
      const float nm = -m * C1;
#pragma unroll
      for (int r = 0; r < 16; ++r) sc[r] = __builtin_amdgcn_exp2f(__builtin_fmaf(sc[r], C1, nm));
#pragma unroll
      for (int j = 0; j < 4; ++j) sp4[j] = __builtin_amdgcn_exp2f(__builtin_fmaf(sp4[j], C1, nm));
      const bf16x8 P0 = pack8(sc, 0), P1 = pack8(sc, 8);
      const u32x4 ppv = {0u, 0u, pk(sp4[0], sp4[1]), pk(sp4[2], sp4[3])};
      const bool mine = (i32 < 16) == (hh == 0);
      const u32x4 v0 = __builtin_bit_cast(u32x4, Vf[0]), v1 = __builtin_bit_cast(u32x4, Vf[1]);
      u32x4 a0, a1, ap;
#pragma unroll
      for (int j = 0; j < 4; ++j) { a0[j] = mine ? v0[j] : ONE2; a1[j] = mine ? v1[j] : ONE2; }
      ap = (u32x4){0u, 0u, mine ? vp[0] : ONE2, mine ? vp[1] : ONE2};
      f32x16 o = MFMA(__builtin_bit_cast(bf16x8, a0), P0, zero);
      o = MFMA(__builtin_bit_cast(bf16x8, a1), P1, o);
      o = MFMA(__builtin_bit_cast(bf16x8, ap), __builtin_bit_cast(bf16x8, ppv), o);
      const int nb = hh == 0 ? 0 : 8, db = hh == 0 ? 8 : 0;
      const float s = gs * __builtin_amdgcn_rcpf(o[db]);
      const u32x4 ov = {pk(o[nb] * s, o[nb + 1] * s), pk(o[nb + 2] * s, o[nb + 3] * s), pk(o[nb + 4] * s, o[nb + 5] * s), pk(o[nb + 6] * s, o[nb + 7] * s)};
      __builtin_amdgcn_raw_buffer_store_b128(ov, rsWS, ofrag + hh * 1024, 0, 16);
    }
  }
  STAMP(7);
  fill(12); fill(13);
  WAIT_VM(8);
  if (lane == 0) flag_store(ws, WS_FO + (((((rt * 2 + sp) * 2 + p) * 2 + BR) * 8 + quad) * 2 + c) * 64, tagO);
  STAMP(8);
}

__global__ __launch_bounds__(512, 2) void gated_mix_kernel(const __hip_bfloat16* __restrict__ x, const __hip_bfloat16* __restrict__ wA,
                                                           const __hip_bfloat16* __restrict__ woA, const __hip_bfloat16* __restrict__ wB,
                                                           const __hip_bfloat16* __restrict__ woB, const __hip_bfloat16* __restrict__ gw,
                                                           __hip_bfloat16* __restrict__ out, char* ws) {
  __shared__ __attribute__((aligned(1024))) char smem[LDS_BYTES];
  const int tid = threadIdx.x, lane = tid & 63, wid = __builtin_amdgcn_readfirstlane(tid >> 6);
  const int b = blockIdx.x, xcd = b & 7, loc = b >> 3;
  const int br = xcd & 1, rt = (xcd >> 1) * 4 + (loc >> 3), quad = loc & 7;
  STAMP(0);
  const rsrc_t rsX = __builtin_amdgcn_make_buffer_rsrc((void*)x, 0, 2048 * 512 * 2, 0x00020000);
  const rsrc_t rsW = __builtin_amdgcn_make_buffer_rsrc((void*)(br ? wB : wA), 0, 512 * 1536 * 2, 0x00020000);
  const rsrc_t rsWoA = __builtin_amdgcn_make_buffer_rsrc((void*)woA, 0, 512 * 512 * 2, 0x00020000);
  const rsrc_t rsWoB = __builtin_amdgcn_make_buffer_rsrc((void*)woB, 0, 512 * 512 * 2, 0x00020000);
  const rsrc_t rsG = __builtin_amdgcn_make_buffer_rsrc((void*)gw, 0, 1024, 0x00020000);
  const rsrc_t rsWS = __builtin_amdgcn_make_buffer_rsrc((void*)ws, 0, WS_USED, 0x00020000);
  const lptr L = (lptr)smem;
  const unsigned lds0 = (unsigned)(uintptr_t)L;

  const int l8 = lane >> 3, c8 = lane & 7;
  const int xrow = rt * 128 + 64 * (wid >> 2) + ((wid >> 1) & 1) + 2 * (16 * (wid & 1) + l8);
  const unsigned vX0 = xrow * 1024 + ((c8 ^ (lane >> 4)) << 4);
  const unsigned vX1 = (xrow + 16) * 1024 + ((c8 ^ (4 + (lane >> 4))) << 4);
  const int wsw = (c8 ^ (((lane >> 4) & 1) << 2)) << 4;
  const unsigned vW = (8 * wid + l8) * 3072 + quad * 128 + wsw;
  const unsigned vWo = (8 * wid + l8) * 1024 + quad * 128 + wsw;

  asm volatile("s_nop 4" ::: "memory");
  if (wid == 0) dma16(lds0 + L_GW, lane * 16, rsG, 0);
#pragma unroll
  for (int f = 0; f < 3; ++f) {
    const unsigned sl = lds0 + f * SLOT;
    dma16(sl + wid * 2048, vX0, rsX, f * 128);
    dma16(sl + wid * 2048 + 1024, vX1, rsX, f * 128);
    dma16(sl + 16384 + wid * 1024, vW, rsW, f * 196608 + 1024);
    dma16(sl + 24576 + wid * 1024, vW, rsW, f * 196608 + 2048);
  }
  const u64 tagO = sload64(ws + WS_EO + rt * 128) + 1;
  u64 tagG, tagP;
  if (br == 0) {
    tagG = tagP = sload64(ws + WS_EKV + quad * 128) + 1;
  } else {
    tagP = sload64(ws + WS_EH + (rt * 8 + quad) * 128) + 1;
    tagG = sload64(ws + WS_EH + ((rt > 0 ? rt - 1 : 0) * 8 + quad) * 128) + 1;
  }
  STAMP(1);
  if (br == 0) phase1<0>(smem, rsX, rsW, rsWoA, rsWoB, rsWS, ws, rt, quad, wid, lane, vX0, vX1, vW, vWo, tagG, tagP, tagO);
  else phase1<1>(smem, rsX, rsW, rsWoA, rsWoB, rsWS, ws, rt, quad, wid, lane, vX0, vX1, vW, vWo, tagG, tagP, tagO);

  const int pt = wid & 1, kq = (wid >> 2) * 2 + ((wid >> 1) & 1);
  const int i32 = lane & 31, h = lane >> 5;
  WAIT_BAR(0);
  bf16x8 wo[32];
  {
    const int q4 = (lane & 15) >> 2, p4 = lane & 3, nsub = (lane >> 4) & 1;
    const int slot = kq == 0 ? 2 : kq == 1 ? 3 : kq == 2 ? 0 : 1;
    const lptr base = L + slot * SLOT + (4 * h + q4) * 128 + 8 * (p4 & 1);
    const int sw = ((q4 >> 1) & 1) << 2;
    const lptr b0 = base + ((2 * nsub + (p4 >> 1)) ^ sw) * 16, b1 = base + ((4 + 2 * nsub + (p4 >> 1)) ^ sw) * 16;
#pragma unroll
    for (int i = 0; i < 16; ++i) {
      wo[2 * i] = tr_frag(b0, i * 2048, i * 2048 + 1024);
      wo[2 * i + 1] = tr_frag(b1, i * 2048, i * 2048 + 1024);
    }
  }
  LBAR();
  STAMP(9);
  poll_flags(ws, WS_FO + (((((rt * 2 + br) * 2 + pt) * 2 + (kq >> 1)) * 8 + (kq & 1) * 4) * 2) * 64, 64, 8, tagO, lane);
  STAMP(10);
  f32x16 a0 = {}, a1 = {};
  {
    const unsigned ob = WS_O + ((((rt * 2 + br) * 2 + pt) * 64) + kq * 16) * 1024 + lane * 16;
    bf16x8 of[16];
#pragma unroll
    for (int i = 0; i < 16; ++i) of[i] = __builtin_bit_cast(bf16x8, __builtin_amdgcn_raw_buffer_load_b128(rsWS, ob, i * 1024, 16));
#pragma unroll
    for (int i = 0; i < 16; ++i) { a0 = MFMA(wo[2 * i], of[i], a0); a1 = MFMA(wo[2 * i + 1], of[i], a1); }
  }
  STAMP(11);
  {
    const lptr pr = L + (kq * 64 + pt * 32 + i32) * PROW + h * 16;
#pragma unroll
    for (int g4 = 0; g4 < 4; ++g4) {
      *(LAS f32x4*)(pr + g4 * 32) = (f32x4){a0[4 * g4], a0[4 * g4 + 1], a0[4 * g4 + 2], a0[4 * g4 + 3]};
      *(LAS f32x4*)(pr + 128 + g4 * 32) = (f32x4){a1[4 * g4], a1[4 * g4 + 1], a1[4 * g4 + 2], a1[4 * g4 + 3]};
    }
  }
  LBAR();
  {
    const int pos = tid >> 3, cg = tid & 7;
    const lptr pr = L + pos * PROW + cg * 32;
    f32x4 s0 = *(LAS f32x4*)pr, s1 = *(LAS f32x4*)(pr + 16);
#pragma unroll
    for (int k = 1; k < 4; ++k) { s0 += *(LAS f32x4*)(pr + k * 64 * PROW); s1 += *(LAS f32x4*)(pr + k * 64 * PROW + 16); }
    const int row = rt * 128 + 64 * br + 2 * (pos & 31) + (pos >> 5);
    const u32x4 ov = {pk(s0[0], s0[1]), pk(s0[2], s0[3]), pk(s1[0], s1[1]), pk(s1[2], s1[3])};
    __builtin_amdgcn_raw_buffer_store_b128(ov, __builtin_amdgcn_make_buffer_rsrc((void*)out, 0, 2048 * 1024, 0x00020000), row * 1024 + quad * 128 + cg * 16, 0, 16);
  }
  if (quad == 0 && br == 0 && tid == 0) flag_store(ws, WS_EO + rt * 128, tagO);
  STAMP(12);
}

__global__ void k_empty(char* ws) { if (ws == nullptr) __builtin_trap(); }
__global__ __launch_bounds__(512, 2) void k_empty_big(char* ws) {
  __shared__ char sm[163840];
  asm volatile("v_mov_b32 v220, 0" ::: "v220");
  if (ws == nullptr) ((volatile char*)sm)[threadIdx.x] = 1;
}
extern "C" void kernel_launch(void* const* d_in, const int* in_sizes, int n_in, void* d_out, int out_size, void* d_ws, size_t ws_size,
                              hipStream_t stream) {
  gated_mix_kernel<<<256, 512, 0, stream>>>((const __hip_bfloat16*)d_in[0], (const __hip_bfloat16*)d_in[1], (const __hip_bfloat16*)d_in[2],
                                            (const __hip_bfloat16*)d_in[4], (const __hip_bfloat16*)d_in[5], (const __hip_bfloat16*)d_in[6],
                                            (__hip_bfloat16*)d_out, (char*)d_ws);
}
```

```cpp
#include <hip/hip_runtime.h>
#include <hip/hip_bf16.h>
#include <stdint.h>

typedef __bf16 bf16x2 __attribute__((ext_vector_type(2)));
typedef __bf16 bf16x8 __attribute__((ext_vector_type(8)));
typedef short s16x4 __attribute__((ext_vector_type(4)));
typedef float f32x2 __attribute__((ext_vector_type(2)));
typedef float f32x4 __attribute__((ext_vector_type(4)));
typedef float f32x16 __attribute__((ext_vector_type(16)));
typedef unsigned u32x2 __attribute__((ext_vector_type(2)));
typedef unsigned u32x4 __attribute__((ext_vector_type(4)));
typedef unsigned long long u64;
#define LAS __attribute__((address_space(3)))
#define GAS __attribute__((address_space(1)))
#define DI __device__ __forceinline__
typedef LAS char* lptr;
typedef GAS u64 gu64;
typedef __amdgpu_buffer_rsrc_t rsrc_t;

constexpr unsigned WS_EO = 0;
constexpr unsigned WS_EKV = 2048;
constexpr unsigned WS_EH = 4096;
constexpr unsigned WS_FKV = 32768;
constexpr unsigned WS_FH = 65536;
constexpr unsigned WS_FO = 131072;
constexpr unsigned WS_KVP = 262144 + 65536;
constexpr unsigned KVP_BYTES = 4352;
constexpr unsigned WS_HALO = 1048576;
constexpr unsigned WS_O = 2097152;
constexpr unsigned WS_STAMP = 8u << 20;
constexpr unsigned WS_USED = 16u << 20;

constexpr int SLOT = 32768;
constexpr int L_GW = 0;
constexpr int L_G = L_GW + 1024;
constexpr int L_PART = L_GW + 2048;
constexpr int L_TOT = L_PART + 8 * 2176;
constexpr int L_RED = SLOT;
constexpr int LDS_BYTES = 163840;
constexpr int PROW = 272;
__host__ __device__ constexpr int slot_of(int f) { return f < 8 ? f % 5 : f == 8 ? 3 : f == 9 ? 4 : f - 9; }

constexpr float LOG2E = 1.4426950408889634f;
constexpr unsigned SPIN_CAP = 1u << 16;

#define MFMA(a, b, c) __builtin_amdgcn_mfma_f32_32x32x16_bf16((a), (b), (c), 0, 0, 0)
#define WAIT_BAR(N) asm volatile("s_waitcnt vmcnt(" #N ") lgkmcnt(0)\n\ts_barrier" ::: "memory")
#define WAIT_BAR_NL(N) asm volatile("s_waitcnt vmcnt(" #N ")\n\ts_barrier" ::: "memory")
#define WAIT_VM(N) asm volatile("s_waitcnt vmcnt(" #N ")" ::: "memory")
#define SBAR() __builtin_amdgcn_sched_barrier(0)
#define LBAR() asm volatile("s_waitcnt lgkmcnt(0)\n\ts_barrier" ::: "memory")

#define STAMP(i) do {} while (0)

DI unsigned pk(float a, float b) { return __builtin_bit_cast(unsigned, __builtin_convertvector((f32x2){a, b}, bf16x2)); }
DI bf16x8 pack8(const f32x16& a, int o) {
  u32x4 r = {pk(a[o], a[o + 1]), pk(a[o + 2], a[o + 3]), pk(a[o + 4], a[o + 5]), pk(a[o + 6], a[o + 7])};
  return __builtin_bit_cast(bf16x8, r);
}
DI unsigned phi_pk(float a, float b) {
  const f32x2 t = {a, b};
  const f32x2 u = t * (f32x2){LOG2E, LOG2E}, w = t + (f32x2){1.f, 1.f};
  const float e0 = __builtin_amdgcn_fmed3f(__builtin_amdgcn_exp2f(u[0]), 0.f, 1.f), e1 = __builtin_amdgcn_fmed3f(__builtin_amdgcn_exp2f(u[1]), 0.f, 1.f);
  const f32x2 r = (f32x2){__builtin_fmaxf(w[0], e0), __builtin_fmaxf(w[1], e1)} + (f32x2){1e-6f, 1e-6f};
  return pk(r[0], r[1]);
}
DI bf16x8 phi8(const f32x16& a, int o) {
  u32x4 r = {phi_pk(a[o], a[o + 1]), phi_pk(a[o + 2], a[o + 3]), phi_pk(a[o + 4], a[o + 5]), phi_pk(a[o + 6], a[o + 7])};
  return __builtin_bit_cast(bf16x8, r);
}
DI void dma16(unsigned lds, unsigned voff, rsrc_t rs, unsigned soff) {
  asm volatile("s_mov_b32 m0, %0\n\ts_nop 0\n\tbuffer_load_dwordx4 %1, %2, %3 offen lds" ::"s"(lds), "v"(voff), "s"(rs), "s"(soff) : "memory");
}
DI bf16x8 lds_frag(lptr p) { return *(const LAS bf16x8*)p; }
DI bf16x8 tr_frag(lptr p, int o0, int o1) {
  const s16x4 lo = __builtin_bit_cast(s16x4, __builtin_amdgcn_ds_read_tr16_b64_v4i16((LAS s16x4*)(p + o0)));
  const s16x4 hi = __builtin_bit_cast(s16x4, __builtin_amdgcn_ds_read_tr16_b64_v4i16((LAS s16x4*)(p + o1)));
  return __builtin_bit_cast(bf16x8, __builtin_shufflevector(lo, hi, 0, 1, 2, 3, 4, 5, 6, 7));
}
DI float pin(float x) { asm("" : "+v"(x)); return x; }
#define ACQ 0
constexpr int LD_AUX = ACQ ? 0 : 16;
DI void acquire() { if (ACQ) __builtin_amdgcn_fence(__ATOMIC_ACQUIRE, "agent"); }
DI void flag_store(char* ws, unsigned off, u64 v) { __hip_atomic_store((gu64*)(ws + off), v, __ATOMIC_RELAXED, __HIP_MEMORY_SCOPE_AGENT); }
DI void poll_flags(char* ws, unsigned off0, unsigned stride, int n, u64 tag, int lane) {
  gu64* p = (gu64*)(ws + off0 + (unsigned)lane * stride);
#pragma unroll 1
  for (unsigned spins = 0; spins < SPIN_CAP; ++spins) {
    u64 v = tag;
    if (lane < n) v = __hip_atomic_load(p, __ATOMIC_RELAXED, __HIP_MEMORY_SCOPE_AGENT);
    if (__all(v == tag)) break;
    __builtin_amdgcn_s_sleep(1);
  }
  __builtin_amdgcn_fence(__ATOMIC_ACQUIRE, "wavefront");
  asm volatile("" ::: "memory");
  acquire();
}
DI float swap_max(float m) {
  auto r = __builtin_amdgcn_permlane32_swap(__float_as_uint(m), __float_as_uint(m), false, false);
  return __builtin_fmaxf(__uint_as_float(r[0]), __uint_as_float(r[1]));
}

template <int BR>
DI void phase1(char* smem_generic, const rsrc_t rsX, const rsrc_t rsW, const rsrc_t rsWoA, const rsrc_t rsWoB, const rsrc_t rsG, const rsrc_t rsWS, char* ws,
               const int rt, const int quad, const int wid, const int lane, const unsigned vX0, const unsigned vX1, const unsigned vW,
               const unsigned vWo, const u64 tagG, const u64 tagP, const u64 tagO) {
  const lptr L = (lptr)smem_generic;
  const unsigned lds0 = (unsigned)(uintptr_t)L;
  const int c = wid >> 2, rq = wid & 3, p = rq & 1, sp = rq >> 1;
  const int i32 = lane & 31, h = lane >> 5;
  const unsigned pieceX = lds0 + wid * 2048, pieceW = lds0 + wid * 1024;

  auto piece = [&](int f, int i) {
    const unsigned sl = (unsigned)slot_of(f) * SLOT;
    if (f < 8) {
      if (i == 0) dma16(pieceX + sl, vX0, rsX, f * 128);
      else if (i == 1) dma16(pieceX + sl + 1024, vX1, rsX, f * 128);
      else dma16(pieceW + sl + 16384 + (i - 2) * 8192, vW, rsW, f * 196608 + (i - 1) * 1024);
    } else if (f < 10) {
      dma16(pieceW + sl + i * 8192, vW, rsW, ((f - 8) * 256 + i * 64) * 3072);
    } else {
      const int kq = f - 10;
      dma16(pieceW + sl + i * 8192, vWo, kq < 2 ? rsWoA : rsWoB, ((kq & 1) * 256 + i * 64) * 1024);
    }
  };
  auto fill = [&](int f) {
#pragma unroll
    for (int i = 0; i < 4; ++i) piece(f, i);
  };

  const int xs = h ^ ((i32 >> 1) & 7);
  const int q4 = (lane & 15) >> 2, p4 = lane & 3, nsub = (lane >> 4) & 1;
  const int wchunk = (4 * c + 2 * nsub + (p4 >> 1)) ^ (((q4 >> 1) & 1) << 2);
  lptr xb[3][4], wb[3];
#pragma unroll
  for (int t = 0; t < 4; ++t) {
    xb[0][t] = L + (32 * rq + i32) * 128 + ((xs ^ (2 * t)) << 4);
    xb[1][t] = xb[0][t] + 2 * SLOT;
    xb[2][t] = xb[0][t] + 4 * SLOT;
  }
  wb[0] = L + (8 * h + q4) * 128 + wchunk * 16 + 8 * (p4 & 1);
  wb[1] = wb[0] + 2 * SLOT;
  wb[2] = wb[0] + 4 * SLOT;
#pragma unroll
  for (int t = 0; t < 4; ++t) { asm("" : "+v"(xb[0][t])); asm("" : "+v"(xb[1][t])); asm("" : "+v"(xb[2][t])); }
  asm("" : "+v"(wb[0])); asm("" : "+v"(wb[1])); asm("" : "+v"(wb[2]));

  bf16x8 xf[32], wk[3], wv[3];
  f32x16 accK = {}, accV = {};
  auto rd1 = [&](int s) {
    const int sl = slot_of(s >> 2), t = s & 3;
    xf[s] = lds_frag(sl < 4 ? xb[sl >> 1][t] + (sl & 1) * SLOT : xb[2][t]);
    const lptr w = wb[sl >> 1];
    const int o = (sl & 1) * SLOT + 16384 + t * 2048;
    wk[s % 3] = tr_frag(w, o, o + 512);
    wv[s % 3] = tr_frag(w, o + 8192, o + 8192 + 512);
  };
  WAIT_BAR(12);
  STAMP(15);
  rd1(0); rd1(1);
  SBAR();
#pragma unroll
  for (int s = 0; s < 32; ++s) {
    const int cc = s >> 2;
    if ((s & 3) == 2 && cc < 7) {
      WAIT_BAR_NL(8);
      if (cc == 6 && wid == 0) dma16(lds0 + L_GW, lane * 16, rsG, 0);
    }
    if (s >= 2 && ((s - 2) >> 2) + 4 < 10) piece(((s - 2) >> 2) + 4, (s - 2) & 3);
    if (s + 2 < 32) rd1(s + 2);
    accK = BR == 0 ? MFMA(xf[s], wk[s % 3], accK) : MFMA(wk[s % 3], xf[s], accK);
    accV = MFMA(xf[s], wv[s % 3], accV);
    SBAR();
  }
  STAMP(2);
  bf16x8 Kf[2], Vf[2];
  if (BR == 0) { Kf[0] = phi8(accK, 0); Kf[1] = phi8(accK, 8); }
  else { Kf[0] = pack8(accK, 0); Kf[1] = pack8(accK, 8); }
  Vf[0] = pack8(accV, 0); Vf[1] = pack8(accV, 8);
  if (BR == 0) {
    const u32x4 one4 = {0x3F803F80u, 0x3F803F80u, 0x3F803F80u, 0x3F803F80u};
    const bf16x8 ones = __builtin_bit_cast(bf16x8, one4);
    f32x16 kv = {}, z = {};
    kv = MFMA(Kf[0], Vf[0], kv); z = MFMA(Kf[0], ones, z);
    kv = MFMA(Kf[1], Vf[1], kv); z = MFMA(Kf[1], ones, z);
    const bool lo = i32 < 16;
    f32x4 u0, u1;
#pragma unroll
    for (int j = 0; j < 4; ++j) { u0[j] = lo ? pin(kv[j]) : pin(kv[8 + j]); u1[j] = lo ? pin(kv[4 + j]) : pin(kv[12 + j]); }
    const lptr pp = L + L_PART + wid * 2176;
    *(LAS f32x4*)(pp + lane * 16) = u0;
    *(LAS f32x4*)(pp + 1024 + lane * 16) = u1;
    if (i32 == 0) {
#pragma unroll
      for (int g4 = 0; g4 < 4; ++g4) *(LAS f32x4*)(pp + 2048 + h * 64 + g4 * 16) = (f32x4){z[4 * g4], z[4 * g4 + 1], z[4 * g4 + 2], z[4 * g4 + 3]};
    }
  } else {
    if (sp == 0) {
      const lptr ex = L + L_PART + (c * 2 + p) * 1024;
      if (i32 >= 24) {
        *(LAS bf16x8*)(ex + h * 128 + (i32 - 24) * 16) = Kf[0];
        *(LAS bf16x8*)(ex + 256 + h * 128 + (i32 - 24) * 16) = Kf[1];
      }
      const u32x4 v1 = __builtin_bit_cast(u32x4, Vf[1]);
      *(LAS u32x2*)(ex + 512 + lane * 8) = (u32x2){v1[2], v1[3]};
    }
  }
  WAIT_BAR(0);
  STAMP(3);
  if (BR == 0) {
    if (wid == 7) {
      f32x4 pa[5][4];
#pragma unroll
      for (int it = 0; it < 5; ++it) {
        const int u = it < 4 ? it * 64 + lane : 256 + (lane & 15);
        const int cc = it < 4 ? (it >> 1) : ((lane >> 3) & 1);
        const int o = it < 4 ? (u & 127) * 16 : 2048 + (lane & 7) * 16;
        const lptr s = L + L_PART + cc * 4 * 2176 + o;
#pragma unroll
        for (int r = 0; r < 4; ++r) pa[it][r] = *(LAS f32x4*)(s + r * 2176);
      }
      SBAR();
#pragma unroll
      for (int it = 0; it < 5; ++it) {
        const f32x4 a = (pa[it][0] + pa[it][1]) + (pa[it][2] + pa[it][3]);
        const unsigned go = WS_KVP + (quad * 16 + rt) * KVP_BYTES + (it < 4 ? it * 64 + lane : 256 + lane) * 16;
        if (it < 4 || lane < 16) __builtin_amdgcn_raw_buffer_store_b128(__builtin_bit_cast(u32x4, a), rsWS, go, 0, 16);
      }
      STAMP(13);
      WAIT_VM(0);
      if (lane == 0) flag_store(ws, WS_FKV + (quad * 16 + rt) * 128, tagP);
      STAMP(14);
    }
  } else {
    if (sp == 1 && rt < 15) {
      const unsigned ex = WS_HALO + ((rt * 8 + quad) * 4 + c * 2 + p) * 1024;
      if (i32 >= 24) {
        __builtin_amdgcn_raw_buffer_store_b128(__builtin_bit_cast(u32x4, Kf[0]), rsWS, ex + h * 128 + (i32 - 24) * 16, 0, 16);
        __builtin_amdgcn_raw_buffer_store_b128(__builtin_bit_cast(u32x4, Kf[1]), rsWS, ex + 256 + h * 128 + (i32 - 24) * 16, 0, 16);
      }
      const u32x4 v1 = __builtin_bit_cast(u32x4, Vf[1]);
      __builtin_amdgcn_raw_buffer_store_b64((u32x2){v1[2], v1[3]}, rsWS, ex + 512 + lane * 8, 0, 16);
    }
  }
  fill(10); fill(11);
  f32x16 accQ = {};
  {
    bf16x8 wq[5];
    auto rd2 = [&](int s) {
      const lptr w = s < 16 ? wb[1] : wb[2];
      const int o = (s < 16 ? SLOT : -16 * 2048) + s * 2048;
      wq[s % 5] = tr_frag(w, o, o + 512);
    };
    rd2(0); rd2(1); rd2(2); rd2(3);
    SBAR();
#pragma unroll
    for (int s = 0; s < 32; ++s) {
      if (s + 4 < 32) rd2(s + 4);
      accQ = MFMA(wq[s % 5], xf[s], accQ);
      if (BR == 1 && s == 7) {
        if (sp == 1 && rt < 15) {
          WAIT_VM(8);
          if (lane == 0) flag_store(ws, WS_FH + ((rt * 8 + quad) * 4 + c * 2 + p) * 64, tagP);
        }
      }
      SBAR();
    }
  }
  bf16x8 Qf[2];
  if (BR == 0) { Qf[0] = phi8(accQ, 0); Qf[1] = phi8(accQ, 8); }
  else { Qf[0] = pack8(accQ, 0); Qf[1] = pack8(accQ, 8); }
  STAMP(4);
  if (c == 0) {
    f32x16 ga = {};
    bf16x8 gf[5];
    const lptr gp = L + L_GW + h * 16;
#pragma unroll
    for (int t = 0; t < 4; ++t) gf[t] = lds_frag(gp + t * 32);
    SBAR();
#pragma unroll
    for (int t = 0; t < 32; ++t) {
      if (t + 4 < 32) gf[(t + 4) % 5] = lds_frag(gp + (t + 4) * 32);
      ga = MFMA(gf[t % 5], xf[t], ga);
      SBAR();
    }
    const float g = __builtin_amdgcn_rcpf(1.f + __builtin_amdgcn_exp2f(-LOG2E * ga[0]));
    if (h == 0) *(LAS float*)(L + L_G + (rq * 32 + i32) * 4) = BR == 0 ? g : 1.f - g;
  }
  const unsigned ofrag = WS_O + ((((rt * 2 + sp) * 2 + p) * 64) + BR * 32 + quad * 4 + 2 * c) * 1024 + lane * 16;
  if (BR == 0) {
    const int u = wid >= 4 ? (wid - 4) * 64 + lane : (wid == 0 && lane < 16 ? 256 + lane : -1);
    if (wid >= 4 || wid == 0) {
      poll_flags(ws, WS_FKV + quad * 16 * 128, 128, 16, tagG, lane);
      STAMP(5);
      if (u >= 0) {
        f32x4 a[16];
#pragma unroll
        for (int r = 0; r < 16; ++r)
          a[r] = __builtin_bit_cast(f32x4, __builtin_amdgcn_raw_buffer_load_b128(rsWS, WS_KVP + quad * 16 * KVP_BYTES + u * 16, r * KVP_BYTES, LD_AUX));
        SBAR();
#pragma unroll
        for (int s = 1; s < 16; s *= 2)
#pragma unroll
          for (int r = 0; r < 16; r += 2 * s) a[r] += a[r + s];
        *(LAS f32x4*)(L + L_TOT + u * 16) = a[0];
      }
      if (rt == 0 && wid == 0 && lane == 0) flag_store(ws, WS_EKV + quad * 128, tagG);
    }
    LBAR();
    STAMP(6);
    fill(12); fill(13);
    const float gs = *(LAS float*)(L + L_G + (rq * 32 + i32) * 4);
    const f32x4 u0 = *(LAS f32x4*)(L + L_TOT + (c * 128 + lane) * 16), u1 = *(LAS f32x4*)(L + L_TOT + (c * 128 + 64 + lane) * 16);
    const lptr zp = L + L_TOT + 4096 + c * 128 + h * 64;
    const f32x4 z0 = *(LAS f32x4*)zp, z1 = *(LAS f32x4*)(zp + 16), z2 = *(LAS f32x4*)(zp + 32), z3 = *(LAS f32x4*)(zp + 48);
    const bool lo = i32 < 16;
    const u32x4 pu = {pk(u0[0], u0[1]), pk(u0[2], u0[3]), pk(u1[0], u1[1]), pk(u1[2], u1[3])};
    const u32x4 pz0 = {pk(z0[0], z0[1]), pk(z0[2], z0[3]), pk(z1[0], z1[1]), pk(z1[2], z1[3])};
    const u32x4 pz1 = {pk(z2[0], z2[1]), pk(z2[2], z2[3]), pk(z3[0], z3[1]), pk(z3[2], z3[3])};
    u32x4 a0, a1;
#pragma unroll
    for (int j = 0; j < 4; ++j) { a0[j] = lo ? pu[j] : pz0[j]; a1[j] = lo ? pz1[j] : pu[j]; }
    const f32x16 zero = {};
    const f32x16 d0 = MFMA(__builtin_bit_cast(bf16x8, a0), Qf[0], zero);
    const f32x16 d1 = MFMA(__builtin_bit_cast(bf16x8, a1), Qf[1], zero);
    const float s0 = gs * __builtin_amdgcn_rcpf(d0[8] + 1e-6f), s1 = gs * __builtin_amdgcn_rcpf(d1[0] + 1e-6f);
    const u32x4 o0 = {pk(d0[0] * s0, d0[1] * s0), pk(d0[2] * s0, d0[3] * s0), pk(d0[4] * s0, d0[5] * s0), pk(d0[6] * s0, d0[7] * s0)};
    const u32x4 o1 = {pk(d1[8] * s1, d1[9] * s1), pk(d1[10] * s1, d1[11] * s1), pk(d1[12] * s1, d1[13] * s1), pk(d1[14] * s1, d1[15] * s1)};
    __builtin_amdgcn_raw_buffer_store_b128(o0, rsWS, ofrag, 0, 16);
    __builtin_amdgcn_raw_buffer_store_b128(o1, rsWS, ofrag + 1024, 0, 16);
  } else {
    LBAR();
    STAMP(5);
    fill(12); fill(13);
    bf16x8 Kp[2];
    u32x2 vp;
    if (sp == 1) {
      const lptr ex = L + L_PART + (c * 2 + p) * 1024;
      Kp[0] = lds_frag(ex + h * 128 + (i32 & 7) * 16);
      Kp[1] = lds_frag(ex + 256 + h * 128 + (i32 & 7) * 16);
      vp = *(LAS u32x2*)(ex + 512 + lane * 8);
    } else if (rt > 0) {
      poll_flags(ws, WS_FH + (((rt - 1) * 8 + quad) * 4 + c * 2 + p) * 64, 64, 1, tagG, lane);
      const unsigned ex = WS_HALO + (((rt - 1) * 8 + quad) * 4 + c * 2 + p) * 1024;
      Kp[0] = __builtin_bit_cast(bf16x8, __builtin_amdgcn_raw_buffer_load_b128(rsWS, ex + h * 128 + (i32 & 7) * 16, 0, LD_AUX));
      Kp[1] = __builtin_bit_cast(bf16x8, __builtin_amdgcn_raw_buffer_load_b128(rsWS, ex + 256 + h * 128 + (i32 & 7) * 16, 0, LD_AUX));
      vp = __builtin_amdgcn_raw_buffer_load_b64(rsWS, ex + 512 + lane * 8, 0, LD_AUX);
      if (wid == 0 && lane == 0) flag_store(ws, WS_EH + ((rt - 1) * 8 + quad) * 128, tagG);
    } else {
      Kp[0] = Kf[0]; Kp[1] = Kf[1]; vp = (u32x2){0u, 0u};
    }
    STAMP(6);
    const bool noprev = (rt == 0 && sp == 0);
    f32x16 bias;
#pragma unroll
    for (int r = 0; r < 16; ++r) bias[r] = ((unsigned)(i32 - ((r & 3) + 8 * (r >> 2) + 4 * h)) <= 7u) ? 0.f : -1e30f;
    float bprev[4];
#pragma unroll
    for (int j = 0; j < 4; ++j) bprev[j] = (!noprev && (24 + 4 * h + j >= i32 + 25)) ? 0.f : -1e30f;
    const float gs = *(LAS float*)(L + L_G + (rq * 32 + i32) * 4);
    constexpr float C1 = 0.25f * LOG2E;
    const unsigned ONE2 = 0x3F803F80u;
    const f32x16 zero = {};
#pragma unroll
    for (int hh = 0; hh < 2; ++hh) {
      f32x16 sc = MFMA(Kf[hh], Qf[hh], bias);
      const f32x16 sv = MFMA(Kp[hh], Qf[hh], zero);
      float sp4[4];
#pragma unroll
      for (int j = 0; j < 4; ++j) sp4[j] = sv[12 + j] + bprev[j];
      float m = __builtin_fmaxf(__builtin_fmaxf(sp4[0], sp4[1]), __builtin_fmaxf(sp4[2], sp4[3]));
#pragma unroll
      for (int r = 0; r < 16; r += 2) m = __builtin_fmaxf(__builtin_fmaxf(m, sc[r]), sc[r + 1]);
      m = swap_max(m);
      const float nm = -m * C1;
#pragma unroll
      for (int r = 0; r < 16; ++r) sc[r] = __builtin_amdgcn_exp2f(__builtin_fmaf(sc[r], C1, nm));
#pragma unroll
      for (int j = 0; j < 4; ++j) sp4[j] = __builtin_amdgcn_exp2f(__builtin_fmaf(sp4[j], C1, nm));
      const bf16x8 P0 = pack8(sc, 0), P1 = pack8(sc, 8);
      const u32x4 ppv = {0u, 0u, pk(sp4[0], sp4[1]), pk(sp4[2], sp4[3])};
      const bool mine = (i32 < 16) == (hh == 0);
      const u32x4 v0 = __builtin_bit_cast(u32x4, Vf[0]), v1 = __builtin_bit_cast(u32x4, Vf[1]);
      u32x4 a0, a1, ap;
#pragma unroll
      for (int j = 0; j < 4; ++j) { a0[j] = mine ? v0[j] : ONE2; a1[j] = mine ? v1[j] : ONE2; }
      ap = (u32x4){0u, 0u, mine ? vp[0] : ONE2, mine ? vp[1] : ONE2};
      f32x16 o = MFMA(__builtin_bit_cast(bf16x8, a0), P0, zero);
      o = MFMA(__builtin_bit_cast(bf16x8, a1), P1, o);
      o = MFMA(__builtin_bit_cast(bf16x8, ap), __builtin_bit_cast(bf16x8, ppv), o);
      const int nb = hh == 0 ? 0 : 8, db = hh == 0 ? 8 : 0;
      const float s = gs * __builtin_amdgcn_rcpf(o[db]);
      const u32x4 ov = {pk(o[nb] * s, o[nb + 1] * s), pk(o[nb + 2] * s, o[nb + 3] * s), pk(o[nb + 4] * s, o[nb + 5] * s), pk(o[nb + 6] * s, o[nb + 7] * s)};
      __builtin_amdgcn_raw_buffer_store_b128(ov, rsWS, ofrag + hh * 1024, 0, 16);
    }
  }
  STAMP(7);
  WAIT_VM(0);
  if (lane == 0) flag_store(ws, WS_FO + (((((rt * 2 + sp) * 2 + p) * 2 + BR) * 8 + quad) * 2 + c) * 64, tagO);
  STAMP(8);
}

__global__ __launch_bounds__(512, 2) void gated_mix_kernel(const __hip_bfloat16* __restrict__ x, const __hip_bfloat16* __restrict__ wA,
                                                           const __hip_bfloat16* __restrict__ woA, const __hip_bfloat16* __restrict__ wB,
                                                           const __hip_bfloat16* __restrict__ woB, const __hip_bfloat16* __restrict__ gw,
                                                           __hip_bfloat16* __restrict__ out, char* ws) {
  __shared__ __attribute__((aligned(1024))) char smem[LDS_BYTES];
  const int tid = threadIdx.x, lane = tid & 63, wid = __builtin_amdgcn_readfirstlane(tid >> 6);
  const int b = blockIdx.x, xcd = b & 7, loc = b >> 3;
  const int br = xcd & 1, rt = (xcd >> 1) * 4 + (loc >> 3), quad = loc & 7;
  STAMP(0);
  const rsrc_t rsX = __builtin_amdgcn_make_buffer_rsrc((void*)x, 0, 2048 * 512 * 2, 0x00020000);
  const rsrc_t rsW = __builtin_amdgcn_make_buffer_rsrc((void*)(br ? wB : wA), 0, 512 * 1536 * 2, 0x00020000);
  const rsrc_t rsWoA = __builtin_amdgcn_make_buffer_rsrc((void*)woA, 0, 512 * 512 * 2, 0x00020000);
  const rsrc_t rsWoB = __builtin_amdgcn_make_buffer_rsrc((void*)woB, 0, 512 * 512 * 2, 0x00020000);
  const rsrc_t rsG = __builtin_amdgcn_make_buffer_rsrc((void*)gw, 0, 1024, 0x00020000);
  const rsrc_t rsWS = __builtin_amdgcn_make_buffer_rsrc((void*)ws, 0, WS_USED, 0x00020000);
  const lptr L = (lptr)smem;
  const unsigned lds0 = (unsigned)(uintptr_t)L;

  const int l8 = lane >> 3, c8 = lane & 7;
  const int xrow = rt * 128 + 64 * (wid >> 2) + ((wid >> 1) & 1) + 2 * (16 * (wid & 1) + l8);
  const unsigned vX0 = xrow * 1024 + ((c8 ^ (lane >> 4)) << 4);
  const unsigned vX1 = (xrow + 16) * 1024 + ((c8 ^ (4 + (lane >> 4))) << 4);
  const int wsw = (c8 ^ (((lane >> 4) & 1) << 2)) << 4;
  const unsigned vW = (8 * wid + l8) * 3072 + quad * 128 + wsw;
  const unsigned vWo = (8 * wid + l8) * 1024 + quad * 128 + wsw;

  asm volatile("s_nop 4" ::: "memory");
  u64 e0, e1, e2;
  {
    const char* p0 = ws + WS_EO + rt * 128;
    const char* p1 = ws + (br == 0 ? WS_EKV + quad * 128 : WS_EH + (rt * 8 + quad) * 128);
    const char* p2 = ws + (br == 0 ? WS_EKV + quad * 128 : WS_EH + ((rt > 0 ? rt - 1 : 0) * 8 + quad) * 128);
    asm volatile("s_load_dwordx2 %0, %3, 0x0\n\ts_load_dwordx2 %1, %4, 0x0\n\ts_load_dwordx2 %2, %5, 0x0"
                 : "=&s"(e0), "=&s"(e1), "=&s"(e2) : "s"(p0), "s"(p1), "s"(p2) : "memory");
  }
#pragma unroll
  for (int f = 0; f < 4; ++f) {
    const unsigned sl = lds0 + f * SLOT;
    dma16(sl + wid * 2048, vX0, rsX, f * 128);
    dma16(sl + wid * 2048 + 1024, vX1, rsX, f * 128);
    dma16(sl + 16384 + wid * 1024, vW, rsW, f * 196608 + 1024);
    dma16(sl + 24576 + wid * 1024, vW, rsW, f * 196608 + 2048);
    if (f < 2) asm volatile("s_barrier" ::: "memory");
  }
  asm volatile("s_waitcnt lgkmcnt(0)" : "+s"(e0), "+s"(e1), "+s"(e2)::"memory");
  const u64 tagO = e0 + 1, tagP = e1 + 1, tagG = e2 + 1;
  STAMP(1);
  if (br == 0) phase1<0>(smem, rsX, rsW, rsWoA, rsWoB, rsG, rsWS, ws, rt, quad, wid, lane, vX0, vX1, vW, vWo, tagG, tagP, tagO);
  else phase1<1>(smem, rsX, rsW, rsWoA, rsWoB, rsG, rsWS, ws, rt, quad, wid, lane, vX0, vX1, vW, vWo, tagG, tagP, tagO);

  const int pt = wid & 1, kq = (wid >> 2) * 2 + ((wid >> 1) & 1);
  const int i32 = lane & 31, h = lane >> 5;
  WAIT_BAR(0);
  bf16x8 wo[32];
  {
    const int q4 = (lane & 15) >> 2, p4 = lane & 3, nsub = (lane >> 4) & 1;
    const int slot = kq + 1;
    const lptr base = L + slot * SLOT + (4 * h + q4) * 128 + 8 * (p4 & 1);
    const int sw = ((q4 >> 1) & 1) << 2;
    const lptr b0 = base + ((2 * nsub + (p4 >> 1)) ^ sw) * 16, b1 = base + ((4 + 2 * nsub + (p4 >> 1)) ^ sw) * 16;
#pragma unroll
    for (int i = 0; i < 16; ++i) {
      wo[2 * i] = tr_frag(b0, i * 2048, i * 2048 + 1024);
      wo[2 * i + 1] = tr_frag(b1, i * 2048, i * 2048 + 1024);
    }
  }
  LBAR();
  STAMP(9);
  poll_flags(ws, WS_FO + (((((rt * 2 + br) * 2 + pt) * 2 + (kq >> 1)) * 8 + (kq & 1) * 4) * 2) * 64, 64, 8, tagO, lane);
  STAMP(10);
  f32x16 a0 = {}, a1 = {};
  {
    const unsigned ob = WS_O + ((((rt * 2 + br) * 2 + pt) * 64) + kq * 16) * 1024 + lane * 16;
    bf16x8 of[16];
#pragma unroll
    for (int i = 0; i < 16; ++i) of[i] = __builtin_bit_cast(bf16x8, __builtin_amdgcn_raw_buffer_load_b128(rsWS, ob, i * 1024, LD_AUX));
    SBAR();
#pragma unroll
    for (int i = 0; i < 16; ++i) { a0 = MFMA(wo[2 * i], of[i], a0); a1 = MFMA(wo[2 * i + 1], of[i], a1); }
  }
  STAMP(11);
  {
    const lptr pr = L + L_RED + (kq * 64 + pt * 32 + i32) * PROW + h * 16;
#pragma unroll
    for (int g4 = 0; g4 < 4; ++g4) {
      *(LAS f32x4*)(pr + g4 * 32) = (f32x4){a0[4 * g4], a0[4 * g4 + 1], a0[4 * g4 + 2], a0[4 * g4 + 3]};
      *(LAS f32x4*)(pr + 128 + g4 * 32) = (f32x4){a1[4 * g4], a1[4 * g4 + 1], a1[4 * g4 + 2], a1[4 * g4 + 3]};
    }
  }
  LBAR();
  {
    const int pos = tid >> 3, cg = tid & 7;
    const lptr pr = L + L_RED + pos * PROW + cg * 32;
    f32x4 s0 = *(LAS f32x4*)pr, s1 = *(LAS f32x4*)(pr + 16);
#pragma unroll
    for (int k = 1; k < 4; ++k) { s0 += *(LAS f32x4*)(pr + k * 64 * PROW); s1 += *(LAS f32x4*)(pr + k * 64 * PROW + 16); }
    const int row = rt * 128 + 64 * br + 2 * (pos & 31) + (pos >> 5);
    const u32x4 ov = {pk(s0[0], s0[1]), pk(s0[2], s0[3]), pk(s1[0], s1[1]), pk(s1[2], s1[3])};
    __builtin_amdgcn_raw_buffer_store_b128(ov, __builtin_amdgcn_make_buffer_rsrc((void*)out, 0, 2048 * 1024, 0x00020000), row * 1024 + quad * 128 + cg * 16, 0, 0);
  }
  if (quad == 0 && br == 0 && tid == 0) flag_store(ws, WS_EO + rt * 128, tagO);
  STAMP(12);
}

extern "C" void kernel_launch(void* const* d_in, const int* in_sizes, int n_in, void* d_out, int out_size, void* d_ws, size_t ws_size,
                              hipStream_t stream) {
  gated_mix_kernel<<<256, 512, 0, stream>>>((const __hip_bfloat16*)d_in[0], (const __hip_bfloat16*)d_in[1], (const __hip_bfloat16*)d_in[2],
                                            (const __hip_bfloat16*)d_in[4], (const __hip_bfloat16*)d_in[5], (const __hip_bfloat16*)d_in[6],
                                            (__hip_bfloat16*)d_out, (char*)d_ws);
}
```

```cpp
#include <hip/hip_runtime.h>
#include <hip/hip_bf16.h>
#include <stdint.h>

typedef __bf16 bf16x2 __attribute__((ext_vector_type(2)));
typedef __bf16 bf16x8 __attribute__((ext_vector_type(8)));
typedef short s16x4 __attribute__((ext_vector_type(4)));
typedef float f32x2 __attribute__((ext_vector_type(2)));
typedef float f32x4 __attribute__((ext_vector_type(4)));
typedef float f32x16 __attribute__((ext_vector_type(16)));
typedef unsigned u32x2 __attribute__((ext_vector_type(2)));
typedef unsigned u32x4 __attribute__((ext_vector_type(4)));
typedef unsigned long long u64;
#define LAS __attribute__((address_space(3)))
#define GAS __attribute__((address_space(1)))
#define DI __device__ __forceinline__
typedef LAS char* lptr;
typedef GAS u64 gu64;
typedef __amdgpu_buffer_rsrc_t rsrc_t;

constexpr unsigned WS_EO = 0;
constexpr unsigned WS_EKV = 2048;
constexpr unsigned WS_EH = 4096;
constexpr unsigned WS_FKV = 32768;
constexpr unsigned WS_FH = 65536;
constexpr unsigned WS_FO = 131072;
constexpr unsigned WS_KVP = 262144 + 65536;
constexpr unsigned KVP_BYTES = 4352;
constexpr unsigned WS_HALO = 1048576;
constexpr unsigned WS_O = 2097152;
constexpr unsigned WS_STAMP = 8u << 20;
constexpr unsigned WS_USED = 16u << 20;

constexpr int SLOT = 32768;
constexpr int L_GW = 0;
constexpr int L_G = L_GW + 1024;
constexpr int L_PART = L_GW + 2048;
constexpr int L_TOT = L_PART + 8 * 2176;
constexpr int L_RED = SLOT;
constexpr int LDS_BYTES = 163840;
constexpr int PROW = 272;
__host__ __device__ constexpr int slot_of(int f) { return f < 8 ? f % 5 : f == 8 ? 3 : f == 9 ? 4 : f - 9; }

constexpr float LOG2E = 1.4426950408889634f;
constexpr unsigned SPIN_CAP = 1u << 16;

#define MFMA(a, b, c) __builtin_amdgcn_mfma_f32_32x32x16_bf16((a), (b), (c), 0, 0, 0)
#define WAIT_BAR(N) asm volatile("s_waitcnt vmcnt(" #N ") lgkmcnt(0)\n\ts_barrier" ::: "memory")
#define WAIT_BAR_NL(N) asm volatile("s_waitcnt vmcnt(" #N ")\n\ts_barrier" ::: "memory")
#define WAIT_VM(N) asm volatile("s_waitcnt vmcnt(" #N ")" ::: "memory")
#define ROTM 6
#define ROT_OF(q, r) ((unsigned)(ROTM == 0 ? 0 : ROTM == 1 ? (q) : ROTM == 2 ? (q) + 2 * (r) : ROTM == 3 ? 2 * (r) : ROTM == 4 ? (r) : ROTM == 5 ? (r) + (q) : (r) + 4 * ((q) & 1)))
#define SBAR() __builtin_amdgcn_sched_barrier(0)
#define LBAR() asm volatile("s_waitcnt lgkmcnt(0)\n\ts_barrier" ::: "memory")

#define STAMP(i) do {} while (0)

DI unsigned pk(float a, float b) { return __builtin_bit_cast(unsigned, __builtin_convertvector((f32x2){a, b}, bf16x2)); }
DI bf16x8 pack8(const f32x16& a, int o) {
  u32x4 r = {pk(a[o], a[o + 1]), pk(a[o + 2], a[o + 3]), pk(a[o + 4], a[o + 5]), pk(a[o + 6], a[o + 7])};
  return __builtin_bit_cast(bf16x8, r);
}
DI unsigned phi_pk(float a, float b) {
  const f32x2 t = {a, b};
  const f32x2 u = t * (f32x2){LOG2E, LOG2E}, w = t + (f32x2){1.f, 1.f};
  const float e0 = __builtin_amdgcn_fmed3f(__builtin_amdgcn_exp2f(u[0]), 0.f, 1.f), e1 = __builtin_amdgcn_fmed3f(__builtin_amdgcn_exp2f(u[1]), 0.f, 1.f);
  const f32x2 r = (f32x2){__builtin_fmaxf(w[0], e0), __builtin_fmaxf(w[1], e1)} + (f32x2){1e-6f, 1e-6f};
  return pk(r[0], r[1]);
}
DI bf16x8 phi8(const f32x16& a, int o) {
  u32x4 r = {phi_pk(a[o], a[o + 1]), phi_pk(a[o + 2], a[o + 3]), phi_pk(a[o + 4], a[o + 5]), phi_pk(a[o + 6], a[o + 7])};
  return __builtin_bit_cast(bf16x8, r);
}
DI void dma16(unsigned lds, unsigned voff, rsrc_t rs, unsigned soff) {
  asm volatile("s_mov_b32 m0, %0\n\ts_nop 0\n\tbuffer_load_dwordx4 %1, %2, %3 offen lds" ::"s"(lds), "v"(voff), "s"(rs), "s"(soff) : "memory");
}
DI bf16x8 lds_frag(lptr p) { return *(const LAS bf16x8*)p; }
DI bf16x8 tr_frag(lptr p, int o0, int o1) {
  const s16x4 lo = __builtin_bit_cast(s16x4, __builtin_amdgcn_ds_read_tr16_b64_v4i16((LAS s16x4*)(p + o0)));
  const s16x4 hi = __builtin_bit_cast(s16x4, __builtin_amdgcn_ds_read_tr16_b64_v4i16((LAS s16x4*)(p + o1)));
  return __builtin_bit_cast(bf16x8, __builtin_shufflevector(lo, hi, 0, 1, 2, 3, 4, 5, 6, 7));
}
DI float pin(float x) { asm("" : "+v"(x)); return x; }
#define ACQ 0
constexpr int LD_AUX = ACQ ? 0 : 16;
DI void acquire() { if (ACQ) __builtin_amdgcn_fence(__ATOMIC_ACQUIRE, "agent"); }
DI void flag_store(char* ws, unsigned off, u64 v) { __hip_atomic_store((gu64*)(ws + off), v, __ATOMIC_RELAXED, __HIP_MEMORY_SCOPE_AGENT); }
DI void poll_flags(char* ws, unsigned off0, unsigned stride, int n, u64 tag, int lane) {
  gu64* p = (gu64*)(ws + off0 + (unsigned)lane * stride);
#pragma unroll 1
  for (unsigned spins = 0; spins < SPIN_CAP; ++spins) {
    u64 v = tag;
    if (lane < n) v = __hip_atomic_load(p, __ATOMIC_RELAXED, __HIP_MEMORY_SCOPE_AGENT);
    if (__all(v == tag)) break;
    __builtin_amdgcn_s_sleep(1);
  }
  __builtin_amdgcn_fence(__ATOMIC_ACQUIRE, "wavefront");
  asm volatile("" ::: "memory");
  acquire();
}
DI float swap_max(float m) {
  auto r = __builtin_amdgcn_permlane32_swap(__float_as_uint(m), __float_as_uint(m), false, false);
  return __builtin_fmaxf(__uint_as_float(r[0]), __uint_as_float(r[1]));
}

template <int BR>
DI void phase1(char* smem_generic, const rsrc_t rsX, const rsrc_t rsW, const rsrc_t rsWoA, const rsrc_t rsWoB, const rsrc_t rsG, const rsrc_t rsWS, char* ws,
               const int rt, const int quad, const int wid, const int lane, const unsigned vX0, const unsigned vX1, const unsigned vW,
               const unsigned vWo, const u64 tagG, const u64 tagP, const u64 tagO) {
  const lptr L = (lptr)smem_generic;
  const unsigned lds0 = (unsigned)(uintptr_t)L;
  const int c = wid >> 2, rq = wid & 3, p = rq & 1, sp = rq >> 1;
  const int i32 = lane & 31, h = lane >> 5;
  const unsigned pieceX = lds0 + wid * 2048, pieceW = lds0 + wid * 1024;
  const unsigned rot = ROT_OF(quad, rt);

  auto piece = [&](int f, int i) {
    const unsigned sl = (unsigned)slot_of(f) * SLOT;
    if (f < 8) {
      const unsigned kc = (f + rot) & 7;
      if (i == 0) dma16(pieceX + sl, vX0, rsX, kc * 128);
      else if (i == 1) dma16(pieceX + sl + 1024, vX1, rsX, kc * 128);
      else dma16(pieceW + sl + 16384 + (i - 2) * 8192, vW, rsW, kc * 196608 + (i - 1) * 1024);
    } else {
      dma16(pieceW + sl + i * 8192, vW, rsW, (((f - 8) * 4 + i + rot) & 7) * 196608);
    }
  };
  auto wo_piece = [&](int which, unsigned q) { dma16(lds0 + (which ? 3 : 1) * SLOT + q * 1024, vWo, which ? rsWoB : rsWoA, q * 8192); };

  const int xs = h ^ ((i32 >> 1) & 7);
  const int q4 = (lane & 15) >> 2, p4 = lane & 3, nsub = (lane >> 4) & 1;
  const int wchunk = (4 * c + 2 * nsub + (p4 >> 1)) ^ (((q4 >> 1) & 1) << 2);
  lptr xb[3][4], wb[3];
#pragma unroll
  for (int t = 0; t < 4; ++t) {
    xb[0][t] = L + (32 * rq + i32) * 128 + ((xs ^ (2 * t)) << 4);
    xb[1][t] = xb[0][t] + 2 * SLOT;
    xb[2][t] = xb[0][t] + 4 * SLOT;
  }
  wb[0] = L + (8 * h + q4) * 128 + wchunk * 16 + 8 * (p4 & 1);
  wb[1] = wb[0] + 2 * SLOT;
  wb[2] = wb[0] + 4 * SLOT;
#pragma unroll
  for (int t = 0; t < 4; ++t) { asm("" : "+v"(xb[0][t])); asm("" : "+v"(xb[1][t])); asm("" : "+v"(xb[2][t])); }
  asm("" : "+v"(wb[0])); asm("" : "+v"(wb[1])); asm("" : "+v"(wb[2]));

  bf16x8 xf[32], wk[3], wv[3];
  f32x16 accK = {}, accV = {};
  auto rd1 = [&](int s) {
    const int sl = slot_of(s >> 2), t = s & 3;
    xf[s] = lds_frag(sl < 4 ? xb[sl >> 1][t] + (sl & 1) * SLOT : xb[2][t]);
    const lptr w = wb[sl >> 1];
    const int o = (sl & 1) * SLOT + 16384 + t * 2048;
    wk[s % 3] = tr_frag(w, o, o + 512);
    wv[s % 3] = tr_frag(w, o + 8192, o + 8192 + 512);
  };
  WAIT_BAR(4);
  STAMP(15);
  rd1(0); rd1(1);
  SBAR();
#pragma unroll
  for (int s = 0; s < 32; ++s) {
    const int cc = s >> 2;
    if ((s & 3) == 2 && cc < 7) {
      if (s == 2) WAIT_BAR_NL(4); else WAIT_BAR_NL(8);
      if (cc == 6 && wid == 0) dma16(lds0 + L_GW, (lane * 16 + rot * 128) & 1023, rsG, 0);
    }
    if (s < 2) { piece(2, 2 * s); piece(2, 2 * s + 1); }
    else if (s < 6) { piece(3 + ((s - 2) >> 1), 2 * (s & 1)); piece(3 + ((s - 2) >> 1), 2 * (s & 1) + 1); }
    else if (((s - 2) >> 2) + 4 < 10) piece(((s - 2) >> 2) + 4, (s - 2) & 3);
    if (s + 2 < 32) rd1(s + 2);
    accK = BR == 0 ? MFMA(xf[s], wk[s % 3], accK) : MFMA(wk[s % 3], xf[s], accK);
    accV = MFMA(xf[s], wv[s % 3], accV);
    SBAR();
  }
  STAMP(2);
  bf16x8 Kf[2], Vf[2];
  if (BR == 0) { Kf[0] = phi8(accK, 0); Kf[1] = phi8(accK, 8); }
  else { Kf[0] = pack8(accK, 0); Kf[1] = pack8(accK, 8); }
  Vf[0] = pack8(accV, 0); Vf[1] = pack8(accV, 8);
  if (BR == 0) {
    const u32x4 one4 = {0x3F803F80u, 0x3F803F80u, 0x3F803F80u, 0x3F803F80u};
    const bf16x8 ones = __builtin_bit_cast(bf16x8, one4);
    f32x16 kv = {}, z = {};
    kv = MFMA(Kf[0], Vf[0], kv); z = MFMA(Kf[0], ones, z);
    kv = MFMA(Kf[1], Vf[1], kv); z = MFMA(Kf[1], ones, z);
    const bool lo = i32 < 16;
    f32x4 u0, u1;
#pragma unroll
    for (int j = 0; j < 4; ++j) { u0[j] = lo ? pin(kv[j]) : pin(kv[8 + j]); u1[j] = lo ? pin(kv[4 + j]) : pin(kv[12 + j]); }
    const lptr pp = L + L_PART + wid * 2176;
    *(LAS f32x4*)(pp + lane * 16) = u0;
    *(LAS f32x4*)(pp + 1024 + lane * 16) = u1;
    if (i32 == 0) {
#pragma unroll
      for (int g4 = 0; g4 < 4; ++g4) *(LAS f32x4*)(pp + 2048 + h * 64 + g4 * 16) = (f32x4){z[4 * g4], z[4 * g4 + 1], z[4 * g4 + 2], z[4 * g4 + 3]};
    }
  } else {
    if (sp == 0) {
      const lptr ex = L + L_PART + (c * 2 + p) * 1024;
      if (i32 >= 24) {
        *(LAS bf16x8*)(ex + h * 128 + (i32 - 24) * 16) = Kf[0];
        *(LAS bf16x8*)(ex + 256 + h * 128 + (i32 - 24) * 16) = Kf[1];
      }
      const u32x4 v1 = __builtin_bit_cast(u32x4, Vf[1]);
      *(LAS u32x2*)(ex + 512 + lane * 8) = (u32x2){v1[2], v1[3]};
    }
  }
  WAIT_BAR(0);
  STAMP(3);
  if (BR == 0) {
    if (wid == 7) {
      __builtin_amdgcn_s_setprio(3);
      f32x4 pa[5][4];
#pragma unroll
      for (int it = 0; it < 5; ++it) {
        const int u = it < 4 ? it * 64 + lane : 256 + (lane & 15);
        const int cc = it < 4 ? (it >> 1) : ((lane >> 3) & 1);
        const int o = it < 4 ? (u & 127) * 16 : 2048 + (lane & 7) * 16;
        const lptr s = L + L_PART + cc * 4 * 2176 + o;
#pragma unroll
        for (int r = 0; r < 4; ++r) pa[it][r] = *(LAS f32x4*)(s + r * 2176);
      }
      SBAR();
#pragma unroll
      for (int it = 0; it < 5; ++it) {
        const f32x4 a = (pa[it][0] + pa[it][1]) + (pa[it][2] + pa[it][3]);
        const unsigned go = WS_KVP + (quad * 16 + rt) * KVP_BYTES + (it < 4 ? it * 64 + lane : 256 + lane) * 16;
        if (it < 4 || lane < 16) __builtin_amdgcn_raw_buffer_store_b128(__builtin_bit_cast(u32x4, a), rsWS, go, 0, 16);
      }
      STAMP(13);
      WAIT_VM(0);
      if (lane == 0) flag_store(ws, WS_FKV + (quad * 16 + rt) * 128, tagP);
      STAMP(14);
      __builtin_amdgcn_s_setprio(0);
    }
  } else {
    if (sp == 1 && rt < 15) {
      const unsigned ex = WS_HALO + ((rt * 8 + quad) * 4 + c * 2 + p) * 1024;
      if (i32 >= 24) {
        __builtin_amdgcn_raw_buffer_store_b128(__builtin_bit_cast(u32x4, Kf[0]), rsWS, ex + h * 128 + (i32 - 24) * 16, 0, 16);
        __builtin_amdgcn_raw_buffer_store_b128(__builtin_bit_cast(u32x4, Kf[1]), rsWS, ex + 256 + h * 128 + (i32 - 24) * 16, 0, 16);
      }
      const u32x4 v1 = __builtin_bit_cast(u32x4, Vf[1]);
      __builtin_amdgcn_raw_buffer_store_b64((u32x2){v1[2], v1[3]}, rsWS, ex + 512 + lane * 8, 0, 16);
    }
  }
  f32x16 accQ = {};
  {
    bf16x8 wq[5];
    auto rd2 = [&](int s) {
      const lptr w = s < 16 ? wb[1] : wb[2];
      const int o = (s < 16 ? SLOT : -16 * 2048) + s * 2048;
      wq[s % 5] = tr_frag(w, o, o + 512);
    };
    rd2(0); rd2(1); rd2(2); rd2(3);
    SBAR();
#pragma unroll
    for (int s = 0; s < 32; ++s) {
      if (s + 4 < 32) rd2(s + 4);
      accQ = MFMA(wq[s % 5], xf[s], accQ);
      if (s >= 12 && s < 20) wo_piece(0, wid * 8 + (s - 12));
      if (BR == 1 && s == 7) {
        if (sp == 1 && rt < 15) {
          WAIT_VM(0);
          if (lane == 0) flag_store(ws, WS_FH + ((rt * 8 + quad) * 4 + c * 2 + p) * 64, tagP);
        }
      }
      SBAR();
    }
  }
  bf16x8 Qf[2];
  if (BR == 0) { Qf[0] = phi8(accQ, 0); Qf[1] = phi8(accQ, 8); }
  else { Qf[0] = pack8(accQ, 0); Qf[1] = pack8(accQ, 8); }
  LBAR();
  STAMP(4);
  if (BR == 0 ? (wid >= 1 && wid <= 3) : (sp == 1)) {
    const unsigned q0 = BR == 0 ? wid - 1 : c * 2 + p, qs = BR == 0 ? 3 : 4;
#pragma unroll 1
    for (unsigned q = q0; q < 64; q += qs) wo_piece(1, q);
  }
  if (c == 0) {
    f32x16 ga = {};
    bf16x8 gf[5];
    const lptr gp = L + L_GW + h * 16;
#pragma unroll
    for (int t = 0; t < 4; ++t) gf[t] = lds_frag(gp + t * 32);
    SBAR();
#pragma unroll
    for (int t = 0; t < 32; ++t) {
      if (t + 4 < 32) gf[(t + 4) % 5] = lds_frag(gp + (t + 4) * 32);
      ga = MFMA(gf[t % 5], xf[t], ga);
      SBAR();
    }
    const float g = __builtin_amdgcn_rcpf(1.f + __builtin_amdgcn_exp2f(-LOG2E * ga[0]));
    if (h == 0) *(LAS float*)(L + L_G + (rq * 32 + i32) * 4) = BR == 0 ? g : 1.f - g;
  }
  const unsigned ofrag = WS_O + ((((rt * 2 + sp) * 2 + p) * 64) + BR * 32 + quad * 4 + 2 * c) * 1024 + lane * 16;
  if (BR == 0) {
    const int u = wid >= 4 ? (wid - 4) * 64 + lane : (wid == 0 && lane < 16 ? 256 + lane : -1);
    if (wid >= 4 || wid == 0) {
      poll_flags(ws, WS_FKV + quad * 16 * 128, 128, 16, tagG, lane);
      STAMP(5);
      if (u >= 0) {
        f32x4 a[16];
#pragma unroll
        for (int r = 0; r < 16; ++r)
          a[r] = __builtin_bit_cast(f32x4, __builtin_amdgcn_raw_buffer_load_b128(rsWS, WS_KVP + quad * 16 * KVP_BYTES + u * 16, r * KVP_BYTES, LD_AUX));
        SBAR();
#pragma unroll
        for (int s = 1; s < 16; s *= 2)
#pragma unroll
          for (int r = 0; r < 16; r += 2 * s) a[r] += a[r + s];
        *(LAS f32x4*)(L + L_TOT + u * 16) = a[0];
      }
      if (rt == 0 && wid == 0 && lane == 0) flag_store(ws, WS_EKV + quad * 128, tagG);
    }
    LBAR();
    STAMP(6);
    const float gs = *(LAS float*)(L + L_G + (rq * 32 + i32) * 4);
    const f32x4 u0 = *(LAS f32x4*)(L + L_TOT + (c * 128 + lane) * 16), u1 = *(LAS f32x4*)(L + L_TOT + (c * 128 + 64 + lane) * 16);
    const lptr zp = L + L_TOT + 4096 + c * 128 + h * 64;
    const f32x4 z0 = *(LAS f32x4*)zp, z1 = *(LAS f32x4*)(zp + 16), z2 = *(LAS f32x4*)(zp + 32), z3 = *(LAS f32x4*)(zp + 48);
    const bool lo = i32 < 16;
    const u32x4 pu = {pk(u0[0], u0[1]), pk(u0[2], u0[3]), pk(u1[0], u1[1]), pk(u1[2], u1[3])};
    const u32x4 pz0 = {pk(z0[0], z0[1]), pk(z0[2], z0[3]), pk(z1[0], z1[1]), pk(z1[2], z1[3])};
    const u32x4 pz1 = {pk(z2[0], z2[1]), pk(z2[2], z2[3]), pk(z3[0], z3[1]), pk(z3[2], z3[3])};
    u32x4 a0, a1;
#pragma unroll
    for (int j = 0; j < 4; ++j) { a0[j] = lo ? pu[j] : pz0[j]; a1[j] = lo ? pz1[j] : pu[j]; }
    const f32x16 zero = {};
    const f32x16 d0 = MFMA(__builtin_bit_cast(bf16x8, a0), Qf[0], zero);
    const f32x16 d1 = MFMA(__builtin_bit_cast(bf16x8, a1), Qf[1], zero);
    const float s0 = gs * __builtin_amdgcn_rcpf(d0[8] + 1e-6f), s1 = gs * __builtin_amdgcn_rcpf(d1[0] + 1e-6f);
    const u32x4 o0 = {pk(d0[0] * s0, d0[1] * s0), pk(d0[2] * s0, d0[3] * s0), pk(d0[4] * s0, d0[5] * s0), pk(d0[6] * s0, d0[7] * s0)};
    const u32x4 o1 = {pk(d1[8] * s1, d1[9] * s1), pk(d1[10] * s1, d1[11] * s1), pk(d1[12] * s1, d1[13] * s1), pk(d1[14] * s1, d1[15] * s1)};
    __builtin_amdgcn_raw_buffer_store_b128(o0, rsWS, ofrag, 0, 16);
    __builtin_amdgcn_raw_buffer_store_b128(o1, rsWS, ofrag + 1024, 0, 16);
  } else {
    LBAR();
    STAMP(5);
    bf16x8 Kp[2];
    u32x2 vp;
    if (sp == 1) {
      const lptr ex = L + L_PART + (c * 2 + p) * 1024;
      Kp[0] = lds_frag(ex + h * 128 + (i32 & 7) * 16);
      Kp[1] = lds_frag(ex + 256 + h * 128 + (i32 & 7) * 16);
      vp = *(LAS u32x2*)(ex + 512 + lane * 8);
    } else if (rt > 0) {
      poll_flags(ws, WS_FH + (((rt - 1) * 8 + quad) * 4 + c * 2 + p) * 64, 64, 1, tagG, lane);
      const unsigned ex = WS_HALO + (((rt - 1) * 8 + quad) * 4 + c * 2 + p) * 1024;
      Kp[0] = __builtin_bit_cast(bf16x8, __builtin_amdgcn_raw_buffer_load_b128(rsWS, ex + h * 128 + (i32 & 7) * 16, 0, LD_AUX));
      Kp[1] = __builtin_bit_cast(bf16x8, __builtin_amdgcn_raw_buffer_load_b128(rsWS, ex + 256 + h * 128 + (i32 & 7) * 16, 0, LD_AUX));
      vp = __builtin_amdgcn_raw_buffer_load_b64(rsWS, ex + 512 + lane * 8, 0, LD_AUX);
      if (wid == 0 && lane == 0) flag_store(ws, WS_EH + ((rt - 1) * 8 + quad) * 128, tagG);
    } else {
      Kp[0] = Kf[0]; Kp[1] = Kf[1]; vp = (u32x2){0u, 0u};
    }
    STAMP(6);
    const bool noprev = (rt == 0 && sp == 0);
    f32x16 bias;
#pragma unroll
    for (int r = 0; r < 16; ++r) bias[r] = ((unsigned)(i32 - ((r & 3) + 8 * (r >> 2) + 4 * h)) <= 7u) ? 0.f : -1e30f;
    float bprev[4];
#pragma unroll
    for (int j = 0; j < 4; ++j) bprev[j] = (!noprev && (24 + 4 * h + j >= i32 + 25)) ? 0.f : -1e30f;
    const float gs = *(LAS float*)(L + L_G + (rq * 32 + i32) * 4);
    constexpr float C1 = 0.25f * LOG2E;
    const unsigned ONE2 = 0x3F803F80u;
    const f32x16 zero = {};
#pragma unroll
    for (int hh = 0; hh < 2; ++hh) {
      f32x16 sc = MFMA(Kf[hh], Qf[hh], bias);
      const f32x16 sv = MFMA(Kp[hh], Qf[hh], zero);
      float sp4[4];
#pragma unroll
      for (int j = 0; j < 4; ++j) sp4[j] = sv[12 + j] + bprev[j];
      float m = __builtin_fmaxf(__builtin_fmaxf(sp4[0], sp4[1]), __builtin_fmaxf(sp4[2], sp4[3]));
#pragma unroll
      for (int r = 0; r < 16; r += 2) m = __builtin_fmaxf(__builtin_fmaxf(m, sc[r]), sc[r + 1]);
      m = swap_max(m);
      const float nm = -m * C1;
#pragma unroll
      for (int r = 0; r < 16; ++r) sc[r] = __builtin_amdgcn_exp2f(__builtin_fmaf(sc[r], C1, nm));
#pragma unroll
      for (int j = 0; j < 4; ++j) sp4[j] = __builtin_amdgcn_exp2f(__builtin_fmaf(sp4[j], C1, nm));
      const bf16x8 P0 = pack8(sc, 0), P1 = pack8(sc, 8);
      const u32x4 ppv = {0u, 0u, pk(sp4[0], sp4[1]), pk(sp4[2], sp4[3])};
      const bool mine = (i32 < 16) == (hh == 0);
      const u32x4 v0 = __builtin_bit_cast(u32x4, Vf[0]), v1 = __builtin_bit_cast(u32x4, Vf[1]);
      u32x4 a0, a1, ap;
#pragma unroll
      for (int j = 0; j < 4; ++j) { a0[j] = mine ? v0[j] : ONE2; a1[j] = mine ? v1[j] : ONE2; }
      ap = (u32x4){0u, 0u, mine ? vp[0] : ONE2, mine ? vp[1] : ONE2};
      f32x16 o = MFMA(__builtin_bit_cast(bf16x8, a0), P0, zero);
      o = MFMA(__builtin_bit_cast(bf16x8, a1), P1, o);
      o = MFMA(__builtin_bit_cast(bf16x8, ap), __builtin_bit_cast(bf16x8, ppv), o);
      const int nb = hh == 0 ? 0 : 8, db = hh == 0 ? 8 : 0;
      const float s = gs * __builtin_amdgcn_rcpf(o[db]);
      const u32x4 ov = {pk(o[nb] * s, o[nb + 1] * s), pk(o[nb + 2] * s, o[nb + 3] * s), pk(o[nb + 4] * s, o[nb + 5] * s), pk(o[nb + 6] * s, o[nb + 7] * s)};
      __builtin_amdgcn_raw_buffer_store_b128(ov, rsWS, ofrag + hh * 1024, 0, 16);
    }
  }
  STAMP(7);
  WAIT_VM(0);
  if (lane == 0) flag_store(ws, WS_FO + (((((rt * 2 + sp) * 2 + p) * 2 + BR) * 8 + quad) * 2 + c) * 64, tagO);
  STAMP(8);
}

__global__ __launch_bounds__(512, 2) void gated_mix_kernel(const __hip_bfloat16* __restrict__ x, const __hip_bfloat16* __restrict__ wA,
                                                           const __hip_bfloat16* __restrict__ woA, const __hip_bfloat16* __restrict__ wB,
                                                           const __hip_bfloat16* __restrict__ woB, const __hip_bfloat16* __restrict__ gw,
                                                           __hip_bfloat16* __restrict__ out, char* ws) {
  __shared__ __attribute__((aligned(1024))) char smem[LDS_BYTES];
  const int tid = threadIdx.x, lane = tid & 63, wid = __builtin_amdgcn_readfirstlane(tid >> 6);
  const int b = blockIdx.x, xcd = b & 7, loc = b >> 3;
  const int br = xcd & 1, rt = (xcd >> 1) * 4 + (loc >> 3), quad = loc & 7;
  STAMP(0);
  const rsrc_t rsX = __builtin_amdgcn_make_buffer_rsrc((void*)x, 0, 2048 * 512 * 2, 0x00020000);
  const rsrc_t rsW = __builtin_amdgcn_make_buffer_rsrc((void*)(br ? wB : wA), 0, 512 * 1536 * 2, 0x00020000);
  const rsrc_t rsWoA = __builtin_amdgcn_make_buffer_rsrc((void*)woA, 0, 512 * 512 * 2, 0x00020000);
  const rsrc_t rsWoB = __builtin_amdgcn_make_buffer_rsrc((void*)woB, 0, 512 * 512 * 2, 0x00020000);
  const rsrc_t rsG = __builtin_amdgcn_make_buffer_rsrc((void*)gw, 0, 1024, 0x00020000);
  const rsrc_t rsWS = __builtin_amdgcn_make_buffer_rsrc((void*)ws, 0, WS_USED, 0x00020000);
  const lptr L = (lptr)smem;
  const unsigned lds0 = (unsigned)(uintptr_t)L;

  const int l8 = lane >> 3, c8 = lane & 7;
  const int xrow = rt * 128 + 64 * (wid >> 2) + ((wid >> 1) & 1) + 2 * (16 * (wid & 1) + l8);
  const unsigned vX0 = xrow * 1024 + ((c8 ^ (lane >> 4)) << 4);
  const unsigned vX1 = (xrow + 16) * 1024 + ((c8 ^ (4 + (lane >> 4))) << 4);
  const int wsw = (c8 ^ (((lane >> 4) & 1) << 2)) << 4;
  const unsigned vW = (8 * wid + l8) * 3072 + quad * 128 + wsw;
  const unsigned vWo = l8 * 1024 + quad * 128 + wsw;

  asm volatile("s_nop 4" ::: "memory");
  u64 e0, e1, e2;
  {
    const char* p0 = ws + WS_EO + rt * 128;
    const char* p1 = ws + (br == 0 ? WS_EKV + quad * 128 : WS_EH + (rt * 8 + quad) * 128);
    const char* p2 = ws + (br == 0 ? WS_EKV + quad * 128 : WS_EH + ((rt > 0 ? rt - 1 : 0) * 8 + quad) * 128);
    asm volatile("s_load_dwordx2 %0, %3, 0x0\n\ts_load_dwordx2 %1, %4, 0x0\n\ts_load_dwordx2 %2, %5, 0x0"
                 : "=&s"(e0), "=&s"(e1), "=&s"(e2) : "s"(p0), "s"(p1), "s"(p2) : "memory");
  }
#pragma unroll
  for (int f = 0; f < 2; ++f) {
    const unsigned sl = lds0 + f * SLOT;
    const unsigned kc = (f + ROT_OF(quad, rt)) & 7;
    dma16(sl + wid * 2048, vX0, rsX, kc * 128);
    dma16(sl + wid * 2048 + 1024, vX1, rsX, kc * 128);
    dma16(sl + 16384 + wid * 1024, vW, rsW, kc * 196608 + 1024);
    dma16(sl + 24576 + wid * 1024, vW, rsW, kc * 196608 + 2048);
    if (f < 1) asm volatile("s_barrier" ::: "memory");
  }
  asm volatile("s_waitcnt lgkmcnt(0)" : "+s"(e0), "+s"(e1), "+s"(e2)::"memory");
  const u64 tagO = e0 + 1, tagP = e1 + 1, tagG = e2 + 1;
  STAMP(1);
  if (br == 0) phase1<0>(smem, rsX, rsW, rsWoA, rsWoB, rsG, rsWS, ws, rt, quad, wid, lane, vX0, vX1, vW, vWo, tagG, tagP, tagO);
  else phase1<1>(smem, rsX, rsW, rsWoA, rsWoB, rsG, rsWS, ws, rt, quad, wid, lane, vX0, vX1, vW, vWo, tagG, tagP, tagO);

  const int pt = wid & 1, kq = (wid >> 2) * 2 + ((wid >> 1) & 1);
  const int i32 = lane & 31, h = lane >> 5;
  WAIT_BAR(0);
  bf16x8 wo[32];
  {
    const int q4 = (lane & 15) >> 2, p4 = lane & 3, nsub = (lane >> 4) & 1;
    const int slot = kq + 1;
    const lptr base = L + slot * SLOT + (4 * h + q4) * 128 + 8 * (p4 & 1);
    const int sw = ((q4 >> 1) & 1) << 2;
    const lptr b0 = base + ((2 * nsub + (p4 >> 1)) ^ sw) * 16, b1 = base + ((4 + 2 * nsub + (p4 >> 1)) ^ sw) * 16;
#pragma unroll
    for (int i = 0; i < 16; ++i) {
      wo[2 * i] = tr_frag(b0, i * 2048, i * 2048 + 1024);
      wo[2 * i + 1] = tr_frag(b1, i * 2048, i * 2048 + 1024);
    }
  }
  LBAR();
  STAMP(9);
  poll_flags(ws, WS_FO + (((((rt * 2 + br) * 2 + pt) * 2 + (kq >> 1)) * 8 + (kq & 1) * 4) * 2) * 64, 64, 8, tagO, lane);
  STAMP(10);
  f32x16 a0 = {}, a1 = {};
  {
    const unsigned ob = WS_O + ((((rt * 2 + br) * 2 + pt) * 64) + kq * 16) * 1024 + lane * 16;
    bf16x8 of[16];
#pragma unroll
    for (int i = 0; i < 16; ++i) of[i] = __builtin_bit_cast(bf16x8, __builtin_amdgcn_raw_buffer_load_b128(rsWS, ob, i * 1024, LD_AUX));
    SBAR();
#pragma unroll
    for (int i = 0; i < 16; ++i) { a0 = MFMA(wo[2 * i], of[i], a0); a1 = MFMA(wo[2 * i + 1], of[i], a1); }
  }
  STAMP(11);
  {
    const lptr pr = L + L_RED + (kq * 64 + pt * 32 + i32) * PROW + h * 16;
#pragma unroll
    for (int g4 = 0; g4 < 4; ++g4) {
      *(LAS f32x4*)(pr + g4 * 32) = (f32x4){a0[4 * g4], a0[4 * g4 + 1], a0[4 * g4 + 2], a0[4 * g4 + 3]};
      *(LAS f32x4*)(pr + 128 + g4 * 32) = (f32x4){a1[4 * g4], a1[4 * g4 + 1], a1[4 * g4 + 2], a1[4 * g4 + 3]};
    }
  }
  LBAR();
  {
    const int pos = tid >> 3, cg = tid & 7;
    const lptr pr = L + L_RED + pos * PROW + cg * 32;
    f32x4 s0 = *(LAS f32x4*)pr, s1 = *(LAS f32x4*)(pr + 16);
#pragma unroll
    for (int k = 1; k < 4; ++k) { s0 += *(LAS f32x4*)(pr + k * 64 * PROW); s1 += *(LAS f32x4*)(pr + k * 64 * PROW + 16); }
    const int row = rt * 128 + 64 * br + 2 * (pos & 31) + (pos >> 5);
    const u32x4 ov = {pk(s0[0], s0[1]), pk(s0[2], s0[3]), pk(s1[0], s1[1]), pk(s1[2], s1[3])};
    __builtin_amdgcn_raw_buffer_store_b128(ov, __builtin_amdgcn_make_buffer_rsrc((void*)out, 0, 2048 * 1024, 0x00020000), row * 1024 + quad * 128 + cg * 16, 0, 0);
  }
  if (quad == 0 && br == 0 && tid == 0) flag_store(ws, WS_EO + rt * 128, tagO);
  STAMP(12);
}

extern "C" void kernel_launch(void* const* d_in, const int* in_sizes, int n_in, void* d_out, int out_size, void* d_ws, size_t ws_size,
                              hipStream_t stream) {
  gated_mix_kernel<<<256, 512, 0, stream>>>((const __hip_bfloat16*)d_in[0], (const __hip_bfloat16*)d_in[1], (const __hip_bfloat16*)d_in[2],
                                            (const __hip_bfloat16*)d_in[4], (const __hip_bfloat16*)d_in[5], (const __hip_bfloat16*)d_in[6],
                                            (__hip_bfloat16*)d_out, (char*)d_ws);
}
```

```cpp
#include <hip/hip_runtime.h>
#include <hip/hip_bf16.h>
#include <stdint.h>

typedef __bf16 bf16x2 __attribute__((ext_vector_type(2)));
typedef __bf16 bf16x8 __attribute__((ext_vector_type(8)));
typedef short s16x4 __attribute__((ext_vector_type(4)));
typedef float f32x2 __attribute__((ext_vector_type(2)));
typedef float f32x4 __attribute__((ext_vector_type(4)));
typedef float f32x16 __attribute__((ext_vector_type(16)));
typedef unsigned u32x2 __attribute__((ext_vector_type(2)));
typedef unsigned u32x4 __attribute__((ext_vector_type(4)));
typedef unsigned long long u64;
#define LAS __attribute__((address_space(3)))
#define GAS __attribute__((address_space(1)))
#define DI __device__ __forceinline__
typedef LAS char* lptr;
typedef GAS u64 gu64;
typedef __amdgpu_buffer_rsrc_t rsrc_t;

constexpr unsigned WS_EO = 0;
constexpr unsigned WS_EKV = 2048;
constexpr unsigned WS_EH = 4096;
constexpr unsigned WS_FKV = 32768;
constexpr unsigned WS_FH = 65536;
constexpr unsigned WS_FO = 131072;
constexpr unsigned WS_KVP = 262144 + 65536;
constexpr unsigned KVP_BYTES = 4352;
constexpr unsigned WS_HALO = 1048576;
constexpr unsigned WS_O = 2097152;
constexpr unsigned WS_STAMP = 8u << 20;
constexpr unsigned WS_USED = 16u << 20;

constexpr int SLOT = 32768;
constexpr int L_GW = 0;
constexpr int L_G = L_GW + 1024;
constexpr int L_EP = L_GW + 1536;
constexpr int L_CNT = L_GW + 1568;
constexpr int L_WOB = L_GW + 1572;
constexpr int L_UIS = L_GW + 1576;
constexpr int L_PART = L_GW + 2048;
constexpr int L_TOT = L_PART + 8 * 2176;
constexpr int L_SINK = L_TOT + 4352;
constexpr int L_RED = SLOT;
constexpr int LDS_BYTES = 163840;
constexpr int PROW = 272;
#define slot_of(f) ((f) < 8 ? (f) % 5 : (f) == 8 ? 3 : 4)

constexpr float LOG2E = 1.4426950408889634f;
constexpr unsigned SPIN_CAP = 1u << 16;

#define MFMA(a, b, c) __builtin_amdgcn_mfma_f32_32x32x16_bf16((a), (b), (c), 0, 0, 0)
#define WAIT_BAR(N) asm volatile("s_waitcnt vmcnt(" #N ") lgkmcnt(0)\n\ts_barrier" ::: "memory")
#define WAIT_BAR_NL(N) asm volatile("s_waitcnt vmcnt(" #N ")\n\ts_barrier" ::: "memory")
#define WAIT_VM(N) asm volatile("s_waitcnt vmcnt(" #N ")" ::: "memory")
#define ROTM 6
#define ROT_OF(q, r) ((unsigned)(ROTM == 0 ? 0 : ROTM == 1 ? (q) : ROTM == 2 ? (q) + 2 * (r) : ROTM == 3 ? 2 * (r) : ROTM == 4 ? (r) : ROTM == 5 ? (r) + (q) : (r) + 4 * ((q) & 1)))
#define TOUCH 0
#define PUBS 7
#define SBAR() __builtin_amdgcn_sched_barrier(0)
#define LBAR() asm volatile("s_waitcnt lgkmcnt(0)\n\ts_barrier" ::: "memory")

#define STAMP(i) do {} while (0)
#define TSDUMP() do {} while (0)

#define NOPK 0
#if NOPK && defined(__HIP_DEVICE_COMPILE__)
#define LAM __attribute__((target("no-packed-fp32-ops"), always_inline))
#else
#define LAM __attribute__((always_inline))
#endif
#if NOPK && defined(__HIP_DEVICE_COMPILE__)
#pragma clang attribute push(__attribute__((target("no-packed-fp32-ops"))), apply_to = function)
#endif
DI float as_f(unsigned u) { return __builtin_bit_cast(float, u); }
DI unsigned as_u(float f) { return __builtin_bit_cast(unsigned, f); }
DI unsigned pk(float a, float b) { return __builtin_bit_cast(unsigned, __builtin_convertvector((f32x2){a, b}, bf16x2)); }
DI bf16x8 pack8(const f32x16& a, int o) {
  u32x4 r = {pk(a[o], a[o + 1]), pk(a[o + 2], a[o + 3]), pk(a[o + 4], a[o + 5]), pk(a[o + 6], a[o + 7])};
  return __builtin_bit_cast(bf16x8, r);
}
DI unsigned phi_pk(float a, float b, f32x2& zsum) {
  const f32x2 t = {a, b};
  const f32x2 u = t * (f32x2){LOG2E, LOG2E}, w = t + (f32x2){1.f, 1.f};
  const float e0 = __builtin_amdgcn_fmed3f(__builtin_amdgcn_exp2f(u[0]), 0.f, 1.f), e1 = __builtin_amdgcn_fmed3f(__builtin_amdgcn_exp2f(u[1]), 0.f, 1.f);
  const f32x2 r = (f32x2){__builtin_fmaxf(w[0], e0), __builtin_fmaxf(w[1], e1)} + (f32x2){1e-6f, 1e-6f};
  zsum += r;
  return pk(r[0], r[1]);
}
DI float opq(float x) { asm("" : "+v"(x)); return x; }
DI unsigned phi_pk1(float a, float b, float& z0, float& z1) {
  const float e0 = __builtin_amdgcn_fmed3f(__builtin_amdgcn_exp2f(a * LOG2E), 0.f, 1.f), e1 = __builtin_amdgcn_fmed3f(__builtin_amdgcn_exp2f(b * LOG2E), 0.f, 1.f);
  const float r0 = opq(__builtin_fmaxf(a + 1.f, e0) + 1e-6f), r1 = __builtin_fmaxf(b + 1.f, e1) + 1e-6f;
  z0 = opq(z0 + r0); z1 += r1;
  return pk(r0, r1);
}
DI bf16x8 phi8(const f32x16& a, int o) {
  f32x2 unused = {};
  u32x4 r = {phi_pk(a[o], a[o + 1], unused), phi_pk(a[o + 2], a[o + 3], unused), phi_pk(a[o + 4], a[o + 5], unused), phi_pk(a[o + 6], a[o + 7], unused)};
  return __builtin_bit_cast(bf16x8, r);
}
DI void dma16(unsigned lds, unsigned voff, rsrc_t rs, unsigned soff) {
  asm volatile("s_mov_b32 m0, %0\n\ts_nop 0\n\tbuffer_load_dwordx4 %1, %2, %3 offen lds" ::"s"(lds), "v"(voff), "s"(rs), "s"(soff) : "memory");
}
DI void dma4(unsigned lds, unsigned voff, rsrc_t rs) {
  asm volatile("s_mov_b32 m0, %0\n\ts_nop 0\n\tbuffer_load_dword %1, %2, 0 offen sc1 lds" ::"s"(lds), "v"(voff), "s"(rs) : "memory");
}
DI void touch(unsigned lds, const char* p) {
  asm volatile("s_mov_b32 m0, %0\n\ts_nop 0\n\tglobal_load_lds_dword %1, off" ::"s"(lds), "v"(p) : "memory");
}
DI u64 lds_tag(lptr p) {
  const u32x2 v = *(const LAS u32x2*)p;
  return (((u64)(unsigned)__builtin_amdgcn_readfirstlane(v[1]) << 32) | (unsigned)__builtin_amdgcn_readfirstlane(v[0])) + 1;
}
DI bf16x8 lds_frag(lptr p) { return *(const LAS bf16x8*)p; }
DI bf16x8 tr_frag(lptr p, int o0, int o1) {
  const s16x4 lo = __builtin_bit_cast(s16x4, __builtin_amdgcn_ds_read_tr16_b64_v4i16((LAS s16x4*)(p + o0)));
  const s16x4 hi = __builtin_bit_cast(s16x4, __builtin_amdgcn_ds_read_tr16_b64_v4i16((LAS s16x4*)(p + o1)));
  return __builtin_bit_cast(bf16x8, __builtin_shufflevector(lo, hi, 0, 1, 2, 3, 4, 5, 6, 7));
}
DI float sel(bool c, float a, float b) { asm("" : "+v"(a), "+v"(b)); return c ? a : b; }
#define ACQ 0
constexpr int LD_AUX = ACQ ? 0 : 16;
DI void acquire() { if (ACQ) __builtin_amdgcn_fence(__ATOMIC_ACQUIRE, "agent"); }
DI void flag_store(char* ws, unsigned off, u64 v) { __hip_atomic_store((gu64*)(ws + off), v, __ATOMIC_RELAXED, __HIP_MEMORY_SCOPE_AGENT); }
DI void poll_flags(char* ws, unsigned off0, unsigned stride, int n, u64 tag, int lane) {
  gu64* p = (gu64*)(ws + off0 + (unsigned)lane * stride);
#pragma unroll 1
  for (unsigned spins = 0; spins < SPIN_CAP; ++spins) {
    u64 v = tag;
    if (lane < n) v = __hip_atomic_load(p, __ATOMIC_RELAXED, __HIP_MEMORY_SCOPE_AGENT);
    if (__builtin_amdgcn_ballot_w64(v != tag) == 0) break;
    __builtin_amdgcn_s_sleep(1);
  }
  __builtin_amdgcn_fence(__ATOMIC_ACQUIRE, "wavefront");
  asm volatile("" ::: "memory");
  acquire();
}
DI void lds_wait(lptr p, unsigned want) {
#pragma unroll 1
  for (unsigned spins = 0; spins < SPIN_CAP; ++spins) {
    if ((unsigned)__builtin_amdgcn_readfirstlane(__hip_atomic_load((LAS unsigned*)p, __ATOMIC_RELAXED, __HIP_MEMORY_SCOPE_WORKGROUP)) == want) break;
    __builtin_amdgcn_s_sleep(2);
  }
  asm volatile("" ::: "memory");
}
DI float sum_lanes_mod4(float x) {
  x += as_f(__builtin_amdgcn_update_dpp(0u, as_u(x), 0x124, 0xf, 0xf, false));
  x += as_f(__builtin_amdgcn_update_dpp(0u, as_u(x), 0x128, 0xf, 0xf, false));
  auto a = __builtin_amdgcn_permlane16_swap(as_u(x), as_u(x), false, false);
  x = as_f(a[0]) + as_f(a[1]);
  auto b = __builtin_amdgcn_permlane32_swap(as_u(x), as_u(x), false, false);
  return as_f(b[0]) + as_f(b[1]);
}
DI float swap_max(float m) {
  auto r = __builtin_amdgcn_permlane32_swap(as_u(m), as_u(m), false, false);
  return __builtin_fmaxf(as_f(r[0]), as_f(r[1]));
}

DI void read_wo(bf16x8 (&wo)[32], lptr L, int kq, int lane) {
  const int h = lane >> 5, q4 = (lane & 15) >> 2, p4 = lane & 3, nsub = (lane >> 4) & 1;
  const lptr base = L + (kq + 1) * SLOT + (4 * h + q4) * 128 + 8 * (p4 & 1);
  const int sw = ((q4 >> 1) & 1) << 2;
  const lptr b0 = base + ((2 * nsub + (p4 >> 1)) ^ sw) * 16, b1 = base + ((4 + 2 * nsub + (p4 >> 1)) ^ sw) * 16;
#pragma unroll
  for (int i = 0; i < 16; ++i) {
    wo[2 * i] = tr_frag(b0, i * 2048, i * 2048 + 1024);
    wo[2 * i + 1] = tr_frag(b1, i * 2048, i * 2048 + 1024);
  }
}

template <int BR>
DI void phase1(char* smem_generic, const rsrc_t rsX, const rsrc_t rsW, const rsrc_t rsWoA, const rsrc_t rsWoB, const rsrc_t rsG, const rsrc_t rsWS, char* ws,
               const int rt, const int quad, const int wid, const int lane, const unsigned vX0, const unsigned vX1, const unsigned vW,
               const unsigned vWo, unsigned& vts, u64& tagO, bf16x8 (&wo)[32], const char* outp) {
  const lptr L = (lptr)smem_generic;
  const unsigned lds0 = (unsigned)(uintptr_t)L;
  const int c = wid >> 2, rq = wid & 3, p = rq & 1, sp = rq >> 1;
  const int i32 = lane & 31, h = lane >> 5;
  const unsigned pieceX = lds0 + wid * 2048, pieceW = lds0 + wid * 1024;
  const unsigned rot = ROT_OF(quad, rt);

  auto piece = [&](int f, int i) LAM {
    const unsigned sl = (unsigned)slot_of(f) * SLOT;
    if (f < 8) {
      const unsigned kc = (f + rot) & 7;
      if (i == 0) dma16(pieceX + sl, vX0, rsX, kc * 128);
      else if (i == 1) dma16(pieceX + sl + 1024, vX1, rsX, kc * 128);
      else dma16(pieceW + sl + 16384 + (i - 2) * 8192, vW, rsW, kc * 196608 + (i - 1) * 1024);
    } else {
      dma16(pieceW + sl + i * 8192, vW, rsW, (((f - 8) * 4 + i + rot) & 7) * 196608);
    }
  };
  auto wo_piece = [&](int which, unsigned q) LAM { dma16(lds0 + (which ? 3 : 1) * SLOT + q * 1024, vWo, which ? rsWoB : rsWoA, q * 8192); };

  const int xs = h ^ ((i32 >> 1) & 7);
  const int q4 = (lane & 15) >> 2, p4 = lane & 3, nsub = (lane >> 4) & 1;
  const int wchunk = (4 * c + 2 * nsub + (p4 >> 1)) ^ (((q4 >> 1) & 1) << 2);
  lptr xb[3][4], wb[3];
#pragma unroll
  for (int t = 0; t < 4; ++t) {
    xb[0][t] = L + (32 * rq + i32) * 128 + ((xs ^ (2 * t)) << 4);
    xb[1][t] = xb[0][t] + 2 * SLOT;
    xb[2][t] = xb[0][t] + 4 * SLOT;
  }
  wb[0] = L + (8 * h + q4) * 128 + wchunk * 16 + 8 * (p4 & 1);
  wb[1] = wb[0] + 2 * SLOT;
  wb[2] = wb[0] + 4 * SLOT;
#pragma unroll
  for (int t = 0; t < 4; ++t) { asm("" : "+v"(xb[0][t])); asm("" : "+v"(xb[1][t])); asm("" : "+v"(xb[2][t])); }
  asm("" : "+v"(wb[0])); asm("" : "+v"(wb[1])); asm("" : "+v"(wb[2]));

  bf16x8 xf[32], wk[3], wv[3];
  f32x16 accK = {}, accV = {};
  constexpr int VLAG = BR == 0 ? 8 : 0;
  auto rdv = [&](int s, int buf) LAM {
    const int sl = slot_of(s >> 2), o = (sl & 1) * SLOT + 16384 + 8192 + (s & 3) * 2048;
    wv[buf] = tr_frag(wb[sl >> 1], o, o + 512);
  };
  auto rd1 = [&](int s) LAM {
    if (s < 32) {
      const int sl = slot_of(s >> 2), t = s & 3;
      xf[s] = lds_frag(sl < 4 ? xb[sl >> 1][t] + (sl & 1) * SLOT : xb[2][t]);
      const int o = (sl & 1) * SLOT + 16384 + t * 2048;
      wk[s % 3] = tr_frag(wb[sl >> 1], o, o + 512);
      if (s < 32 - VLAG) rdv(s, s % 3);
    } else {
      rdv(s - VLAG, s % 3);
    }
  };
  WAIT_BAR(4);
  STAMP(15);
  rd1(0); rd1(1);
  SBAR();
  unsigned kpk[8];
  float zs0 = 0.f, zs1 = 0.f;
#pragma unroll
  for (int s = 0; s < 32 + VLAG; ++s) {
    const int cc = s >> 2;
    if ((s & 3) == 2 && cc < 7) {
      if (s == 2) WAIT_BAR_NL(4); else WAIT_BAR_NL(8);
      if (cc == 6 && wid == 0) {
        dma16(lds0 + L_GW, (lane * 16 + rot * 128) & 1023, rsG, 0);
        const unsigned e0 = WS_EO + rt * 128;
        const unsigned e1 = BR == 0 ? WS_EKV + quad * 128 : WS_EH + (rt * 8 + quad) * 128;
        const unsigned e2 = BR == 0 ? e1 : WS_EH + ((rt > 0 ? rt - 1 : 0) * 8 + quad) * 128;
        const unsigned eo = (lane < 2 ? e0 : lane < 4 ? e1 : e2) + (lane & 1) * 4;
        if (lane < 6) dma4(lds0 + L_EP, eo, rsWS);
        if (lane < 3) *(LAS unsigned*)(L + L_CNT + lane * 4) = 0;
      }
    }
    if (s < 2) { piece(2, 2 * s); piece(2, 2 * s + 1); }
    else if (s < 6) { piece(3 + ((s - 2) >> 1), 2 * (s & 1)); piece(3 + ((s - 2) >> 1), 2 * (s & 1) + 1); }
    else if (((s - 2) >> 2) + 4 < 10) piece(((s - 2) >> 2) + 4, (s - 2) & 3);
    if (s + 2 < 32 + VLAG) rd1(s + 2);
    if (s < 32) accK = BR == 0 ? MFMA(xf[s], wk[s % 3], accK) : MFMA(wk[s % 3], xf[s], accK);
    if (s < 32 - VLAG) accV = MFMA(xf[s], wv[s % 3], accV);
    if (s >= 32) {
      accV = MFMA(xf[s - VLAG], wv[s % 3], accV);
      kpk[s - 32] = phi_pk1(accK[2 * (s - 32)], accK[2 * (s - 32) + 1], zs0, zs1);
    }
    SBAR();
  }
  STAMP(2);
  bf16x8 Kf[2], Vf[2];
  if (BR == 0) {
    Kf[0] = __builtin_bit_cast(bf16x8, (u32x4){kpk[0], kpk[1], kpk[2], kpk[3]});
    Kf[1] = __builtin_bit_cast(bf16x8, (u32x4){kpk[4], kpk[5], kpk[6], kpk[7]});
  } else { Kf[0] = pack8(accK, 0); Kf[1] = pack8(accK, 8); }
  Vf[0] = pack8(accV, 0); Vf[1] = pack8(accV, 8);
  if (BR == 0) {
    f32x16 kv = {};
    kv = MFMA(Kf[0], Vf[0], kv);
    kv = MFMA(Kf[1], Vf[1], kv);
    const lptr pp = L + L_PART + wid * 2176;
    {
      const float zh = zs0 + zs1;
      auto sw = __builtin_amdgcn_permlane32_swap(as_u(zh), as_u(zh), false, false);
      if (h == 0) *(LAS float*)(pp + 2048 + i32 * 4) = as_f(sw[0]) + as_f(sw[1]);
    }
    const bool lo = i32 < 16;
    f32x4 u0, u1;
#pragma unroll
    for (int j = 0; j < 4; ++j) { u0[j] = sel(lo, kv[j], kv[8 + j]); u1[j] = sel(lo, kv[4 + j], kv[12 + j]); }
    *(LAS f32x4*)(pp + lane * 16) = u0;
    *(LAS f32x4*)(pp + 1024 + lane * 16) = u1;
  } else {
    if (sp == 0) {
      const lptr ex = L + L_PART + (c * 2 + p) * 1024;
      if (i32 >= 24) {
        *(LAS bf16x8*)(ex + h * 128 + (i32 - 24) * 16) = Kf[0];
        *(LAS bf16x8*)(ex + 256 + h * 128 + (i32 - 24) * 16) = Kf[1];
      }
      const u32x4 v1 = __builtin_bit_cast(u32x4, Vf[1]);
      *(LAS u32x2*)(ex + 512 + lane * 8) = (u32x2){v1[2], v1[3]};
    }
  }
  WAIT_BAR(0);
  STAMP(3);
  if (BR == 0) {
    if (wid == 3) {
      __builtin_amdgcn_s_setprio(3);
      f32x4 pa[5][4];
#pragma unroll
      for (int it = 0; it < 5; ++it) {
        const int u = it < 4 ? it * 64 + lane : 256 + (lane & 15);
        const int cc = it < 4 ? (it >> 1) : ((lane >> 3) & 1);
        const int o = it < 4 ? (u & 127) * 16 : 2048 + (lane & 7) * 16;
        const lptr s = L + L_PART + cc * 4 * 2176 + o;
#pragma unroll
        for (int r = 0; r < 4; ++r) pa[it][r] = *(LAS f32x4*)(s + r * 2176);
      }
      SBAR();
#pragma unroll
      for (int it = 0; it < 5; ++it) {
        f32x4 a;
#pragma unroll
        for (int j = 0; j < 4; ++j) a[j] = opq(opq(pa[it][0][j] + pa[it][1][j]) + opq(pa[it][2][j] + pa[it][3][j]));
        const unsigned go = WS_KVP + (quad * 16 + rt) * KVP_BYTES + (it < 4 ? it * 64 + lane : 256 + lane) * 16;
        if (it < 4 || lane < 16) __builtin_amdgcn_raw_buffer_store_b128(__builtin_bit_cast(u32x4, a), rsWS, go, 0, 16);
      }
      __builtin_amdgcn_s_setprio(0);
    }
  } else {
    if (sp == 1 && rt < 15) {
      const unsigned ex = WS_HALO + ((rt * 8 + quad) * 4 + c * 2 + p) * 1024;
      if (i32 >= 24) {
        __builtin_amdgcn_raw_buffer_store_b128(__builtin_bit_cast(u32x4, Kf[0]), rsWS, ex + h * 128 + (i32 - 24) * 16, 0, 16);
        __builtin_amdgcn_raw_buffer_store_b128(__builtin_bit_cast(u32x4, Kf[1]), rsWS, ex + 256 + h * 128 + (i32 - 24) * 16, 0, 16);
      }
      const u32x4 v1 = __builtin_bit_cast(u32x4, Vf[1]);
      __builtin_amdgcn_raw_buffer_store_b64((u32x2){v1[2], v1[3]}, rsWS, ex + 512 + lane * 8, 0, 16);
    }
  }
  SBAR();
  tagO = lds_tag(L + L_EP);
  const u64 tagP = lds_tag(L + L_EP + 8), tagG = lds_tag(L + L_EP + 16);
  if (BR == 0 && c == 1) __builtin_amdgcn_s_setprio(1);
  f32x16 accQ = {};
  {
    bf16x8 wq[5];
    auto rd2 = [&](int s) LAM {
      const lptr w = s < 16 ? wb[1] : wb[2];
      const int o = (s < 16 ? SLOT : -16 * 2048) + s * 2048;
      wq[s % 5] = tr_frag(w, o, o + 512);
    };
    rd2(0); rd2(1); rd2(2); rd2(3);
    SBAR();
#pragma unroll
    for (int s = 0; s < 32; ++s) {
      if (s + 4 < 32) rd2(s + 4);
      accQ = MFMA(wq[s % 5], xf[s], accQ);
      if (s >= 12 && s < 20) wo_piece(0, wid * 8 + (s - 12));
      if (s == PUBS) {
        if (BR == 0 ? (wid == 3) : (sp == 1 && rt < 15)) {
          WAIT_VM(0);
          if (lane == 0) flag_store(ws, BR == 0 ? WS_FKV + (quad * 16 + rt) * 128 : WS_FH + ((rt * 8 + quad) * 4 + c * 2 + p) * 64, tagP);
          STAMP(14);
        }
      }
      SBAR();
    }
  }
  bf16x8 Qf[2];
  if (BR == 0) { Qf[0] = phi8(accQ, 0); Qf[1] = phi8(accQ, 8); }
  else { Qf[0] = pack8(accQ, 0); Qf[1] = pack8(accQ, 8); }
  STAMP(4);
  asm volatile("s_waitcnt vmcnt(0) lgkmcnt(0)" ::: "memory");
  if (lane == 0) __hip_atomic_fetch_add((LAS unsigned*)(L + L_CNT), 1u, __ATOMIC_RELAXED, __HIP_MEMORY_SCOPE_WORKGROUP);
  STAMP(16);
  auto gate = [&](auto pre) LAM {
    f32x16 ga = {};
    bf16x8 gf[4];
    const lptr gp = L + L_GW + h * 16;
#pragma unroll
    for (int t = 0; t < 3; ++t) gf[t] = lds_frag(gp + t * 32);
    SBAR();
#pragma unroll
    for (int t = 0; t < 32; ++t) {
      pre(t);
      if (t + 3 < 32) gf[(t + 3) % 4] = lds_frag(gp + (t + 3) * 32);
      ga = MFMA(gf[t % 4], xf[t], ga);
      SBAR();
    }
    const float g = __builtin_amdgcn_rcpf(1.f + __builtin_amdgcn_exp2f(-LOG2E * ga[0]));
    if (h == 0) *(LAS float*)(L + L_G + (p * 32 + i32) * 4) = g;
  };
  const unsigned ofrag = WS_O + ((((rt * 2 + sp) * 2 + p) * 64) + BR * 32 + quad * 4 + 2 * c) * 1024 + lane * 16;
  if (BR == 0) {
    if (wid < 2) { gate([&](int) LAM {}); STAMP(20); }
    if (c == 0) {
      lds_wait(L + L_CNT, 8);
      STAMP(21);
      read_wo(wo, L, wid >> 1, lane);
      STAMP(22);
      lds_wait(L + L_UIS, 4);
      STAMP(17);
#pragma unroll
      for (int i = 0; i < 16; ++i) wo_piece(1, wid * 16 + i);
      STAMP(18);
    }
    if (c == 1) {
      poll_flags(ws, WS_FKV + quad * 16 * 128, 128, 16, tagG, lane);
      STAMP(5);
      const unsigned u = rq * 64 + lane;
      f32x4 a[16];
#pragma unroll
      for (int r = 0; r < 16; ++r)
        a[r] = __builtin_bit_cast(f32x4, __builtin_amdgcn_raw_buffer_load_b128(rsWS, WS_KVP + quad * 16 * KVP_BYTES + u * 16, r * KVP_BYTES, LD_AUX));
      f32x4 zv = __builtin_bit_cast(f32x4, __builtin_amdgcn_raw_buffer_load_b128(rsWS, WS_KVP + (quad * 16 + (lane >> 2)) * KVP_BYTES + (256 + rq * 4 + (lane & 3)) * 16, 0, LD_AUX));
      SBAR();
      if (lane == 0) __hip_atomic_fetch_add((LAS unsigned*)(L + L_UIS), 1u, __ATOMIC_RELAXED, __HIP_MEMORY_SCOPE_WORKGROUP);
      SBAR();
#pragma unroll
      for (int s = 1; s < 16; s *= 2)
#pragma unroll
        for (int r = 0; r < 16; r += 2 * s) a[r] += a[r + s];
      *(LAS f32x4*)(L + L_TOT + u * 16) = a[0];
#pragma unroll
      for (int j = 0; j < 4; ++j) zv[j] = sum_lanes_mod4(zv[j]);
      if (lane < 4) *(LAS f32x4*)(L + L_TOT + (256 + rq * 4 + lane) * 16) = zv;
      STAMP(23);
    }
    LBAR();
    STAMP(6);
    const f32x4 u0 = *(LAS f32x4*)(L + L_TOT + (c * 128 + lane) * 16), u1 = *(LAS f32x4*)(L + L_TOT + (c * 128 + 64 + lane) * 16);
    const lptr zp = L + L_TOT + 4096 + c * 128 + h * 16;
    const f32x4 z0 = *(LAS f32x4*)zp, z1 = *(LAS f32x4*)(zp + 32), z2 = *(LAS f32x4*)(zp + 64), z3 = *(LAS f32x4*)(zp + 96);
    const bool lo = i32 < 16;
    const u32x4 pu = {pk(u0[0], u0[1]), pk(u0[2], u0[3]), pk(u1[0], u1[1]), pk(u1[2], u1[3])};
    const u32x4 pz0 = {pk(z0[0], z0[1]), pk(z0[2], z0[3]), pk(z1[0], z1[1]), pk(z1[2], z1[3])};
    const u32x4 pz1 = {pk(z2[0], z2[1]), pk(z2[2], z2[3]), pk(z3[0], z3[1]), pk(z3[2], z3[3])};
    u32x4 a0, a1;
#pragma unroll
    for (int j = 0; j < 4; ++j) { a0[j] = lo ? pu[j] : pz0[j]; a1[j] = lo ? pz1[j] : pu[j]; }
    const f32x16 zero = {};
    const f32x16 d0 = MFMA(__builtin_bit_cast(bf16x8, a0), Qf[0], zero);
    const f32x16 d1 = MFMA(__builtin_bit_cast(bf16x8, a1), Qf[1], zero);
    const float s0 = __builtin_amdgcn_rcpf(d0[8] + 1e-6f), s1 = __builtin_amdgcn_rcpf(d1[0] + 1e-6f);
    const u32x4 o0 = {pk(d0[0] * s0, d0[1] * s0), pk(d0[2] * s0, d0[3] * s0), pk(d0[4] * s0, d0[5] * s0), pk(d0[6] * s0, d0[7] * s0)};
    const u32x4 o1 = {pk(d1[8] * s1, d1[9] * s1), pk(d1[10] * s1, d1[11] * s1), pk(d1[12] * s1, d1[13] * s1), pk(d1[14] * s1, d1[15] * s1)};
    __builtin_amdgcn_raw_buffer_store_b128(o0, rsWS, ofrag, 0, 16);
    __builtin_amdgcn_raw_buffer_store_b128(o1, rsWS, ofrag + 1024, 0, 16);
    if (rt == 0 && wid == 4 && lane == 0) flag_store(ws, WS_EKV + quad * 128, tagG);
  } else {
    bf16x8 Kp[2];
    u32x2 vp;
    if (sp == 1) {
      const lptr ex = L + L_PART + (c * 2 + p) * 1024;
      Kp[0] = lds_frag(ex + h * 128 + (i32 & 7) * 16);
      Kp[1] = lds_frag(ex + 256 + h * 128 + (i32 & 7) * 16);
      vp = *(LAS u32x2*)(ex + 512 + lane * 8);
    } else if (rt > 0) {
      poll_flags(ws, WS_FH + (((rt - 1) * 8 + quad) * 4 + c * 2 + p) * 64, 64, 1, tagG, lane);
      const unsigned ex = WS_HALO + (((rt - 1) * 8 + quad) * 4 + c * 2 + p) * 1024;
      Kp[0] = __builtin_bit_cast(bf16x8, __builtin_amdgcn_raw_buffer_load_b128(rsWS, ex + h * 128 + (i32 & 7) * 16, 0, LD_AUX));
      Kp[1] = __builtin_bit_cast(bf16x8, __builtin_amdgcn_raw_buffer_load_b128(rsWS, ex + 256 + h * 128 + (i32 & 7) * 16, 0, LD_AUX));
      vp = __builtin_amdgcn_raw_buffer_load_b64(rsWS, ex + 512 + lane * 8, 0, LD_AUX);
      if (wid == 0 && lane == 0) flag_store(ws, WS_EH + ((rt - 1) * 8 + quad) * 128, tagG);
    } else {
      Kp[0] = Kf[0]; Kp[1] = Kf[1]; vp = (u32x2){0u, 0u};
    }
    STAMP(5);
    STAMP(6);
    const bool noprev = (rt == 0 && sp == 0);
    f32x16 bias;
#pragma unroll
    for (int r = 0; r < 16; ++r) bias[r] = ((unsigned)(i32 - ((r & 3) + 8 * (r >> 2) + 4 * h)) <= 7u) ? 0.f : -1e30f;
    float bprev[4];
#pragma unroll
    for (int j = 0; j < 4; ++j) bprev[j] = (!noprev && (24 + 4 * h + j >= i32 + 25)) ? 0.f : -1e30f;
    constexpr float C1 = 0.25f * LOG2E;
    const unsigned ONE2 = 0x3F803F80u;
    const f32x16 zero = {};
#pragma unroll
    for (int hh = 0; hh < 2; ++hh) {
      f32x16 sc = MFMA(Kf[hh], Qf[hh], bias);
      const f32x16 sv = MFMA(Kp[hh], Qf[hh], zero);
      float sp4[4];
#pragma unroll
      for (int j = 0; j < 4; ++j) sp4[j] = sv[12 + j] + bprev[j];
      float m = __builtin_fmaxf(__builtin_fmaxf(sp4[0], sp4[1]), __builtin_fmaxf(sp4[2], sp4[3]));
#pragma unroll
      for (int r = 0; r < 16; r += 2) m = __builtin_fmaxf(__builtin_fmaxf(m, sc[r]), sc[r + 1]);
      m = swap_max(m);
      const float nm = -m * C1;
#pragma unroll
      for (int r = 0; r < 16; ++r) sc[r] = __builtin_amdgcn_exp2f(__builtin_fmaf(sc[r], C1, nm));
#pragma unroll
      for (int j = 0; j < 4; ++j) sp4[j] = __builtin_amdgcn_exp2f(__builtin_fmaf(sp4[j], C1, nm));
      const bf16x8 P0 = pack8(sc, 0), P1 = pack8(sc, 8);
      const u32x4 ppv = {0u, 0u, pk(sp4[0], sp4[1]), pk(sp4[2], sp4[3])};
      const bool mine = (i32 < 16) == (hh == 0);
      const u32x4 v0 = __builtin_bit_cast(u32x4, Vf[0]), v1 = __builtin_bit_cast(u32x4, Vf[1]);
      u32x4 a0, a1, ap;
#pragma unroll
      for (int j = 0; j < 4; ++j) { a0[j] = mine ? v0[j] : ONE2; a1[j] = mine ? v1[j] : ONE2; }
      ap = (u32x4){0u, 0u, mine ? vp[0] : ONE2, mine ? vp[1] : ONE2};
      f32x16 o = MFMA(__builtin_bit_cast(bf16x8, a0), P0, zero);
      o = MFMA(__builtin_bit_cast(bf16x8, a1), P1, o);
      o = MFMA(__builtin_bit_cast(bf16x8, ap), __builtin_bit_cast(bf16x8, ppv), o);
      const int nb = hh == 0 ? 0 : 8, db = hh == 0 ? 8 : 0;
      const float s = __builtin_amdgcn_rcpf(o[db]);
      const u32x4 ov = {pk(o[nb] * s, o[nb + 1] * s), pk(o[nb + 2] * s, o[nb + 3] * s), pk(o[nb + 4] * s, o[nb + 5] * s), pk(o[nb + 6] * s, o[nb + 7] * s)};
      __builtin_amdgcn_raw_buffer_store_b128(ov, rsWS, ofrag + hh * 1024, 0, 16);
    }
  }
  STAMP(7);
  WAIT_VM(0);
  if (lane == 0) flag_store(ws, WS_FO + (((((rt * 2 + sp) * 2 + p) * 2 + BR) * 8 + quad) * 2 + c) * 64, tagO);
  if (BR == 0 && c == 0 && lane == 0) __hip_atomic_fetch_add((LAS unsigned*)(L + L_WOB), 1u, __ATOMIC_RELAXED, __HIP_MEMORY_SCOPE_WORKGROUP);
  STAMP(8);
  __builtin_amdgcn_s_setprio(0);
  if (BR == 1 && c == 1) {
    lds_wait(L + L_CNT, 8);
#pragma unroll
    for (int i = 0; i < 16; ++i) wo_piece(1, rq * 16 + i);
    STAMP(18);
  }
  if (BR == 1 && wid >= 6) gate([&](int) LAM {});
}

__global__ __launch_bounds__(512, 2) void gated_mix_kernel(const __hip_bfloat16* __restrict__ x, const __hip_bfloat16* __restrict__ wA,
                                                           const __hip_bfloat16* __restrict__ woA, const __hip_bfloat16* __restrict__ wB,
                                                           const __hip_bfloat16* __restrict__ woB, const __hip_bfloat16* __restrict__ gw,
                                                           __hip_bfloat16* __restrict__ out, char* ws) {
  __shared__ __attribute__((aligned(1024))) char smem[LDS_BYTES];
  const int tid = threadIdx.x, lane = tid & 63, wid = __builtin_amdgcn_readfirstlane(tid >> 6);
  const int b = blockIdx.x, xcd = b & 7, loc = b >> 3;
  const int br = xcd & 1, rt = (xcd >> 1) * 4 + (loc >> 3), quad = loc & 7;
  unsigned vts = 0;
  STAMP(0);
  const rsrc_t rsX = __builtin_amdgcn_make_buffer_rsrc((void*)x, 0, 2048 * 512 * 2, 0x00020000);
  const rsrc_t rsW = __builtin_amdgcn_make_buffer_rsrc((void*)(br ? wB : wA), 0, 512 * 1536 * 2, 0x00020000);
  const rsrc_t rsWoA = __builtin_amdgcn_make_buffer_rsrc((void*)woA, 0, 512 * 512 * 2, 0x00020000);
  const rsrc_t rsWoB = __builtin_amdgcn_make_buffer_rsrc((void*)woB, 0, 512 * 512 * 2, 0x00020000);
  const rsrc_t rsG = __builtin_amdgcn_make_buffer_rsrc((void*)gw, 0, 1024, 0x00020000);
  const rsrc_t rsWS = __builtin_amdgcn_make_buffer_rsrc((void*)ws, 0, WS_USED, 0x00020000);
  const lptr L = (lptr)smem;
  const unsigned lds0 = (unsigned)(uintptr_t)L;

  const int l8 = lane >> 3, c8 = lane & 7;
  const int xrow = rt * 128 + 64 * (wid >> 2) + ((wid >> 1) & 1) + 2 * (16 * (wid & 1) + l8);
  const unsigned vX0 = xrow * 1024 + ((c8 ^ (lane >> 4)) << 4);
  const unsigned vX1 = (xrow + 16) * 1024 + ((c8 ^ (4 + (lane >> 4))) << 4);
  const int wsw = (c8 ^ (((lane >> 4) & 1) << 2)) << 4;
  const unsigned vW = (8 * wid + l8) * 3072 + quad * 128 + wsw;
  const unsigned vWo = l8 * 1024 + quad * 128 + wsw;

  asm volatile("s_nop 4" ::: "memory");
#pragma unroll
  for (int f = 0; f < 2; ++f) {
    const unsigned sl = lds0 + f * SLOT;
    const unsigned kc = (f + ROT_OF(quad, rt)) & 7;
    dma16(sl + wid * 2048, vX0, rsX, kc * 128);
    dma16(sl + wid * 2048 + 1024, vX1, rsX, kc * 128);
    dma16(sl + 16384 + wid * 1024, vW, rsW, kc * 196608 + 1024);
    dma16(sl + 24576 + wid * 1024, vW, rsW, kc * 196608 + 2048);
    if (f < 1) asm volatile("s_barrier" ::: "memory");
  }
  STAMP(1);
  u64 tagO;
  bf16x8 wo[32];
  if (br == 0) phase1<0>(smem, rsX, rsW, rsWoA, rsWoB, rsG, rsWS, ws, rt, quad, wid, lane, vX0, vX1, vW, vWo, vts, tagO, wo, (const char*)out);
  else phase1<1>(smem, rsX, rsW, rsWoA, rsWoB, rsG, rsWS, ws, rt, quad, wid, lane, vX0, vX1, vW, vWo, vts, tagO, wo, (const char*)out);

  const int pt = wid & 1, kq = (wid >> 2) * 2 + ((wid >> 1) & 1);
  const int i32 = lane & 31, h = lane >> 5;
  const unsigned fwait = WS_FO + (((((rt * 2 + br) * 2 + pt) * 2 + (kq >> 1)) * 8 + (kq & 1) * 4) * 2) * 64;
  const unsigned ob = WS_O + ((((rt * 2 + br) * 2 + pt) * 64) + kq * 16) * 1024 + lane * 16;
  bf16x8 of[16];
  auto load_o = [&]() LAM {
#pragma unroll
    for (int i = 0; i < 16; ++i) of[i] = __builtin_bit_cast(bf16x8, __builtin_amdgcn_raw_buffer_load_b128(rsWS, ob, i * 1024, LD_AUX));
  };
  bool ready = false;
  if (!(br == 0 && kq < 2)) {
    u64 v0 = tagO;
    if (lane < 8) v0 = __hip_atomic_load((gu64*)(ws + fwait + lane * 64), __ATOMIC_RELAXED, __HIP_MEMORY_SCOPE_AGENT);
    ready = __builtin_amdgcn_ballot_w64(v0 != tagO) == 0;
    WAIT_VM(0);
    if (br == 1 && kq >= 2 && lane == 0) __hip_atomic_fetch_add((LAS unsigned*)(L + L_WOB), 1u, __ATOMIC_RELAXED, __HIP_MEMORY_SCOPE_WORKGROUP);
    STAMP(26);
    if (ready) load_o();
    if (kq >= 2) lds_wait(L + L_WOB, 4); else lds_wait(L + L_CNT, 8);
    STAMP(19);
    read_wo(wo, L, kq, lane);
  }
  STAMP(9);
  if (!ready) {
    poll_flags(ws, fwait, 64, 8, tagO, lane);
    load_o();
  }
  STAMP(10);
  SBAR();
  f32x16 a0 = {}, a1 = {};
#pragma unroll
  for (int i = 0; i < 16; ++i) { a0 = MFMA(wo[2 * i], of[i], a0); a1 = MFMA(wo[2 * i + 1], of[i], a1); }
  STAMP(11);
  LBAR();
  STAMP(24);
  {
    const lptr pr = L + L_RED + (kq * 64 + pt * 32 + i32) * PROW + h * 16;
#pragma unroll
    for (int g4 = 0; g4 < 4; ++g4) {
      *(LAS f32x4*)(pr + g4 * 32) = (f32x4){a0[4 * g4], a0[4 * g4 + 1], a0[4 * g4 + 2], a0[4 * g4 + 3]};
      *(LAS f32x4*)(pr + 128 + g4 * 32) = (f32x4){a1[4 * g4], a1[4 * g4 + 1], a1[4 * g4 + 2], a1[4 * g4 + 3]};
    }
  }
  LBAR();
  STAMP(13);
  {
    const int pos = tid >> 3, cg = tid & 7;
    const lptr pr = L + L_RED + pos * PROW + cg * 32;
    const float g = *(LAS float*)(L + L_G + pos * 4), gb = 1.f - g;
    const f32x4 s0 = (*(LAS f32x4*)pr + *(LAS f32x4*)(pr + 64 * PROW)) * g + (*(LAS f32x4*)(pr + 128 * PROW) + *(LAS f32x4*)(pr + 192 * PROW)) * gb;
    const f32x4 s1 = (*(LAS f32x4*)(pr + 16) + *(LAS f32x4*)(pr + 64 * PROW + 16)) * g + (*(LAS f32x4*)(pr + 128 * PROW + 16) + *(LAS f32x4*)(pr + 192 * PROW + 16)) * gb;
    const int row = rt * 128 + 64 * br + 2 * (pos & 31) + (pos >> 5);
    const u32x4 ov = {pk(s0[0], s0[1]), pk(s0[2], s0[3]), pk(s1[0], s1[1]), pk(s1[2], s1[3])};
    __builtin_amdgcn_raw_buffer_store_b128(ov, __builtin_amdgcn_make_buffer_rsrc((void*)out, 0, 2048 * 1024, 0x00020000), row * 1024 + quad * 128 + cg * 16, 0, 0);
  }
  if (quad == 0 && br == 0 && tid == 0) flag_store(ws, WS_EO + rt * 128, tagO);
  STAMP(12);
  TSDUMP();
}

#if NOPK && defined(__HIP_DEVICE_COMPILE__)
#pragma clang attribute pop
#endif

extern "C" void kernel_launch(void* const* d_in, const int* in_sizes, int n_in, void* d_out, int out_size, void* d_ws, size_t ws_size,
                              hipStream_t stream) {
  gated_mix_kernel<<<256, 512, 0, stream>>>((const __hip_bfloat16*)d_in[0], (const __hip_bfloat16*)d_in[1], (const __hip_bfloat16*)d_in[2],
                                            (const __hip_bfloat16*)d_in[4], (const __hip_bfloat16*)d_in[5], (const __hip_bfloat16*)d_in[6],
                                            (__hip_bfloat16*)d_out, (char*)d_ws);
}
```

```cpp
#define DIAGTHR 20.5f
#include <hip/hip_runtime.h>
#include <hip/hip_bf16.h>
#include <stdint.h>

typedef __bf16 bf16x2 __attribute__((ext_vector_type(2)));
typedef __bf16 bf16x8 __attribute__((ext_vector_type(8)));
typedef short s16x4 __attribute__((ext_vector_type(4)));
typedef float f32x2 __attribute__((ext_vector_type(2)));
typedef float f32x4 __attribute__((ext_vector_type(4)));
typedef float f32x16 __attribute__((ext_vector_type(16)));
typedef unsigned u32x2 __attribute__((ext_vector_type(2)));
typedef unsigned u32x4 __attribute__((ext_vector_type(4)));
typedef unsigned long long u64;
#define LAS __attribute__((address_space(3)))
#define GAS __attribute__((address_space(1)))
#define DI __device__ __forceinline__
typedef LAS char* lptr;
typedef GAS u64 gu64;
typedef __amdgpu_buffer_rsrc_t rsrc_t;

constexpr unsigned WS_EO = 0;
constexpr unsigned WS_EKV = 2048;
constexpr unsigned WS_EH = 4096;
constexpr unsigned WS_FKV = 32768;
constexpr unsigned WS_FH = 65536;
constexpr unsigned WS_FO = 131072;
constexpr unsigned WS_KVP = 262144 + 65536;
constexpr unsigned KVP_BYTES = 4352;
constexpr unsigned WS_HALO = 1048576;
constexpr unsigned WS_O = 2097152;
constexpr unsigned WS_STAMP = 8u << 20;
constexpr unsigned WS_USED = 16u << 20;

constexpr int SLOT = 32768;
constexpr int L_GW = 0;
constexpr int L_G = L_GW + 1024;
constexpr int L_EP = L_GW + 1536;
constexpr int L_CNT = L_GW + 1568;
constexpr int L_WOB = L_GW + 1572;
constexpr int L_UIS = L_GW + 1576;
constexpr int L_PART = L_GW + 2048;
constexpr int L_TOT = L_PART + 8 * 2176;
constexpr int L_RED = SLOT;
constexpr int LDS_BYTES = 163840;
constexpr int PROW = 272;
#define slot_of(f) ((f) < 8 ? (f) % 5 : (f) == 8 ? 3 : 4)

constexpr float LOG2E = 1.4426950408889634f;
constexpr unsigned SPIN_CAP = 1u << 16;

#define MFMA(a, b, c) __builtin_amdgcn_mfma_f32_32x32x16_bf16((a), (b), (c), 0, 0, 0)
#define WAIT_BAR(N) asm volatile("s_waitcnt vmcnt(" #N ") lgkmcnt(0)\n\ts_barrier" ::: "memory")
#define WAIT_BAR_NL(N) asm volatile("s_waitcnt vmcnt(" #N ")\n\ts_barrier" ::: "memory")
#define WAIT_VM(N) asm volatile("s_waitcnt vmcnt(" #N ")" ::: "memory")
#define ROTM 6
#define ROT_OF(q, r) ((unsigned)(ROTM == 0 ? 0 : ROTM == 1 ? (q) : ROTM == 2 ? (q) + 2 * (r) : ROTM == 3 ? 2 * (r) : ROTM == 4 ? (r) : ROTM == 5 ? (r) + (q) : (r) + 4 * ((q) & 1)))
#define PUBS 7
#define SBAR() __builtin_amdgcn_sched_barrier(0)
#define LBAR() asm volatile("s_waitcnt lgkmcnt(0)\n\ts_barrier" ::: "memory")

#define STAMP(i) do {} while (0)
#define TSDUMP() do {} while (0)

#define NOPK 0
#if NOPK && defined(__HIP_DEVICE_COMPILE__)
#define LAM __attribute__((target("no-packed-fp32-ops"), always_inline))
#else
#define LAM __attribute__((always_inline))
#endif
#if NOPK && defined(__HIP_DEVICE_COMPILE__)
#pragma clang attribute push(__attribute__((target("no-packed-fp32-ops"))), apply_to = function)
#endif
DI float as_f(unsigned u) { return __builtin_bit_cast(float, u); }
DI unsigned as_u(float f) { return __builtin_bit_cast(unsigned, f); }
DI unsigned pk(float a, float b) { return __builtin_bit_cast(unsigned, __builtin_convertvector((f32x2){a, b}, bf16x2)); }
DI bf16x8 pack8(const f32x16& a, int o) {
  u32x4 r = {pk(a[o], a[o + 1]), pk(a[o + 2], a[o + 3]), pk(a[o + 4], a[o + 5]), pk(a[o + 6], a[o + 7])};
  return __builtin_bit_cast(bf16x8, r);
}
DI unsigned phi_pk(float a, float b, f32x2& zsum) {
  const f32x2 t = {a, b};
  const f32x2 u = t * (f32x2){LOG2E, LOG2E}, w = t + (f32x2){1.f, 1.f};
  const float e0 = __builtin_amdgcn_fmed3f(__builtin_amdgcn_exp2f(u[0]), 0.f, 1.f), e1 = __builtin_amdgcn_fmed3f(__builtin_amdgcn_exp2f(u[1]), 0.f, 1.f);
  const f32x2 r = (f32x2){__builtin_fmaxf(w[0], e0), __builtin_fmaxf(w[1], e1)} + (f32x2){1e-6f, 1e-6f};
  zsum += r;
  return pk(r[0], r[1]);
}
DI float opq(float x) { asm("" : "+v"(x)); return x; }
DI unsigned phi_pk1(float a, float b, float& z0, float& z1) {
  const float e0 = __builtin_amdgcn_fmed3f(__builtin_amdgcn_exp2f(a * LOG2E), 0.f, 1.f), e1 = __builtin_amdgcn_fmed3f(__builtin_amdgcn_exp2f(b * LOG2E), 0.f, 1.f);
  const float r0 = opq(__builtin_fmaxf(a + 1.f, e0) + 1e-6f), r1 = __builtin_fmaxf(b + 1.f, e1) + 1e-6f;
  z0 = opq(z0 + r0); z1 += r1;
  return pk(r0, r1);
}
DI bf16x8 phi8(const f32x16& a, int o) {
  f32x2 unused = {};
  u32x4 r = {phi_pk(a[o], a[o + 1], unused), phi_pk(a[o + 2], a[o + 3], unused), phi_pk(a[o + 4], a[o + 5], unused), phi_pk(a[o + 6], a[o + 7], unused)};
  return __builtin_bit_cast(bf16x8, r);
}
DI void dma16(unsigned lds, unsigned voff, rsrc_t rs, unsigned soff) {
  asm volatile("s_mov_b32 m0, %0\n\ts_nop 0\n\tbuffer_load_dwordx4 %1, %2, %3 offen lds" ::"s"(lds), "v"(voff), "s"(rs), "s"(soff) : "memory");
}
DI void dma4(unsigned lds, unsigned voff, rsrc_t rs) {
  asm volatile("s_mov_b32 m0, %0\n\ts_nop 0\n\tbuffer_load_dword %1, %2, 0 offen sc1 lds" ::"s"(lds), "v"(voff), "s"(rs) : "memory");
}
DI u64 lds_tag(lptr p) {
  const u32x2 v = *(const LAS u32x2*)p;
  return (((u64)(unsigned)__builtin_amdgcn_readfirstlane(v[1]) << 32) | (unsigned)__builtin_amdgcn_readfirstlane(v[0])) + 1;
}
DI bf16x8 lds_frag(lptr p) { return *(const LAS bf16x8*)p; }
DI bf16x8 tr_frag(lptr p, int o0, int o1) {
  const s16x4 lo = __builtin_bit_cast(s16x4, __builtin_amdgcn_ds_read_tr16_b64_v4i16((LAS s16x4*)(p + o0)));
  const s16x4 hi = __builtin_bit_cast(s16x4, __builtin_amdgcn_ds_read_tr16_b64_v4i16((LAS s16x4*)(p + o1)));
  return __builtin_bit_cast(bf16x8, __builtin_shufflevector(lo, hi, 0, 1, 2, 3, 4, 5, 6, 7));
}
DI float sel(bool c, float a, float b) { asm("" : "+v"(a), "+v"(b)); return c ? a : b; }
#define ACQ 0
constexpr int LD_AUX = ACQ ? 0 : 16;
DI void acquire() { if (ACQ) __builtin_amdgcn_fence(__ATOMIC_ACQUIRE, "agent"); }
DI void flag_store(char* ws, unsigned off, u64 v) { __hip_atomic_store((gu64*)(ws + off), v, __ATOMIC_RELAXED, __HIP_MEMORY_SCOPE_AGENT); }
DI void poll_flags(char* ws, unsigned off0, unsigned stride, int n, u64 tag, int lane) {
  gu64* p = (gu64*)(ws + off0 + (unsigned)lane * stride);
#pragma unroll 1
  for (unsigned spins = 0; spins < SPIN_CAP; ++spins) {
    u64 v = tag;
    if (lane < n) v = __hip_atomic_load(p, __ATOMIC_RELAXED, __HIP_MEMORY_SCOPE_AGENT);
    if (__builtin_amdgcn_ballot_w64(v != tag) == 0) break;
    __builtin_amdgcn_s_sleep(1);
  }
  __builtin_amdgcn_fence(__ATOMIC_ACQUIRE, "wavefront");
  asm volatile("" ::: "memory");
  acquire();
}
DI void lds_wait(lptr p, unsigned want) {
#pragma unroll 1
  for (unsigned spins = 0; spins < SPIN_CAP; ++spins) {
    if ((unsigned)__builtin_amdgcn_readfirstlane(__hip_atomic_load((LAS unsigned*)p, __ATOMIC_RELAXED, __HIP_MEMORY_SCOPE_WORKGROUP)) == want) break;
    __builtin_amdgcn_s_sleep(2);
  }
  asm volatile("" ::: "memory");
}
DI float sum_lanes_mod4(float x) {
  x += as_f(__builtin_amdgcn_update_dpp(0u, as_u(x), 0x124, 0xf, 0xf, false));
  x += as_f(__builtin_amdgcn_update_dpp(0u, as_u(x), 0x128, 0xf, 0xf, false));
  auto a = __builtin_amdgcn_permlane16_swap(as_u(x), as_u(x), false, false);
  x = as_f(a[0]) + as_f(a[1]);
  auto b = __builtin_amdgcn_permlane32_swap(as_u(x), as_u(x), false, false);
  return as_f(b[0]) + as_f(b[1]);
}
DI float swap_max(float m) {
  auto r = __builtin_amdgcn_permlane32_swap(as_u(m), as_u(m), false, false);
  return __builtin_fmaxf(as_f(r[0]), as_f(r[1]));
}

DI void read_wo(bf16x8 (&wo)[32], lptr L, int kq, int lane) {
  const int h = lane >> 5, q4 = (lane & 15) >> 2, p4 = lane & 3, nsub = (lane >> 4) & 1;
  const lptr base = L + (kq + 1) * SLOT + (4 * h + q4) * 128 + 8 * (p4 & 1);
  const int sw = ((q4 >> 1) & 1) << 2;
  const lptr b0 = base + ((2 * nsub + (p4 >> 1)) ^ sw) * 16, b1 = base + ((4 + 2 * nsub + (p4 >> 1)) ^ sw) * 16;
#pragma unroll
  for (int i = 0; i < 16; ++i) {
    wo[2 * i] = tr_frag(b0, i * 2048, i * 2048 + 1024);
    wo[2 * i + 1] = tr_frag(b1, i * 2048, i * 2048 + 1024);
  }
}

template <int BR>
DI void phase1(char* smem_generic, const rsrc_t rsX, const rsrc_t rsW, const rsrc_t rsWoA, const rsrc_t rsWoB, const rsrc_t rsG, const rsrc_t rsWS, char* ws,
               const int rt, const int quad, const int wid, const int lane, const unsigned vX0, const unsigned vX1, const unsigned vW,
               const unsigned vWo, unsigned& vts, u64& tagO, bf16x8 (&wo)[32]) {
  const lptr L = (lptr)smem_generic;
  const unsigned lds0 = (unsigned)(uintptr_t)L;
  const int c = wid >> 2, rq = wid & 3, p = rq & 1, sp = rq >> 1;
  const int i32 = lane & 31, h = lane >> 5;
  const unsigned pieceX = lds0 + wid * 2048, pieceW = lds0 + wid * 1024;
  const unsigned rot = ROT_OF(quad, rt);

  auto piece = [&](int f, int i) LAM {
    const unsigned sl = (unsigned)slot_of(f) * SLOT;
    if (f < 8) {
      const unsigned kc = (f + rot) & 7;
      if (i == 0) dma16(pieceX + sl, vX0, rsX, kc * 128);
      else if (i == 1) dma16(pieceX + sl + 1024, vX1, rsX, kc * 128);
      else dma16(pieceW + sl + 16384 + (i - 2) * 8192, vW, rsW, kc * 196608 + (i - 1) * 1024);
    } else {
      dma16(pieceW + sl + i * 8192, vW, rsW, (((f - 8) * 4 + i + rot) & 7) * 196608);
    }
  };
  auto wo_piece = [&](int which, unsigned q) LAM { dma16(lds0 + (which ? 3 : 1) * SLOT + q * 1024, vWo, which ? rsWoB : rsWoA, q * 8192); };

  const int xs = h ^ ((i32 >> 1) & 7);
  const int q4 = (lane & 15) >> 2, p4 = lane & 3, nsub = (lane >> 4) & 1;
  const int wchunk = (4 * c + 2 * nsub + (p4 >> 1)) ^ (((q4 >> 1) & 1) << 2);
  lptr xb[3][4], wb[3];
#pragma unroll
  for (int t = 0; t < 4; ++t) {
    xb[0][t] = L + (32 * rq + i32) * 128 + ((xs ^ (2 * t)) << 4);
    xb[1][t] = xb[0][t] + 2 * SLOT;
    xb[2][t] = xb[0][t] + 4 * SLOT;
  }
  wb[0] = L + (8 * h + q4) * 128 + wchunk * 16 + 8 * (p4 & 1);
  wb[1] = wb[0] + 2 * SLOT;
  wb[2] = wb[0] + 4 * SLOT;
#pragma unroll
  for (int t = 0; t < 4; ++t) { asm("" : "+v"(xb[0][t])); asm("" : "+v"(xb[1][t])); asm("" : "+v"(xb[2][t])); }
  asm("" : "+v"(wb[0])); asm("" : "+v"(wb[1])); asm("" : "+v"(wb[2]));

  bf16x8 xf[32], wk[3], wv[3];
  f32x16 accK = {}, accV = {};
  constexpr int VLAG = BR == 0 ? 8 : 0;
  auto rdv = [&](int s, int buf) LAM {
    const int sl = slot_of(s >> 2), o = (sl & 1) * SLOT + 16384 + 8192 + (s & 3) * 2048;
    wv[buf] = tr_frag(wb[sl >> 1], o, o + 512);
  };
  auto rd1 = [&](int s) LAM {
    if (s < 32) {
      const int sl = slot_of(s >> 2), t = s & 3;
      xf[s] = lds_frag(sl < 4 ? xb[sl >> 1][t] + (sl & 1) * SLOT : xb[2][t]);
      const int o = (sl & 1) * SLOT + 16384 + t * 2048;
      wk[s % 3] = tr_frag(wb[sl >> 1], o, o + 512);
      if (s < 32 - VLAG) rdv(s, s % 3);
    } else {
      rdv(s - VLAG, s % 3);
    }
  };
  WAIT_BAR(4);
  STAMP(15);
  rd1(0); rd1(1);
  SBAR();
  unsigned kpk[8];
  float zs0 = 0.f, zs1 = 0.f;
#pragma unroll
  for (int s = 0; s < 32 + VLAG; ++s) {
    const int cc = s >> 2;
    if ((s & 3) == 2 && cc < 7) {
      if (s == 2) WAIT_BAR_NL(4); else WAIT_BAR_NL(8);
      if (cc == 6 && wid == 0) {
        dma16(lds0 + L_GW, (lane * 16 + rot * 128) & 1023, rsG, 0);
        const unsigned e0 = WS_EO + rt * 128;
        const unsigned e1 = BR == 0 ? WS_EKV + quad * 128 : WS_EH + (rt * 8 + quad) * 128;
        const unsigned e2 = BR == 0 ? e1 : WS_EH + ((rt > 0 ? rt - 1 : 0) * 8 + quad) * 128;
        const unsigned eo = (lane < 2 ? e0 : lane < 4 ? e1 : e2) + (lane & 1) * 4;
        if (lane < 6) dma4(lds0 + L_EP, eo, rsWS);
        if (lane < 3) *(LAS unsigned*)(L + L_CNT + lane * 4) = 0;
      }
    }
    if (s < 2) { piece(2, 2 * s); piece(2, 2 * s + 1); }
    else if (s < 6) { piece(3 + ((s - 2) >> 1), 2 * (s & 1)); piece(3 + ((s - 2) >> 1), 2 * (s & 1) + 1); }
    else if (((s - 2) >> 2) + 4 < 10) piece(((s - 2) >> 2) + 4, (s - 2) & 3);
    if (s + 2 < 32 + VLAG) rd1(s + 2);
    if (s < 32) accK = BR == 0 ? MFMA(xf[s], wk[s % 3], accK) : MFMA(wk[s % 3], xf[s], accK);
    if (s < 32 - VLAG) accV = MFMA(xf[s], wv[s % 3], accV);
    if (s >= 32) {
      accV = MFMA(xf[s - VLAG], wv[s % 3], accV);
      kpk[s - 32] = phi_pk1(accK[2 * (s - 32)], accK[2 * (s - 32) + 1], zs0, zs1);
    }
    SBAR();
  }
  STAMP(2);
  bf16x8 Kf[2], Vf[2];
  if (BR == 0) {
    Kf[0] = __builtin_bit_cast(bf16x8, (u32x4){kpk[0], kpk[1], kpk[2], kpk[3]});
    Kf[1] = __builtin_bit_cast(bf16x8, (u32x4){kpk[4], kpk[5], kpk[6], kpk[7]});
  } else { Kf[0] = pack8(accK, 0); Kf[1] = pack8(accK, 8); }
  Vf[0] = pack8(accV, 0); Vf[1] = pack8(accV, 8);
  if (BR == 0) {
    f32x16 kv = {};
    kv = MFMA(Kf[0], Vf[0], kv);
    kv = MFMA(Kf[1], Vf[1], kv);
    const lptr pp = L + L_PART + wid * 2176;
    {
      const float zh = zs0 + zs1;
      auto sw = __builtin_amdgcn_permlane32_swap(as_u(zh), as_u(zh), false, false);
      if (h == 0) *(LAS float*)(pp + 2048 + i32 * 4) = as_f(sw[0]) + as_f(sw[1]);
    }
    const bool lo = i32 < 16;
    f32x4 u0, u1;
#pragma unroll
    for (int j = 0; j < 4; ++j) { u0[j] = sel(lo, kv[j], kv[8 + j]); u1[j] = sel(lo, kv[4 + j], kv[12 + j]); }
    *(LAS f32x4*)(pp + lane * 16) = u0;
    *(LAS f32x4*)(pp + 1024 + lane * 16) = u1;
  } else {
    if (sp == 0) {
      const lptr ex = L + L_PART + (c * 2 + p) * 1024;
      if (i32 >= 24) {
        *(LAS bf16x8*)(ex + h * 128 + (i32 - 24) * 16) = Kf[0];
        *(LAS bf16x8*)(ex + 256 + h * 128 + (i32 - 24) * 16) = Kf[1];
      }
      const u32x4 v1 = __builtin_bit_cast(u32x4, Vf[1]);
      *(LAS u32x2*)(ex + 512 + lane * 8) = (u32x2){v1[2], v1[3]};
    }
  }
  WAIT_BAR(0);
  STAMP(3);
  if (BR == 0) {
    if (wid == 3) {
      __builtin_amdgcn_s_setprio(3);
      f32x4 pa[5][4];
#pragma unroll
      for (int it = 0; it < 5; ++it) {
        const int u = it < 4 ? it * 64 + lane : 256 + (lane & 15);
        const int cc = it < 4 ? (it >> 1) : ((lane >> 3) & 1);
        const int o = it < 4 ? (u & 127) * 16 : 2048 + (lane & 7) * 16;
        const lptr s = L + L_PART + cc * 4 * 2176 + o;
#pragma unroll
        for (int r = 0; r < 4; ++r) pa[it][r] = *(LAS f32x4*)(s + r * 2176);
      }
      SBAR();
#pragma unroll
      for (int it = 0; it < 5; ++it) {
        f32x4 a;
#pragma unroll
        for (int j = 0; j < 4; ++j) a[j] = opq(opq(pa[it][0][j] + pa[it][1][j]) + opq(pa[it][2][j] + pa[it][3][j]));
        const unsigned go = WS_KVP + (quad * 16 + rt) * KVP_BYTES + (it < 4 ? it * 64 + lane : 256 + lane) * 16;
        if (it < 4 || lane < 16) __builtin_amdgcn_raw_buffer_store_b128(__builtin_bit_cast(u32x4, a), rsWS, go, 0, 16);
      }
      __builtin_amdgcn_s_setprio(0);
    }
  } else {
    if (sp == 1 && rt < 15) {
      const unsigned ex = WS_HALO + ((rt * 8 + quad) * 4 + c * 2 + p) * 1024;
      if (i32 >= 24) {
        __builtin_amdgcn_raw_buffer_store_b128(__builtin_bit_cast(u32x4, Kf[0]), rsWS, ex + h * 128 + (i32 - 24) * 16, 0, 16);
        __builtin_amdgcn_raw_buffer_store_b128(__builtin_bit_cast(u32x4, Kf[1]), rsWS, ex + 256 + h * 128 + (i32 - 24) * 16, 0, 16);
      }
      const u32x4 v1 = __builtin_bit_cast(u32x4, Vf[1]);
      __builtin_amdgcn_raw_buffer_store_b64((u32x2){v1[2], v1[3]}, rsWS, ex + 512 + lane * 8, 0, 16);
    }
  }
  SBAR();
  tagO = lds_tag(L + L_EP);
  const u64 tagP = lds_tag(L + L_EP + 8), tagG = lds_tag(L + L_EP + 16);
  if (BR == 0 && c == 1) __builtin_amdgcn_s_setprio(1);
  f32x16 accQ = {};
  {
    bf16x8 wq[5];
    auto rd2 = [&](int s) LAM {
      const lptr w = s < 16 ? wb[1] : wb[2];
      const int o = (s < 16 ? SLOT : -16 * 2048) + s * 2048;
      wq[s % 5] = tr_frag(w, o, o + 512);
    };
    rd2(0); rd2(1); rd2(2); rd2(3);
    SBAR();
#pragma unroll
    for (int s = 0; s < 32; ++s) {
      if (s + 4 < 32) rd2(s + 4);
      accQ = MFMA(wq[s % 5], xf[s], accQ);
      if (s >= 12 && s < 20) wo_piece(0, wid * 8 + (s - 12));
      if (s == PUBS) {
        if (BR == 0 ? (wid == 3) : (sp == 1 && rt < 15)) {
          WAIT_VM(0);
          if (lane == 0) flag_store(ws, BR == 0 ? WS_FKV + (quad * 16 + rt) * 128 : WS_FH + ((rt * 8 + quad) * 4 + c * 2 + p) * 64, tagP);
          STAMP(14);
        }
      }
      SBAR();
    }
  }
  bf16x8 Qf[2];
  if (BR == 0) { Qf[0] = phi8(accQ, 0); Qf[1] = phi8(accQ, 8); }
  else { Qf[0] = pack8(accQ, 0); Qf[1] = pack8(accQ, 8); }
  STAMP(4);
  asm volatile("s_waitcnt vmcnt(0) lgkmcnt(0)" ::: "memory");
  if (lane == 0) __hip_atomic_fetch_add((LAS unsigned*)(L + L_CNT), 1u, __ATOMIC_RELAXED, __HIP_MEMORY_SCOPE_WORKGROUP);
  STAMP(16);
  auto gate = [&](auto pre) LAM {
    f32x16 ga = {};
    bf16x8 gf[4];
    const lptr gp = L + L_GW + h * 16;
#pragma unroll
    for (int t = 0; t < 3; ++t) gf[t] = lds_frag(gp + t * 32);
    SBAR();
#pragma unroll
    for (int t = 0; t < 32; ++t) {
      pre(t);
      if (t + 3 < 32) gf[(t + 3) % 4] = lds_frag(gp + (t + 3) * 32);
      ga = MFMA(gf[t % 4], xf[t], ga);
      SBAR();
    }
    const float g = __builtin_amdgcn_rcpf(1.f + __builtin_amdgcn_exp2f(-LOG2E * ga[0]));
    if (h == 0) *(LAS float*)(L + L_G + (p * 32 + i32) * 4) = g;
  };
  const unsigned ofrag = WS_O + ((((rt * 2 + sp) * 2 + p) * 64) + BR * 32 + quad * 4 + 2 * c) * 1024 + lane * 16;
  if (BR == 0) {
    if (wid < 2) { gate([&](int) LAM {}); STAMP(20); }
    if (c == 0) {
      lds_wait(L + L_CNT, 8);
      STAMP(21);
      read_wo(wo, L, wid >> 1, lane);
      STAMP(22);
      lds_wait(L + L_UIS, 4);
      STAMP(17);
#pragma unroll
      for (int i = 0; i < 16; ++i) wo_piece(1, wid * 16 + i);
      STAMP(18);
    }
    if (c == 1) {
      poll_flags(ws, WS_FKV + quad * 16 * 128, 128, 16, tagG, lane);
      STAMP(5);
      const unsigned u = rq * 64 + lane;
      f32x4 a[16];
#pragma unroll
      for (int r = 0; r < 16; ++r)
        a[r] = __builtin_bit_cast(f32x4, __builtin_amdgcn_raw_buffer_load_b128(rsWS, WS_KVP + quad * 16 * KVP_BYTES + u * 16, r * KVP_BYTES, LD_AUX));
      f32x4 zv = __builtin_bit_cast(f32x4, __builtin_amdgcn_raw_buffer_load_b128(rsWS, WS_KVP + (quad * 16 + (lane >> 2)) * KVP_BYTES + (256 + rq * 4 + (lane & 3)) * 16, 0, LD_AUX));
      SBAR();
      if (lane == 0) __hip_atomic_fetch_add((LAS unsigned*)(L + L_UIS), 1u, __ATOMIC_RELAXED, __HIP_MEMORY_SCOPE_WORKGROUP);
      SBAR();
#pragma unroll
      for (int s = 1; s < 16; s *= 2)
#pragma unroll
        for (int r = 0; r < 16; r += 2 * s) a[r] += a[r + s];
      *(LAS f32x4*)(L + L_TOT + u * 16) = a[0];
#pragma unroll
      for (int j = 0; j < 4; ++j) zv[j] = sum_lanes_mod4(zv[j]);
      if (lane < 4) *(LAS f32x4*)(L + L_TOT + (256 + rq * 4 + lane) * 16) = zv;
      STAMP(23);
    }
    LBAR();
    STAMP(6);
    const f32x4 u0 = *(LAS f32x4*)(L + L_TOT + (c * 128 + lane) * 16), u1 = *(LAS f32x4*)(L + L_TOT + (c * 128 + 64 + lane) * 16);
    const lptr zp = L + L_TOT + 4096 + c * 128 + h * 16;
    const f32x4 z0 = *(LAS f32x4*)zp, z1 = *(LAS f32x4*)(zp + 32), z2 = *(LAS f32x4*)(zp + 64), z3 = *(LAS f32x4*)(zp + 96);
    const bool lo = i32 < 16;
    const u32x4 pu = {pk(u0[0], u0[1]), pk(u0[2], u0[3]), pk(u1[0], u1[1]), pk(u1[2], u1[3])};
    const u32x4 pz0 = {pk(z0[0], z0[1]), pk(z0[2], z0[3]), pk(z1[0], z1[1]), pk(z1[2], z1[3])};
    const u32x4 pz1 = {pk(z2[0], z2[1]), pk(z2[2], z2[3]), pk(z3[0], z3[1]), pk(z3[2], z3[3])};
    u32x4 a0, a1;
#pragma unroll
    for (int j = 0; j < 4; ++j) { a0[j] = lo ? pu[j] : pz0[j]; a1[j] = lo ? pz1[j] : pu[j]; }
    const f32x16 zero = {};
    const f32x16 d0 = MFMA(__builtin_bit_cast(bf16x8, a0), Qf[0], zero);
    const f32x16 d1 = MFMA(__builtin_bit_cast(bf16x8, a1), Qf[1], zero);
    const float s0 = __builtin_amdgcn_rcpf(d0[8] + 1e-6f), s1 = __builtin_amdgcn_rcpf(d1[0] + 1e-6f);
    const u32x4 o0 = {pk(d0[0] * s0, d0[1] * s0), pk(d0[2] * s0, d0[3] * s0), pk(d0[4] * s0, d0[5] * s0), pk(d0[6] * s0, d0[7] * s0)};
    const u32x4 o1 = {pk(d1[8] * s1, d1[9] * s1), pk(d1[10] * s1, d1[11] * s1), pk(d1[12] * s1, d1[13] * s1), pk(d1[14] * s1, d1[15] * s1)};
    __builtin_amdgcn_raw_buffer_store_b128(o0, rsWS, ofrag, 0, 16);
    __builtin_amdgcn_raw_buffer_store_b128(o1, rsWS, ofrag + 1024, 0, 16);
    if (rt == 0 && wid == 4 && lane == 0) flag_store(ws, WS_EKV + quad * 128, tagG);
  } else {
    bf16x8 Kp[2];
    u32x2 vp;
    if (sp == 1) {
      const lptr ex = L + L_PART + (c * 2 + p) * 1024;
      Kp[0] = lds_frag(ex + h * 128 + (i32 & 7) * 16);
      Kp[1] = lds_frag(ex + 256 + h * 128 + (i32 & 7) * 16);
      vp = *(LAS u32x2*)(ex + 512 + lane * 8);
    } else if (rt > 0) {
      poll_flags(ws, WS_FH + (((rt - 1) * 8 + quad) * 4 + c * 2 + p) * 64, 64, 1, tagG, lane);
      const unsigned ex = WS_HALO + (((rt - 1) * 8 + quad) * 4 + c * 2 + p) * 1024;
      Kp[0] = __builtin_bit_cast(bf16x8, __builtin_amdgcn_raw_buffer_load_b128(rsWS, ex + h * 128 + (i32 & 7) * 16, 0, LD_AUX));
      Kp[1] = __builtin_bit_cast(bf16x8, __builtin_amdgcn_raw_buffer_load_b128(rsWS, ex + 256 + h * 128 + (i32 & 7) * 16, 0, LD_AUX));
      vp = __builtin_amdgcn_raw_buffer_load_b64(rsWS, ex + 512 + lane * 8, 0, LD_AUX);
      if (wid == 0 && lane == 0) flag_store(ws, WS_EH + ((rt - 1) * 8 + quad) * 128, tagG);
    } else {
      Kp[0] = Kf[0]; Kp[1] = Kf[1]; vp = (u32x2){0u, 0u};
    }
    STAMP(5);
    STAMP(6);
    const bool noprev = (rt == 0 && sp == 0);
    f32x16 bias;
#pragma unroll
    for (int r = 0; r < 16; ++r) bias[r] = ((unsigned)(i32 - ((r & 3) + 8 * (r >> 2) + 4 * h)) <= 7u) ? 0.f : -1e30f;
    float bprev[4];
#pragma unroll
    for (int j = 0; j < 4; ++j) bprev[j] = (!noprev && (24 + 4 * h + j >= i32 + 25)) ? 0.f : -1e30f;
    constexpr float C1 = 0.25f * LOG2E;
    const unsigned ONE2 = 0x3F803F80u;
    const f32x16 zero = {};
#pragma unroll
    for (int hh = 0; hh < 2; ++hh) {
      f32x16 sc = MFMA(Kf[hh], Qf[hh], bias);
      const f32x16 sv = MFMA(Kp[hh], Qf[hh], zero);
      float sp4[4];
#pragma unroll
      for (int j = 0; j < 4; ++j) sp4[j] = sv[12 + j] + bprev[j];
      float m = __builtin_fmaxf(__builtin_fmaxf(sp4[0], sp4[1]), __builtin_fmaxf(sp4[2], sp4[3]));
#pragma unroll
      for (int r = 0; r < 16; r += 2) m = __builtin_fmaxf(__builtin_fmaxf(m, sc[r]), sc[r + 1]);
      m = swap_max(m);
      const float nm = -m * C1;
#pragma unroll
      for (int r = 0; r < 16; ++r) sc[r] = __builtin_amdgcn_exp2f(__builtin_fmaf(sc[r], C1, nm));
#pragma unroll
      for (int j = 0; j < 4; ++j) sp4[j] = __builtin_amdgcn_exp2f(__builtin_fmaf(sp4[j], C1, nm));
      const bf16x8 P0 = pack8(sc, 0), P1 = pack8(sc, 8);
      const u32x4 ppv = {0u, 0u, pk(sp4[0], sp4[1]), pk(sp4[2], sp4[3])};
      const bool mine = (i32 < 16) == (hh == 0);
      const u32x4 v0 = __builtin_bit_cast(u32x4, Vf[0]), v1 = __builtin_bit_cast(u32x4, Vf[1]);
      u32x4 a0, a1, ap;
#pragma unroll
      for (int j = 0; j < 4; ++j) { a0[j] = mine ? v0[j] : ONE2; a1[j] = mine ? v1[j] : ONE2; }
      ap = (u32x4){0u, 0u, mine ? vp[0] : ONE2, mine ? vp[1] : ONE2};
      f32x16 o = MFMA(__builtin_bit_cast(bf16x8, a0), P0, zero);
      o = MFMA(__builtin_bit_cast(bf16x8, a1), P1, o);
      o = MFMA(__builtin_bit_cast(bf16x8, ap), __builtin_bit_cast(bf16x8, ppv), o);
      const int nb = hh == 0 ? 0 : 8, db = hh == 0 ? 8 : 0;
      const float s = __builtin_amdgcn_rcpf(o[db]);
      const u32x4 ov = {pk(o[nb] * s, o[nb + 1] * s), pk(o[nb + 2] * s, o[nb + 3] * s), pk(o[nb + 4] * s, o[nb + 5] * s), pk(o[nb + 6] * s, o[nb + 7] * s)};
      __builtin_amdgcn_raw_buffer_store_b128(ov, rsWS, ofrag + hh * 1024, 0, 16);
    }
  }
  STAMP(7);
  WAIT_VM(0);
  if (lane == 0) flag_store(ws, WS_FO + (((((rt * 2 + sp) * 2 + p) * 2 + BR) * 8 + quad) * 2 + c) * 64, tagO);
  if (BR == 0 && c == 0 && lane == 0) __hip_atomic_fetch_add((LAS unsigned*)(L + L_WOB), 1u, __ATOMIC_RELAXED, __HIP_MEMORY_SCOPE_WORKGROUP);
  STAMP(8);
  __builtin_amdgcn_s_setprio(0);
  if (BR == 1 && c == 1) {
    lds_wait(L + L_CNT, 8);
#pragma unroll
    for (int i = 0; i < 16; ++i) wo_piece(1, rq * 16 + i);
    STAMP(18);
  }
  if (BR == 1 && wid >= 6) gate([&](int) LAM {});
}

__global__ __launch_bounds__(512, 2) void gated_mix_kernel(const __hip_bfloat16* __restrict__ x, const __hip_bfloat16* __restrict__ wA,
                                                           const __hip_bfloat16* __restrict__ woA, const __hip_bfloat16* __restrict__ wB,
                                                           const __hip_bfloat16* __restrict__ woB, const __hip_bfloat16* __restrict__ gw,
                                                           __hip_bfloat16* __restrict__ out, char* ws) {
  __shared__ __attribute__((aligned(1024))) char smem[LDS_BYTES];
  const int tid = threadIdx.x, lane = tid & 63, wid = __builtin_amdgcn_readfirstlane(tid >> 6);
  const int b = blockIdx.x, xcd = b & 7, loc = b >> 3;
  const int br = xcd & 1, rt = (xcd >> 1) * 4 + (loc >> 3), quad = loc & 7;
  unsigned vts = 0;
  const u64 dstart = __builtin_amdgcn_s_memrealtime();
  if (tid == 0) __hip_atomic_store((gu64*)(ws + WS_STAMP + b * 8), dstart, __ATOMIC_RELAXED, __HIP_MEMORY_SCOPE_AGENT);
  const rsrc_t rsX = __builtin_amdgcn_make_buffer_rsrc((void*)x, 0, 2048 * 512 * 2, 0x00020000);
  const rsrc_t rsW = __builtin_amdgcn_make_buffer_rsrc((void*)(br ? wB : wA), 0, 512 * 1536 * 2, 0x00020000);
  const rsrc_t rsWoA = __builtin_amdgcn_make_buffer_rsrc((void*)woA, 0, 512 * 512 * 2, 0x00020000);
  const rsrc_t rsWoB = __builtin_amdgcn_make_buffer_rsrc((void*)woB, 0, 512 * 512 * 2, 0x00020000);
  const rsrc_t rsG = __builtin_amdgcn_make_buffer_rsrc((void*)gw, 0, 1024, 0x00020000);
  const rsrc_t rsWS = __builtin_amdgcn_make_buffer_rsrc((void*)ws, 0, WS_USED, 0x00020000);
  const lptr L = (lptr)smem;
  const unsigned lds0 = (unsigned)(uintptr_t)L;

  const int l8 = lane >> 3, c8 = lane & 7;
  const int xrow = rt * 128 + 64 * (wid >> 2) + ((wid >> 1) & 1) + 2 * (16 * (wid & 1) + l8);
  const unsigned vX0 = xrow * 1024 + ((c8 ^ (lane >> 4)) << 4);
  const unsigned vX1 = (xrow + 16) * 1024 + ((c8 ^ (4 + (lane >> 4))) << 4);
  const int wsw = (c8 ^ (((lane >> 4) & 1) << 2)) << 4;
  const unsigned vW = (8 * wid + l8) * 3072 + quad * 128 + wsw;
  const unsigned vWo = l8 * 1024 + quad * 128 + wsw;

  asm volatile("s_nop 4" ::: "memory");
#pragma unroll
  for (int f = 0; f < 2; ++f) {
    const unsigned sl = lds0 + f * SLOT;
    const unsigned kc = (f + ROT_OF(quad, rt)) & 7;
    dma16(sl + wid * 2048, vX0, rsX, kc * 128);
    dma16(sl + wid * 2048 + 1024, vX1, rsX, kc * 128);
    dma16(sl + 16384 + wid * 1024, vW, rsW, kc * 196608 + 1024);
    dma16(sl + 24576 + wid * 1024, vW, rsW, kc * 196608 + 2048);
    if (f < 1) asm volatile("s_barrier" ::: "memory");
  }
  STAMP(1);
  u64 tagO;
  bf16x8 wo[32];
  if (br == 0) phase1<0>(smem, rsX, rsW, rsWoA, rsWoB, rsG, rsWS, ws, rt, quad, wid, lane, vX0, vX1, vW, vWo, vts, tagO, wo);
  else phase1<1>(smem, rsX, rsW, rsWoA, rsWoB, rsG, rsWS, ws, rt, quad, wid, lane, vX0, vX1, vW, vWo, vts, tagO, wo);

  const int pt = wid & 1, kq = (wid >> 2) * 2 + ((wid >> 1) & 1);
  const int i32 = lane & 31, h = lane >> 5;
  const unsigned fwait = WS_FO + (((((rt * 2 + br) * 2 + pt) * 2 + (kq >> 1)) * 8 + (kq & 1) * 4) * 2) * 64;
  const unsigned ob = WS_O + ((((rt * 2 + br) * 2 + pt) * 64) + kq * 16) * 1024 + lane * 16;
  bf16x8 of[16];
  auto load_o = [&]() LAM {
#pragma unroll
    for (int i = 0; i < 16; ++i) of[i] = __builtin_bit_cast(bf16x8, __builtin_amdgcn_raw_buffer_load_b128(rsWS, ob, i * 1024, LD_AUX));
  };
  bool ready = false;
  if (!(br == 0 && kq < 2)) {
    u64 v0 = tagO;
    if (lane < 8) v0 = __hip_atomic_load((gu64*)(ws + fwait + lane * 64), __ATOMIC_RELAXED, __HIP_MEMORY_SCOPE_AGENT);
    ready = __builtin_amdgcn_ballot_w64(v0 != tagO) == 0;
    WAIT_VM(0);
    if (br == 1 && kq >= 2 && lane == 0) __hip_atomic_fetch_add((LAS unsigned*)(L + L_WOB), 1u, __ATOMIC_RELAXED, __HIP_MEMORY_SCOPE_WORKGROUP);
    STAMP(26);
    if (ready) load_o();
    if (kq >= 2) lds_wait(L + L_WOB, 4); else lds_wait(L + L_CNT, 8);
    STAMP(19);
    read_wo(wo, L, kq, lane);
  }
  STAMP(9);
  if (!ready) {
    poll_flags(ws, fwait, 64, 8, tagO, lane);
    load_o();
  }
  STAMP(10);
  SBAR();
  f32x16 a0 = {}, a1 = {};
#pragma unroll
  for (int i = 0; i < 16; ++i) { a0 = MFMA(wo[2 * i], of[i], a0); a1 = MFMA(wo[2 * i + 1], of[i], a1); }
  STAMP(11);
  LBAR();
  STAMP(24);
  {
    const lptr pr = L + L_RED + (kq * 64 + pt * 32 + i32) * PROW + h * 16;
#pragma unroll
    for (int g4 = 0; g4 < 4; ++g4) {
      *(LAS f32x4*)(pr + g4 * 32) = (f32x4){a0[4 * g4], a0[4 * g4 + 1], a0[4 * g4 + 2], a0[4 * g4 + 3]};
      *(LAS f32x4*)(pr + 128 + g4 * 32) = (f32x4){a1[4 * g4], a1[4 * g4 + 1], a1[4 * g4 + 2], a1[4 * g4 + 3]};
    }
  }
  LBAR();
  STAMP(13);
  {
    const int pos = tid >> 3, cg = tid & 7;
    const lptr pr = L + L_RED + pos * PROW + cg * 32;
    const float g = *(LAS float*)(L + L_G + pos * 4), gb = 1.f - g;
    const f32x4 s0 = (*(LAS f32x4*)pr + *(LAS f32x4*)(pr + 64 * PROW)) * g + (*(LAS f32x4*)(pr + 128 * PROW) + *(LAS f32x4*)(pr + 192 * PROW)) * gb;
    const f32x4 s1 = (*(LAS f32x4*)(pr + 16) + *(LAS f32x4*)(pr + 64 * PROW + 16)) * g + (*(LAS f32x4*)(pr + 128 * PROW + 16) + *(LAS f32x4*)(pr + 192 * PROW + 16)) * gb;
    const int row = rt * 128 + 64 * br + 2 * (pos & 31) + (pos >> 5);
    const u32x4 ov = {pk(s0[0], s0[1]), pk(s0[2], s0[3]), pk(s1[0], s1[1]), pk(s1[2], s1[3])};
    __builtin_amdgcn_raw_buffer_store_b128(ov, __builtin_amdgcn_make_buffer_rsrc((void*)out, 0, 2048 * 1024, 0x00020000), row * 1024 + quad * 128 + cg * 16, 0, 0);
  }
  if (quad == 0 && br == 0 && tid == 0) flag_store(ws, WS_EO + rt * 128, tagO);
  asm volatile("s_waitcnt vmcnt(0)" ::: "memory");
  __syncthreads();
  if (wid == 0) {
    const u64 dend = __builtin_amdgcn_s_memrealtime();
    long long mx = 0;
    for (int i = 0; i < 4; ++i) {
      const u64 s = __hip_atomic_load((gu64*)(ws + WS_STAMP + (i * 64 + lane) * 8), __ATOMIC_RELAXED, __HIP_MEMORY_SCOPE_AGENT);
      const long long d = (long long)(dend - s);
      if (d >= 0 && d < 100000) mx = d > mx ? d : mx;
    }
    for (int m = 32; m >= 1; m >>= 1) { long long c2 = __shfl_xor(mx, m); mx = c2 > mx ? c2 : mx; }
    const float v = (float)mx * 0.01f;
    if (lane == 0 && v > DIAGTHR) out[(rt * 128 + 64 * br) * 512 + quad * 64] = __float2bfloat16(v);
  }
}

#if NOPK && defined(__HIP_DEVICE_COMPILE__)
#pragma clang attribute pop
#endif

extern "C" void kernel_launch(void* const* d_in, const int* in_sizes, int n_in, void* d_out, int out_size, void* d_ws, size_t ws_size,
                              hipStream_t stream) {
  gated_mix_kernel<<<256, 512, 0, stream>>>((const __hip_bfloat16*)d_in[0], (const __hip_bfloat16*)d_in[1], (const __hip_bfloat16*)d_in[2],
                                            (const __hip_bfloat16*)d_in[4], (const __hip_bfloat16*)d_in[5], (const __hip_bfloat16*)d_in[6],
                                            (__hip_bfloat16*)d_out, (char*)d_ws);
}
```

```cpp
#include <hip/hip_runtime.h>
#include <hip/hip_bf16.h>
#include <stdint.h>

typedef __bf16 bf16x2 __attribute__((ext_vector_type(2)));
typedef __bf16 bf16x8 __attribute__((ext_vector_type(8)));
typedef short s16x4 __attribute__((ext_vector_type(4)));
typedef float f32x2 __attribute__((ext_vector_type(2)));
typedef float f32x4 __attribute__((ext_vector_type(4)));
typedef float f32x16 __attribute__((ext_vector_type(16)));
typedef unsigned u32x2 __attribute__((ext_vector_type(2)));
typedef unsigned u32x4 __attribute__((ext_vector_type(4)));
typedef unsigned long long u64;
#define LAS __attribute__((address_space(3)))
#define GAS __attribute__((address_space(1)))
#define DI __device__ __forceinline__
typedef LAS char* lptr;
typedef GAS u64 gu64;
typedef __amdgpu_buffer_rsrc_t rsrc_t;

constexpr unsigned WS_EO = 0;
constexpr unsigned WS_EKV = 2048;
constexpr unsigned WS_EH = 4096;
constexpr unsigned WS_FKV = 32768;
constexpr unsigned WS_FH = 65536;
constexpr unsigned WS_FO = 131072;
constexpr unsigned WS_KVP = 262144 + 65536;
constexpr unsigned KVP_BYTES = 4352;
constexpr unsigned WS_HALO = 1048576;
constexpr unsigned WS_O = 2097152;
constexpr unsigned WS_STAMP = 8u << 20;
constexpr unsigned WS_USED = 16u << 20;

constexpr int SLOT = 32768;
constexpr int L_GW = 0;
constexpr int L_G = L_GW + 1024;
constexpr int L_EP = L_GW + 1536;
constexpr int L_CNT = L_GW + 1568;
constexpr int L_WOB = L_GW + 1572;
constexpr int L_UIS = L_GW + 1576;
constexpr int L_PART = L_GW + 2048;
constexpr int L_TOT = L_PART + 8 * 2176;
constexpr int L_SINK = L_TOT + 4352;
constexpr int L_RED = SLOT;
constexpr int LDS_BYTES = 163840;
constexpr int PROW = 272;
#define slot_of(f) ((f) < 8 ? (f) % 5 : (f) == 8 ? 3 : 4)

constexpr float LOG2E = 1.4426950408889634f;
constexpr unsigned SPIN_CAP = 1u << 16;

#define MFMA(a, b, c) __builtin_amdgcn_mfma_f32_32x32x16_bf16((a), (b), (c), 0, 0, 0)
#define WAIT_BAR(N) asm volatile("s_waitcnt vmcnt(" #N ") lgkmcnt(0)\n\ts_barrier" ::: "memory")
#define WAIT_BAR_NL(N) asm volatile("s_waitcnt vmcnt(" #N ")\n\ts_barrier" ::: "memory")
#define WAIT_VM(N) asm volatile("s_waitcnt vmcnt(" #N ")" ::: "memory")
#define ROTM 6
#define ROT_OF(q, r) ((unsigned)(ROTM == 0 ? 0 : ROTM == 1 ? (q) : ROTM == 2 ? (q) + 2 * (r) : ROTM == 3 ? 2 * (r) : ROTM == 4 ? (r) : ROTM == 5 ? (r) + (q) : (r) + 4 * ((q) & 1)))
#define TOUCH 1
#define PUBS 7
#define SBAR() __builtin_amdgcn_sched_barrier(0)
#define LBAR() asm volatile("s_waitcnt lgkmcnt(0)\n\ts_barrier" ::: "memory")

#define STAMP(i) do {} while (0)
#define TSDUMP() do {} while (0)

#define NOPK 0
#if NOPK && defined(__HIP_DEVICE_COMPILE__)
#define LAM __attribute__((target("no-packed-fp32-ops"), always_inline))
#else
#define LAM __attribute__((always_inline))
#endif
#if NOPK && defined(__HIP_DEVICE_COMPILE__)
#pragma clang attribute push(__attribute__((target("no-packed-fp32-ops"))), apply_to = function)
#endif
DI float as_f(unsigned u) { return __builtin_bit_cast(float, u); }
DI unsigned as_u(float f) { return __builtin_bit_cast(unsigned, f); }
DI unsigned pk(float a, float b) { return __builtin_bit_cast(unsigned, __builtin_convertvector((f32x2){a, b}, bf16x2)); }
DI bf16x8 pack8(const f32x16& a, int o) {
  u32x4 r = {pk(a[o], a[o + 1]), pk(a[o + 2], a[o + 3]), pk(a[o + 4], a[o + 5]), pk(a[o + 6], a[o + 7])};
  return __builtin_bit_cast(bf16x8, r);
}
DI unsigned phi_pk(float a, float b, f32x2& zsum) {
  const f32x2 t = {a, b};
  const f32x2 u = t * (f32x2){LOG2E, LOG2E}, w = t + (f32x2){1.f, 1.f};
  const float e0 = __builtin_amdgcn_fmed3f(__builtin_amdgcn_exp2f(u[0]), 0.f, 1.f), e1 = __builtin_amdgcn_fmed3f(__builtin_amdgcn_exp2f(u[1]), 0.f, 1.f);
  const f32x2 r = (f32x2){__builtin_fmaxf(w[0], e0), __builtin_fmaxf(w[1], e1)} + (f32x2){1e-6f, 1e-6f};
  zsum += r;
  return pk(r[0], r[1]);
}
DI float opq(float x) { asm("" : "+v"(x)); return x; }
DI unsigned phi_pk1(float a, float b, float& z0, float& z1) {
  const float e0 = __builtin_amdgcn_fmed3f(__builtin_amdgcn_exp2f(a * LOG2E), 0.f, 1.f), e1 = __builtin_amdgcn_fmed3f(__builtin_amdgcn_exp2f(b * LOG2E), 0.f, 1.f);
  const float r0 = opq(__builtin_fmaxf(a + 1.f, e0) + 1e-6f), r1 = __builtin_fmaxf(b + 1.f, e1) + 1e-6f;
  z0 = opq(z0 + r0); z1 += r1;
  return pk(r0, r1);
}
DI bf16x8 phi8(const f32x16& a, int o) {
  f32x2 unused = {};
  u32x4 r = {phi_pk(a[o], a[o + 1], unused), phi_pk(a[o + 2], a[o + 3], unused), phi_pk(a[o + 4], a[o + 5], unused), phi_pk(a[o + 6], a[o + 7], unused)};
  return __builtin_bit_cast(bf16x8, r);
}
DI void dma16(unsigned lds, unsigned voff, rsrc_t rs, unsigned soff) {
  asm volatile("s_mov_b32 m0, %0\n\ts_nop 0\n\tbuffer_load_dwordx4 %1, %2, %3 offen lds" ::"s"(lds), "v"(voff), "s"(rs), "s"(soff) : "memory");
}
DI void dma4(unsigned lds, unsigned voff, rsrc_t rs) {
  asm volatile("s_mov_b32 m0, %0\n\ts_nop 0\n\tbuffer_load_dword %1, %2, 0 offen sc1 lds" ::"s"(lds), "v"(voff), "s"(rs) : "memory");
}
DI void touch(unsigned lds, const char* p) {
  asm volatile("s_mov_b32 m0, %0\n\ts_nop 0\n\tglobal_load_lds_dword %1, off" ::"s"(lds), "v"(p) : "memory");
}
DI u64 lds_tag(lptr p) {
  const u32x2 v = *(const LAS u32x2*)p;
  return (((u64)(unsigned)__builtin_amdgcn_readfirstlane(v[1]) << 32) | (unsigned)__builtin_amdgcn_readfirstlane(v[0])) + 1;
}
DI bf16x8 lds_frag(lptr p) { return *(const LAS bf16x8*)p; }
DI bf16x8 tr_frag(lptr p, int o0, int o1) {
  const s16x4 lo = __builtin_bit_cast(s16x4, __builtin_amdgcn_ds_read_tr16_b64_v4i16((LAS s16x4*)(p + o0)));
  const s16x4 hi = __builtin_bit_cast(s16x4, __builtin_amdgcn_ds_read_tr16_b64_v4i16((LAS s16x4*)(p + o1)));
  return __builtin_bit_cast(bf16x8, __builtin_shufflevector(lo, hi, 0, 1, 2, 3, 4, 5, 6, 7));
}
DI float sel(bool c, float a, float b) { asm("" : "+v"(a), "+v"(b)); return c ? a : b; }
#define ACQ 0
constexpr int LD_AUX = ACQ ? 0 : 16;
DI void acquire() { if (ACQ) __builtin_amdgcn_fence(__ATOMIC_ACQUIRE, "agent"); }
DI void flag_store(char* ws, unsigned off, u64 v) { __hip_atomic_store((gu64*)(ws + off), v, __ATOMIC_RELAXED, __HIP_MEMORY_SCOPE_AGENT); }
DI void poll_flags(char* ws, unsigned off0, unsigned stride, int n, u64 tag, int lane) {
  gu64* p = (gu64*)(ws + off0 + (unsigned)lane * stride);
#pragma unroll 1
  for (unsigned spins = 0; spins < SPIN_CAP; ++spins) {
    u64 v = tag;
    if (lane < n) v = __hip_atomic_load(p, __ATOMIC_RELAXED, __HIP_MEMORY_SCOPE_AGENT);
    if (__builtin_amdgcn_ballot_w64(v != tag) == 0) break;
    __builtin_amdgcn_s_sleep(1);
  }
  __builtin_amdgcn_fence(__ATOMIC_ACQUIRE, "wavefront");
  asm volatile("" ::: "memory");
  acquire();
}
DI void lds_wait(lptr p, unsigned want) {
#pragma unroll 1
  for (unsigned spins = 0; spins < SPIN_CAP; ++spins) {
    if ((unsigned)__builtin_amdgcn_readfirstlane(__hip_atomic_load((LAS unsigned*)p, __ATOMIC_RELAXED, __HIP_MEMORY_SCOPE_WORKGROUP)) == want) break;
    __builtin_amdgcn_s_sleep(2);
  }
  asm volatile("" ::: "memory");
}
DI float sum_lanes_mod4(float x) {
  x += as_f(__builtin_amdgcn_update_dpp(0u, as_u(x), 0x124, 0xf, 0xf, false));
  x += as_f(__builtin_amdgcn_update_dpp(0u, as_u(x), 0x128, 0xf, 0xf, false));
  auto a = __builtin_amdgcn_permlane16_swap(as_u(x), as_u(x), false, false);
  x = as_f(a[0]) + as_f(a[1]);
  auto b = __builtin_amdgcn_permlane32_swap(as_u(x), as_u(x), false, false);
  return as_f(b[0]) + as_f(b[1]);
}
DI float swap_max(float m) {
  auto r = __builtin_amdgcn_permlane32_swap(as_u(m), as_u(m), false, false);
  return __builtin_fmaxf(as_f(r[0]), as_f(r[1]));
}

DI void read_wo(bf16x8 (&wo)[32], lptr L, int kq, int lane) {
  const int h = lane >> 5, q4 = (lane & 15) >> 2, p4 = lane & 3, nsub = (lane >> 4) & 1;
  const lptr base = L + (kq + 1) * SLOT + (4 * h + q4) * 128 + 8 * (p4 & 1);
  const int sw = ((q4 >> 1) & 1) << 2;
  const lptr b0 = base + ((2 * nsub + (p4 >> 1)) ^ sw) * 16, b1 = base + ((4 + 2 * nsub + (p4 >> 1)) ^ sw) * 16;
#pragma unroll
  for (int i = 0; i < 16; ++i) {
    wo[2 * i] = tr_frag(b0, i * 2048, i * 2048 + 1024);
    wo[2 * i + 1] = tr_frag(b1, i * 2048, i * 2048 + 1024);
  }
}

template <int BR>
DI void phase1(char* smem_generic, const rsrc_t rsX, const rsrc_t rsW, const rsrc_t rsWoA, const rsrc_t rsWoB, const rsrc_t rsG, const rsrc_t rsWS, char* ws,
               const int rt, const int quad, const int wid, const int lane, const unsigned vX0, const unsigned vX1, const unsigned vW,
               const unsigned vWo, unsigned& vts, u64& tagO, bf16x8 (&wo)[32], const char* outp, const void* woap, const void* wobp) {
  const lptr L = (lptr)smem_generic;
  const unsigned lds0 = (unsigned)(uintptr_t)L;
  const int c = wid >> 2, rq = wid & 3, p = rq & 1, sp = rq >> 1;
  const int i32 = lane & 31, h = lane >> 5;
  const unsigned pieceX = lds0 + wid * 2048, pieceW = lds0 + wid * 1024;
  const unsigned rot = ROT_OF(quad, rt);

  auto piece = [&](int f, int i) LAM {
    const unsigned sl = (unsigned)slot_of(f) * SLOT;
    if (f < 8) {
      const unsigned kc = (f + rot) & 7;
      if (i == 0) dma16(pieceX + sl, vX0, rsX, kc * 128);
      else if (i == 1) dma16(pieceX + sl + 1024, vX1, rsX, kc * 128);
      else dma16(pieceW + sl + 16384 + (i - 2) * 8192, vW, rsW, kc * 196608 + (i - 1) * 1024);
    } else {
      dma16(pieceW + sl + i * 8192, vW, rsW, (((f - 8) * 4 + i + rot) & 7) * 196608);
    }
  };
  auto wo_piece = [&](int which, unsigned q) LAM { dma16(lds0 + (which ? 3 : 1) * SLOT + q * 1024, vWo, which ? rsWoB : rsWoA, q * 8192); };

  const int xs = h ^ ((i32 >> 1) & 7);
  const int q4 = (lane & 15) >> 2, p4 = lane & 3, nsub = (lane >> 4) & 1;
  const int wchunk = (4 * c + 2 * nsub + (p4 >> 1)) ^ (((q4 >> 1) & 1) << 2);
  lptr xb[3][4], wb[3];
#pragma unroll
  for (int t = 0; t < 4; ++t) {
    xb[0][t] = L + (32 * rq + i32) * 128 + ((xs ^ (2 * t)) << 4);
    xb[1][t] = xb[0][t] + 2 * SLOT;
    xb[2][t] = xb[0][t] + 4 * SLOT;
  }
  wb[0] = L + (8 * h + q4) * 128 + wchunk * 16 + 8 * (p4 & 1);
  wb[1] = wb[0] + 2 * SLOT;
  wb[2] = wb[0] + 4 * SLOT;
#pragma unroll
  for (int t = 0; t < 4; ++t) { asm("" : "+v"(xb[0][t])); asm("" : "+v"(xb[1][t])); asm("" : "+v"(xb[2][t])); }
  asm("" : "+v"(wb[0])); asm("" : "+v"(wb[1])); asm("" : "+v"(wb[2]));

  bf16x8 xf[32], wk[3], wv[3];
  f32x16 accK = {}, accV = {};
  constexpr int VLAG = BR == 0 ? 8 : 0;
  auto rdv = [&](int s, int buf) LAM {
    const int sl = slot_of(s >> 2), o = (sl & 1) * SLOT + 16384 + 8192 + (s & 3) * 2048;
    wv[buf] = tr_frag(wb[sl >> 1], o, o + 512);
  };
  auto rd1 = [&](int s) LAM {
    if (s < 32) {
      const int sl = slot_of(s >> 2), t = s & 3;
      xf[s] = lds_frag(sl < 4 ? xb[sl >> 1][t] + (sl & 1) * SLOT : xb[2][t]);
      const int o = (sl & 1) * SLOT + 16384 + t * 2048;
      wk[s % 3] = tr_frag(wb[sl >> 1], o, o + 512);
      if (s < 32 - VLAG) rdv(s, s % 3);
    } else {
      rdv(s - VLAG, s % 3);
    }
  };
  WAIT_BAR(4);
  STAMP(15);
  rd1(0); rd1(1);
  SBAR();
  unsigned kpk[8];
  float zs0 = 0.f, zs1 = 0.f;
#pragma unroll
  for (int s = 0; s < 32 + VLAG; ++s) {
    const int cc = s >> 2;
    if ((s & 3) == 2 && cc < 7) {
      if (s == 2) WAIT_BAR_NL(4); else WAIT_BAR_NL(8);
      if (cc == 6 && wid == 0) {
        dma16(lds0 + L_GW, (lane * 16 + rot * 128) & 1023, rsG, 0);
        const unsigned e0 = WS_EO + rt * 128;
        const unsigned e1 = BR == 0 ? WS_EKV + quad * 128 : WS_EH + (rt * 8 + quad) * 128;
        const unsigned e2 = BR == 0 ? e1 : WS_EH + ((rt > 0 ? rt - 1 : 0) * 8 + quad) * 128;
        const unsigned eo = (lane < 2 ? e0 : lane < 4 ? e1 : e2) + (lane & 1) * 4;
        if (lane < 6) dma4(lds0 + L_EP, eo, rsWS);
        if (lane < 3) *(LAS unsigned*)(L + L_CNT + lane * 4) = 0;
      }
    }
    if (s == 27) {
      const char* a = ws;
      if (wid == 0) a = (const char*)woap + (lane & 7) * 65536;
      else if (wid == 1) a = (const char*)wobp + (lane & 7) * 65536;
      else if (wid == 2) a = ws + WS_O + (lane < 2 ? (rt * 2 + BR) * 131072 + lane * 65536 : (rt * 4 + (lane & 3)) * 65536 + (BR * 32 + quad * 4) * 1024);
      else if (wid == 3) a = ws + (BR == 0 ? WS_KVP + quad * 16 * KVP_BYTES + (lane & 1) * 65536 : WS_HALO + (rt * 8 + quad) * 4096);
      else if (wid == 4) a = outp + (rt * 128 + 64 * BR) * 1024 + (lane & 1) * 63 * 1024 + quad * 128;
      else if (wid == 5) a = ws + ((lane & 1) ? WS_FO + rt * 8192 : (BR == 0 ? WS_FKV + quad * 2048 : WS_FH + (rt * 8 + quad) * 256));
      if (lane < 8) touch(lds0 + L_SINK + wid * 32, a);
    }
    if (s < 2) { piece(2, 2 * s); piece(2, 2 * s + 1); }
    else if (s < 6) { piece(3 + ((s - 2) >> 1), 2 * (s & 1)); piece(3 + ((s - 2) >> 1), 2 * (s & 1) + 1); }
    else if (((s - 2) >> 2) + 4 < 10) piece(((s - 2) >> 2) + 4, (s - 2) & 3);
    if (s + 2 < 32 + VLAG) rd1(s + 2);
    if (s < 32) accK = BR == 0 ? MFMA(xf[s], wk[s % 3], accK) : MFMA(wk[s % 3], xf[s], accK);
    if (s < 32 - VLAG) accV = MFMA(xf[s], wv[s % 3], accV);
    if (s >= 32) {
      accV = MFMA(xf[s - VLAG], wv[s % 3], accV);
      kpk[s - 32] = phi_pk1(accK[2 * (s - 32)], accK[2 * (s - 32) + 1], zs0, zs1);
    }
    SBAR();
  }
  STAMP(2);
  bf16x8 Kf[2], Vf[2];
  if (BR == 0) {
    Kf[0] = __builtin_bit_cast(bf16x8, (u32x4){kpk[0], kpk[1], kpk[2], kpk[3]});
    Kf[1] = __builtin_bit_cast(bf16x8, (u32x4){kpk[4], kpk[5], kpk[6], kpk[7]});
  } else { Kf[0] = pack8(accK, 0); Kf[1] = pack8(accK, 8); }
  Vf[0] = pack8(accV, 0); Vf[1] = pack8(accV, 8);
  if (BR == 0) {
    f32x16 kv = {};
    kv = MFMA(Kf[0], Vf[0], kv);
    kv = MFMA(Kf[1], Vf[1], kv);
    const lptr pp = L + L_PART + wid * 2176;
    {
      const float zh = zs0 + zs1;
      auto sw = __builtin_amdgcn_permlane32_swap(as_u(zh), as_u(zh), false, false);
      if (h == 0) *(LAS float*)(pp + 2048 + i32 * 4) = as_f(sw[0]) + as_f(sw[1]);
    }
    const bool lo = i32 < 16;
    f32x4 u0, u1;
#pragma unroll
    for (int j = 0; j < 4; ++j) { u0[j] = sel(lo, kv[j], kv[8 + j]); u1[j] = sel(lo, kv[4 + j], kv[12 + j]); }
    *(LAS f32x4*)(pp + lane * 16) = u0;
    *(LAS f32x4*)(pp + 1024 + lane * 16) = u1;
  } else {
    if (sp == 0) {
      const lptr ex = L + L_PART + (c * 2 + p) * 1024;
      if (i32 >= 24) {
        *(LAS bf16x8*)(ex + h * 128 + (i32 - 24) * 16) = Kf[0];
        *(LAS bf16x8*)(ex + 256 + h * 128 + (i32 - 24) * 16) = Kf[1];
      }
      const u32x4 v1 = __builtin_bit_cast(u32x4, Vf[1]);
      *(LAS u32x2*)(ex + 512 + lane * 8) = (u32x2){v1[2], v1[3]};
    }
  }
  WAIT_BAR(1);
  STAMP(3);
  if (BR == 0) {
    if (wid == 3) {
      __builtin_amdgcn_s_setprio(3);
      f32x4 pa[5][4];
#pragma unroll
      for (int it = 0; it < 5; ++it) {
        const int u = it < 4 ? it * 64 + lane : 256 + (lane & 15);
        const int cc = it < 4 ? (it >> 1) : ((lane >> 3) & 1);
        const int o = it < 4 ? (u & 127) * 16 : 2048 + (lane & 7) * 16;
        const lptr s = L + L_PART + cc * 4 * 2176 + o;
#pragma unroll
        for (int r = 0; r < 4; ++r) pa[it][r] = *(LAS f32x4*)(s + r * 2176);
      }
      SBAR();
#pragma unroll
      for (int it = 0; it < 5; ++it) {
        f32x4 a;
#pragma unroll
        for (int j = 0; j < 4; ++j) a[j] = opq(opq(pa[it][0][j] + pa[it][1][j]) + opq(pa[it][2][j] + pa[it][3][j]));
        const unsigned go = WS_KVP + (quad * 16 + rt) * KVP_BYTES + (it < 4 ? it * 64 + lane : 256 + lane) * 16;
        if (it < 4 || lane < 16) __builtin_amdgcn_raw_buffer_store_b128(__builtin_bit_cast(u32x4, a), rsWS, go, 0, 16);
      }
      __builtin_amdgcn_s_setprio(0);
    }
  } else {
    if (sp == 1 && rt < 15) {
      const unsigned ex = WS_HALO + ((rt * 8 + quad) * 4 + c * 2 + p) * 1024;
      if (i32 >= 24) {
        __builtin_amdgcn_raw_buffer_store_b128(__builtin_bit_cast(u32x4, Kf[0]), rsWS, ex + h * 128 + (i32 - 24) * 16, 0, 16);
        __builtin_amdgcn_raw_buffer_store_b128(__builtin_bit_cast(u32x4, Kf[1]), rsWS, ex + 256 + h * 128 + (i32 - 24) * 16, 0, 16);
      }
      const u32x4 v1 = __builtin_bit_cast(u32x4, Vf[1]);
      __builtin_amdgcn_raw_buffer_store_b64((u32x2){v1[2], v1[3]}, rsWS, ex + 512 + lane * 8, 0, 16);
    }
  }
  SBAR();
  tagO = lds_tag(L + L_EP);
  const u64 tagP = lds_tag(L + L_EP + 8), tagG = lds_tag(L + L_EP + 16);
  if (BR == 0 && c == 1) __builtin_amdgcn_s_setprio(1);
  f32x16 accQ = {};
  {
    bf16x8 wq[5];
    auto rd2 = [&](int s) LAM {
      const lptr w = s < 16 ? wb[1] : wb[2];
      const int o = (s < 16 ? SLOT : -16 * 2048) + s * 2048;
      wq[s % 5] = tr_frag(w, o, o + 512);
    };
    rd2(0); rd2(1); rd2(2); rd2(3);
    SBAR();
#pragma unroll
    for (int s = 0; s < 32; ++s) {
      if (s + 4 < 32) rd2(s + 4);
      accQ = MFMA(wq[s % 5], xf[s], accQ);
      if (s >= 12 && s < 20) wo_piece(0, wid * 8 + (s - 12));
      if (s == PUBS) {
        if (BR == 0 ? (wid == 3) : (sp == 1 && rt < 15)) {
          WAIT_VM(0);
          if (lane == 0) flag_store(ws, BR == 0 ? WS_FKV + (quad * 16 + rt) * 128 : WS_FH + ((rt * 8 + quad) * 4 + c * 2 + p) * 64, tagP);
          STAMP(14);
        }
      }
      SBAR();
    }
  }
  bf16x8 Qf[2];
  if (BR == 0) { Qf[0] = phi8(accQ, 0); Qf[1] = phi8(accQ, 8); }
  else { Qf[0] = pack8(accQ, 0); Qf[1] = pack8(accQ, 8); }
  STAMP(4);
  asm volatile("s_waitcnt vmcnt(0) lgkmcnt(0)" ::: "memory");
  if (lane == 0) __hip_atomic_fetch_add((LAS unsigned*)(L + L_CNT), 1u, __ATOMIC_RELAXED, __HIP_MEMORY_SCOPE_WORKGROUP);
  STAMP(16);
  auto gate = [&](auto pre) LAM {
    f32x16 ga = {};
    bf16x8 gf[4];
    const lptr gp = L + L_GW + h * 16;
#pragma unroll
    for (int t = 0; t < 3; ++t) gf[t] = lds_frag(gp + t * 32);
    SBAR();
#pragma unroll
    for (int t = 0; t < 32; ++t) {
      pre(t);
      if (t + 3 < 32) gf[(t + 3) % 4] = lds_frag(gp + (t + 3) * 32);
      ga = MFMA(gf[t % 4], xf[t], ga);
      SBAR();
    }
    const float g = __builtin_amdgcn_rcpf(1.f + __builtin_amdgcn_exp2f(-LOG2E * ga[0]));
    if (h == 0) *(LAS float*)(L + L_G + (p * 32 + i32) * 4) = g;
  };
  const unsigned ofrag = WS_O + ((((rt * 2 + sp) * 2 + p) * 64) + BR * 32 + quad * 4 + 2 * c) * 1024 + lane * 16;
  if (BR == 0) {
    if (wid < 2) { gate([&](int) LAM {}); STAMP(20); }
    if (c == 0) {
      lds_wait(L + L_CNT, 8);
      STAMP(21);
      read_wo(wo, L, wid >> 1, lane);
      STAMP(22);
      lds_wait(L + L_UIS, 4);
      STAMP(17);
#pragma unroll
      for (int i = 0; i < 16; ++i) wo_piece(1, wid * 16 + i);
      STAMP(18);
    }
    if (c == 1) {
      poll_flags(ws, WS_FKV + quad * 16 * 128, 128, 16, tagG, lane);
      STAMP(5);
      const unsigned u = rq * 64 + lane;
      f32x4 a[16];
#pragma unroll
      for (int r = 0; r < 16; ++r)
        a[r] = __builtin_bit_cast(f32x4, __builtin_amdgcn_raw_buffer_load_b128(rsWS, WS_KVP + quad * 16 * KVP_BYTES + u * 16, r * KVP_BYTES, LD_AUX));
      f32x4 zv = __builtin_bit_cast(f32x4, __builtin_amdgcn_raw_buffer_load_b128(rsWS, WS_KVP + (quad * 16 + (lane >> 2)) * KVP_BYTES + (256 + rq * 4 + (lane & 3)) * 16, 0, LD_AUX));
      SBAR();
      if (lane == 0) __hip_atomic_fetch_add((LAS unsigned*)(L + L_UIS), 1u, __ATOMIC_RELAXED, __HIP_MEMORY_SCOPE_WORKGROUP);
      SBAR();
#pragma unroll
      for (int s = 1; s < 16; s *= 2)
#pragma unroll
        for (int r = 0; r < 16; r += 2 * s) a[r] += a[r + s];
      *(LAS f32x4*)(L + L_TOT + u * 16) = a[0];
#pragma unroll
      for (int j = 0; j < 4; ++j) zv[j] = sum_lanes_mod4(zv[j]);
      if (lane < 4) *(LAS f32x4*)(L + L_TOT + (256 + rq * 4 + lane) * 16) = zv;
      STAMP(23);
    }
    LBAR();
    STAMP(6);
    const f32x4 u0 = *(LAS f32x4*)(L + L_TOT + (c * 128 + lane) * 16), u1 = *(LAS f32x4*)(L + L_TOT + (c * 128 + 64 + lane) * 16);
    const lptr zp = L + L_TOT + 4096 + c * 128 + h * 16;
    const f32x4 z0 = *(LAS f32x4*)zp, z1 = *(LAS f32x4*)(zp + 32), z2 = *(LAS f32x4*)(zp + 64), z3 = *(LAS f32x4*)(zp + 96);
    const bool lo = i32 < 16;
    const u32x4 pu = {pk(u0[0], u0[1]), pk(u0[2], u0[3]), pk(u1[0], u1[1]), pk(u1[2], u1[3])};
    const u32x4 pz0 = {pk(z0[0], z0[1]), pk(z0[2], z0[3]), pk(z1[0], z1[1]), pk(z1[2], z1[3])};
    const u32x4 pz1 = {pk(z2[0], z2[1]), pk(z2[2], z2[3]), pk(z3[0], z3[1]), pk(z3[2], z3[3])};
    u32x4 a0, a1;
#pragma unroll
    for (int j = 0; j < 4; ++j) { a0[j] = lo ? pu[j] : pz0[j]; a1[j] = lo ? pz1[j] : pu[j]; }
    const f32x16 zero = {};
    const f32x16 d0 = MFMA(__builtin_bit_cast(bf16x8, a0), Qf[0], zero);
    const f32x16 d1 = MFMA(__builtin_bit_cast(bf16x8, a1), Qf[1], zero);
    const float s0 = __builtin_amdgcn_rcpf(d0[8] + 1e-6f), s1 = __builtin_amdgcn_rcpf(d1[0] + 1e-6f);
    const u32x4 o0 = {pk(d0[0] * s0, d0[1] * s0), pk(d0[2] * s0, d0[3] * s0), pk(d0[4] * s0, d0[5] * s0), pk(d0[6] * s0, d0[7] * s0)};
    const u32x4 o1 = {pk(d1[8] * s1, d1[9] * s1), pk(d1[10] * s1, d1[11] * s1), pk(d1[12] * s1, d1[13] * s1), pk(d1[14] * s1, d1[15] * s1)};
    __builtin_amdgcn_raw_buffer_store_b128(o0, rsWS, ofrag, 0, 16);
    __builtin_amdgcn_raw_buffer_store_b128(o1, rsWS, ofrag + 1024, 0, 16);
    if (rt == 0 && wid == 4 && lane == 0) flag_store(ws, WS_EKV + quad * 128, tagG);
  } else {
    bf16x8 Kp[2];
    u32x2 vp;
    if (sp == 1) {
      const lptr ex = L + L_PART + (c * 2 + p) * 1024;
      Kp[0] = lds_frag(ex + h * 128 + (i32 & 7) * 16);
      Kp[1] = lds_frag(ex + 256 + h * 128 + (i32 & 7) * 16);
      vp = *(LAS u32x2*)(ex + 512 + lane * 8);
    } else if (rt > 0) {
      poll_flags(ws, WS_FH + (((rt - 1) * 8 + quad) * 4 + c * 2 + p) * 64, 64, 1, tagG, lane);
      const unsigned ex = WS_HALO + (((rt - 1) * 8 + quad) * 4 + c * 2 + p) * 1024;
      Kp[0] = __builtin_bit_cast(bf16x8, __builtin_amdgcn_raw_buffer_load_b128(rsWS, ex + h * 128 + (i32 & 7) * 16, 0, LD_AUX));
      Kp[1] = __builtin_bit_cast(bf16x8, __builtin_amdgcn_raw_buffer_load_b128(rsWS, ex + 256 + h * 128 + (i32 & 7) * 16, 0, LD_AUX));
      vp = __builtin_amdgcn_raw_buffer_load_b64(rsWS, ex + 512 + lane * 8, 0, LD_AUX);
      if (wid == 0 && lane == 0) flag_store(ws, WS_EH + ((rt - 1) * 8 + quad) * 128, tagG);
    } else {
      Kp[0] = Kf[0]; Kp[1] = Kf[1]; vp = (u32x2){0u, 0u};
    }
    STAMP(5);
    STAMP(6);
    const bool noprev = (rt == 0 && sp == 0);
    f32x16 bias;
#pragma unroll
    for (int r = 0; r < 16; ++r) bias[r] = ((unsigned)(i32 - ((r & 3) + 8 * (r >> 2) + 4 * h)) <= 7u) ? 0.f : -1e30f;
    float bprev[4];
#pragma unroll
    for (int j = 0; j < 4; ++j) bprev[j] = (!noprev && (24 + 4 * h + j >= i32 + 25)) ? 0.f : -1e30f;
    constexpr float C1 = 0.25f * LOG2E;
    const unsigned ONE2 = 0x3F803F80u;
    const f32x16 zero = {};
#pragma unroll
    for (int hh = 0; hh < 2; ++hh) {
      f32x16 sc = MFMA(Kf[hh], Qf[hh], bias);
      const f32x16 sv = MFMA(Kp[hh], Qf[hh], zero);
      float sp4[4];
#pragma unroll
      for (int j = 0; j < 4; ++j) sp4[j] = sv[12 + j] + bprev[j];
      float m = __builtin_fmaxf(__builtin_fmaxf(sp4[0], sp4[1]), __builtin_fmaxf(sp4[2], sp4[3]));
#pragma unroll
      for (int r = 0; r < 16; r += 2) m = __builtin_fmaxf(__builtin_fmaxf(m, sc[r]), sc[r + 1]);
      m = swap_max(m);
      const float nm = -m * C1;
#pragma unroll
      for (int r = 0; r < 16; ++r) sc[r] = __builtin_amdgcn_exp2f(__builtin_fmaf(sc[r], C1, nm));
#pragma unroll
      for (int j = 0; j < 4; ++j) sp4[j] = __builtin_amdgcn_exp2f(__builtin_fmaf(sp4[j], C1, nm));
      const bf16x8 P0 = pack8(sc, 0), P1 = pack8(sc, 8);
      const u32x4 ppv = {0u, 0u, pk(sp4[0], sp4[1]), pk(sp4[2], sp4[3])};
      const bool mine = (i32 < 16) == (hh == 0);
      const u32x4 v0 = __builtin_bit_cast(u32x4, Vf[0]), v1 = __builtin_bit_cast(u32x4, Vf[1]);
      u32x4 a0, a1, ap;
#pragma unroll
      for (int j = 0; j < 4; ++j) { a0[j] = mine ? v0[j] : ONE2; a1[j] = mine ? v1[j] : ONE2; }
      ap = (u32x4){0u, 0u, mine ? vp[0] : ONE2, mine ? vp[1] : ONE2};
      f32x16 o = MFMA(__builtin_bit_cast(bf16x8, a0), P0, zero);
      o = MFMA(__builtin_bit_cast(bf16x8, a1), P1, o);
      o = MFMA(__builtin_bit_cast(bf16x8, ap), __builtin_bit_cast(bf16x8, ppv), o);
      const int nb = hh == 0 ? 0 : 8, db = hh == 0 ? 8 : 0;
      const float s = __builtin_amdgcn_rcpf(o[db]);
      const u32x4 ov = {pk(o[nb] * s, o[nb + 1] * s), pk(o[nb + 2] * s, o[nb + 3] * s), pk(o[nb + 4] * s, o[nb + 5] * s), pk(o[nb + 6] * s, o[nb + 7] * s)};
      __builtin_amdgcn_raw_buffer_store_b128(ov, rsWS, ofrag + hh * 1024, 0, 16);
    }
  }
  STAMP(7);
  WAIT_VM(0);
  if (lane == 0) flag_store(ws, WS_FO + (((((rt * 2 + sp) * 2 + p) * 2 + BR) * 8 + quad) * 2 + c) * 64, tagO);
  if (BR == 0 && c == 0 && lane == 0) __hip_atomic_fetch_add((LAS unsigned*)(L + L_WOB), 1u, __ATOMIC_RELAXED, __HIP_MEMORY_SCOPE_WORKGROUP);
  STAMP(8);
  __builtin_amdgcn_s_setprio(0);
  if (BR == 1 && c == 1) {
    lds_wait(L + L_CNT, 8);
#pragma unroll
    for (int i = 0; i < 16; ++i) wo_piece(1, rq * 16 + i);
    STAMP(18);
  }
  if (BR == 1 && wid >= 6) gate([&](int) LAM {});
}

__global__ __launch_bounds__(512, 2) void gated_mix_kernel(const __hip_bfloat16* __restrict__ x, const __hip_bfloat16* __restrict__ wA,
                                                           const __hip_bfloat16* __restrict__ woA, const __hip_bfloat16* __restrict__ wB,
                                                           const __hip_bfloat16* __restrict__ woB, const __hip_bfloat16* __restrict__ gw,
                                                           __hip_bfloat16* __restrict__ out, char* ws) {
  __shared__ __attribute__((aligned(1024))) char smem[LDS_BYTES];
  const int tid = threadIdx.x, lane = tid & 63, wid = __builtin_amdgcn_readfirstlane(tid >> 6);
  const int b = blockIdx.x, xcd = b & 7, loc = b >> 3;
  const int br = xcd & 1, rt = (xcd >> 1) * 4 + (loc >> 3), quad = loc & 7;
  unsigned vts = 0;
  STAMP(0);
  const rsrc_t rsX = __builtin_amdgcn_make_buffer_rsrc((void*)x, 0, 2048 * 512 * 2, 0x00020000);
  const rsrc_t rsW = __builtin_amdgcn_make_buffer_rsrc((void*)(br ? wB : wA), 0, 512 * 1536 * 2, 0x00020000);
  const rsrc_t rsWoA = __builtin_amdgcn_make_buffer_rsrc((void*)woA, 0, 512 * 512 * 2, 0x00020000);
  const rsrc_t rsWoB = __builtin_amdgcn_make_buffer_rsrc((void*)woB, 0, 512 * 512 * 2, 0x00020000);
  const rsrc_t rsG = __builtin_amdgcn_make_buffer_rsrc((void*)gw, 0, 1024, 0x00020000);
  const rsrc_t rsWS = __builtin_amdgcn_make_buffer_rsrc((void*)ws, 0, WS_USED, 0x00020000);
  const lptr L = (lptr)smem;
  const unsigned lds0 = (unsigned)(uintptr_t)L;

  const int l8 = lane >> 3, c8 = lane & 7;
  const int xrow = rt * 128 + 64 * (wid >> 2) + ((wid >> 1) & 1) + 2 * (16 * (wid & 1) + l8);
  const unsigned vX0 = xrow * 1024 + ((c8 ^ (lane >> 4)) << 4);
  const unsigned vX1 = (xrow + 16) * 1024 + ((c8 ^ (4 + (lane >> 4))) << 4);
  const int wsw = (c8 ^ (((lane >> 4) & 1) << 2)) << 4;
  const unsigned vW = (8 * wid + l8) * 3072 + quad * 128 + wsw;
  const unsigned vWo = l8 * 1024 + quad * 128 + wsw;

  asm volatile("s_nop 4" ::: "memory");
#pragma unroll
  for (int f = 0; f < 2; ++f) {
    const unsigned sl = lds0 + f * SLOT;
    const unsigned kc = (f + ROT_OF(quad, rt)) & 7;
    dma16(sl + wid * 2048, vX0, rsX, kc * 128);
    dma16(sl + wid * 2048 + 1024, vX1, rsX, kc * 128);
    dma16(sl + 16384 + wid * 1024, vW, rsW, kc * 196608 + 1024);
    dma16(sl + 24576 + wid * 1024, vW, rsW, kc * 196608 + 2048);
    if (f < 1) asm volatile("s_barrier" ::: "memory");
  }
  STAMP(1);
  u64 tagO;
  bf16x8 wo[32];
  if (br == 0) phase1<0>(smem, rsX, rsW, rsWoA, rsWoB, rsG, rsWS, ws, rt, quad, wid, lane, vX0, vX1, vW, vWo, vts, tagO, wo, (const char*)out, woA, woB);
  else phase1<1>(smem, rsX, rsW, rsWoA, rsWoB, rsG, rsWS, ws, rt, quad, wid, lane, vX0, vX1, vW, vWo, vts, tagO, wo, (const char*)out, woA, woB);

  const int pt = wid & 1, kq = (wid >> 2) * 2 + ((wid >> 1) & 1);
  const int i32 = lane & 31, h = lane >> 5;
  const unsigned fwait = WS_FO + (((((rt * 2 + br) * 2 + pt) * 2 + (kq >> 1)) * 8 + (kq & 1) * 4) * 2) * 64;
  const unsigned ob = WS_O + ((((rt * 2 + br) * 2 + pt) * 64) + kq * 16) * 1024 + lane * 16;
  bf16x8 of[16];
  auto load_o = [&]() LAM {
#pragma unroll
    for (int i = 0; i < 16; ++i) of[i] = __builtin_bit_cast(bf16x8, __builtin_amdgcn_raw_buffer_load_b128(rsWS, ob, i * 1024, LD_AUX));
  };
  bool ready = false;
  if (!(br == 0 && kq < 2)) {
    u64 v0 = tagO;
    if (lane < 8) v0 = __hip_atomic_load((gu64*)(ws + fwait + lane * 64), __ATOMIC_RELAXED, __HIP_MEMORY_SCOPE_AGENT);
    ready = __builtin_amdgcn_ballot_w64(v0 != tagO) == 0;
    WAIT_VM(0);
    if (br == 1 && kq >= 2 && lane == 0) __hip_atomic_fetch_add((LAS unsigned*)(L + L_WOB), 1u, __ATOMIC_RELAXED, __HIP_MEMORY_SCOPE_WORKGROUP);
    STAMP(26);
    if (ready) load_o();
    if (kq >= 2) lds_wait(L + L_WOB, 4); else lds_wait(L + L_CNT, 8);
    STAMP(19);
    read_wo(wo, L, kq, lane);
  }
  STAMP(9);
  if (!ready) {
    poll_flags(ws, fwait, 64, 8, tagO, lane);
    load_o();
  }
  STAMP(10);
  SBAR();
  f32x16 a0 = {}, a1 = {};
#pragma unroll
  for (int i = 0; i < 16; ++i) { a0 = MFMA(wo[2 * i], of[i], a0); a1 = MFMA(wo[2 * i + 1], of[i], a1); }
  STAMP(11);
  LBAR();
  STAMP(24);
  {
    const lptr pr = L + L_RED + (kq * 64 + pt * 32 + i32) * PROW + h * 16;
#pragma unroll
    for (int g4 = 0; g4 < 4; ++g4) {
      *(LAS f32x4*)(pr + g4 * 32) = (f32x4){a0[4 * g4], a0[4 * g4 + 1], a0[4 * g4 + 2], a0[4 * g4 + 3]};
      *(LAS f32x4*)(pr + 128 + g4 * 32) = (f32x4){a1[4 * g4], a1[4 * g4 + 1], a1[4 * g4 + 2], a1[4 * g4 + 3]};
    }
  }
  LBAR();
  STAMP(13);
  {
    const int pos = tid >> 3, cg = tid & 7;
    const lptr pr = L + L_RED + pos * PROW + cg * 32;
    const float g = *(LAS float*)(L + L_G + pos * 4), gb = 1.f - g;
    const f32x4 s0 = (*(LAS f32x4*)pr + *(LAS f32x4*)(pr + 64 * PROW)) * g + (*(LAS f32x4*)(pr + 128 * PROW) + *(LAS f32x4*)(pr + 192 * PROW)) * gb;
    const f32x4 s1 = (*(LAS f32x4*)(pr + 16) + *(LAS f32x4*)(pr + 64 * PROW + 16)) * g + (*(LAS f32x4*)(pr + 128 * PROW + 16) + *(LAS f32x4*)(pr + 192 * PROW + 16)) * gb;
    const int row = rt * 128 + 64 * br + 2 * (pos & 31) + (pos >> 5);
    const u32x4 ov = {pk(s0[0], s0[1]), pk(s0[2], s0[3]), pk(s1[0], s1[1]), pk(s1[2], s1[3])};
    __builtin_amdgcn_raw_buffer_store_b128(ov, __builtin_amdgcn_make_buffer_rsrc((void*)out, 0, 2048 * 1024, 0x00020000), row * 1024 + quad * 128 + cg * 16, 0, 0);
  }
  if (quad == 0 && br == 0 && tid == 0) flag_store(ws, WS_EO + rt * 128, tagO);
  STAMP(12);
  TSDUMP();
}

#if NOPK && defined(__HIP_DEVICE_COMPILE__)
#pragma clang attribute pop
#endif

extern "C" void kernel_launch(void* const* d_in, const int* in_sizes, int n_in, void* d_out, int out_size, void* d_ws, size_t ws_size,
                              hipStream_t stream) {
  gated_mix_kernel<<<256, 512, 0, stream>>>((const __hip_bfloat16*)d_in[0], (const __hip_bfloat16*)d_in[1], (const __hip_bfloat16*)d_in[2],
                                            (const __hip_bfloat16*)d_in[4], (const __hip_bfloat16*)d_in[5], (const __hip_bfloat16*)d_in[6],
                                            (__hip_bfloat16*)d_out, (char*)d_ws);
}
```

```cpp
#include <hip/hip_runtime.h>
#include <hip/hip_bf16.h>
#include <stdint.h>

typedef __bf16 bf16x2 __attribute__((ext_vector_type(2)));
typedef __bf16 bf16x8 __attribute__((ext_vector_type(8)));
typedef short s16x4 __attribute__((ext_vector_type(4)));
typedef float f32x2 __attribute__((ext_vector_type(2)));
typedef float f32x4 __attribute__((ext_vector_type(4)));
typedef float f32x16 __attribute__((ext_vector_type(16)));
typedef unsigned u32x2 __attribute__((ext_vector_type(2)));
typedef unsigned u32x4 __attribute__((ext_vector_type(4)));
typedef unsigned long long u64;
#define LAS __attribute__((address_space(3)))
#define GAS __attribute__((address_space(1)))
#define DI __device__ __forceinline__
typedef LAS char* lptr;
typedef GAS u64 gu64;
typedef __amdgpu_buffer_rsrc_t rsrc_t;

constexpr unsigned WS_EO = 0;
constexpr unsigned WS_EKV = 2048;
constexpr unsigned WS_EH = 4096;
constexpr unsigned WS_FKV = 32768;
constexpr unsigned WS_FH = 65536;
constexpr unsigned WS_FO = 131072;
constexpr unsigned WS_KVP = 262144 + 65536;
constexpr unsigned KVP_BYTES = 4352;
constexpr unsigned WS_HALO = 1048576;
constexpr unsigned WS_O = 2097152;
constexpr unsigned WS_STAMP = 8u << 20;
constexpr unsigned WS_USED = 16u << 20;

constexpr int SLOT = 32768;
constexpr int L_GW = 0;
constexpr int L_G = L_GW + 1024;
constexpr int L_EP = L_GW + 1536;
constexpr int L_CNT = L_GW + 1568;
constexpr int L_WOB = L_GW + 1572;
constexpr int L_UIS = L_GW + 1576;
constexpr int L_PART = L_GW + 2048;
constexpr int L_TOT = L_PART + 8 * 2176;
constexpr int L_RED = SLOT;
constexpr int LDS_BYTES = 163840;
constexpr int PROW = 272;
#define slot_of(f) ((f) < 8 ? (f) % 5 : (f) == 8 ? 3 : 4)

constexpr float LOG2E = 1.4426950408889634f;
constexpr unsigned SPIN_CAP = 1u << 16;

#define MFMA(a, b, c) __builtin_amdgcn_mfma_f32_32x32x16_bf16((a), (b), (c), 0, 0, 0)
#define WAIT_BAR(N) asm volatile("s_waitcnt vmcnt(" #N ") lgkmcnt(0)\n\ts_barrier" ::: "memory")
#define WAIT_BAR_NL(N) asm volatile("s_waitcnt vmcnt(" #N ")\n\ts_barrier" ::: "memory")
#define WAIT_VM(N) asm volatile("s_waitcnt vmcnt(" #N ")" ::: "memory")
#define ROTM 6
#define ROT_OF(q, r) ((unsigned)(ROTM == 0 ? 0 : ROTM == 1 ? (q) : ROTM == 2 ? (q) + 2 * (r) : ROTM == 3 ? 2 * (r) : ROTM == 4 ? (r) : ROTM == 5 ? (r) + (q) : (r) + 4 * ((q) & 1)))
#define PUBS 7
#define SBAR() __builtin_amdgcn_sched_barrier(0)
#define LBAR() asm volatile("s_waitcnt lgkmcnt(0)\n\ts_barrier" ::: "memory")

#define STAMP(i) do {} while (0)
#define TSDUMP() do {} while (0)

#define NOPK 0
#if NOPK && defined(__HIP_DEVICE_COMPILE__)
#define LAM __attribute__((target("no-packed-fp32-ops"), always_inline))
#else
#define LAM __attribute__((always_inline))
#endif
#if NOPK && defined(__HIP_DEVICE_COMPILE__)
#pragma clang attribute push(__attribute__((target("no-packed-fp32-ops"))), apply_to = function)
#endif
DI float as_f(unsigned u) { return __builtin_bit_cast(float, u); }
DI unsigned as_u(float f) { return __builtin_bit_cast(unsigned, f); }
DI unsigned pk(float a, float b) { return __builtin_bit_cast(unsigned, __builtin_convertvector((f32x2){a, b}, bf16x2)); }
DI bf16x8 pack8(const f32x16& a, int o) {
  u32x4 r = {pk(a[o], a[o + 1]), pk(a[o + 2], a[o + 3]), pk(a[o + 4], a[o + 5]), pk(a[o + 6], a[o + 7])};
  return __builtin_bit_cast(bf16x8, r);
}
DI unsigned phi_pk(float a, float b, f32x2& zsum) {
  const f32x2 t = {a, b};
  const f32x2 u = t * (f32x2){LOG2E, LOG2E}, w = t + (f32x2){1.f, 1.f};
  const float e0 = __builtin_amdgcn_fmed3f(__builtin_amdgcn_exp2f(u[0]), 0.f, 1.f), e1 = __builtin_amdgcn_fmed3f(__builtin_amdgcn_exp2f(u[1]), 0.f, 1.f);
  const f32x2 r = (f32x2){__builtin_fmaxf(w[0], e0), __builtin_fmaxf(w[1], e1)} + (f32x2){1e-6f, 1e-6f};
  zsum += r;
  return pk(r[0], r[1]);
}
DI float opq(float x) { asm("" : "+v"(x)); return x; }
DI unsigned phi_pk1(float a, float b, float& z0, float& z1) {
  const float e0 = __builtin_amdgcn_fmed3f(__builtin_amdgcn_exp2f(a * LOG2E), 0.f, 1.f), e1 = __builtin_amdgcn_fmed3f(__builtin_amdgcn_exp2f(b * LOG2E), 0.f, 1.f);
  const float r0 = opq(__builtin_fmaxf(a + 1.f, e0) + 1e-6f), r1 = __builtin_fmaxf(b + 1.f, e1) + 1e-6f;
  z0 = opq(z0 + r0); z1 += r1;
  return pk(r0, r1);
}
DI bf16x8 phi8(const f32x16& a, int o) {
  f32x2 unused = {};
  u32x4 r = {phi_pk(a[o], a[o + 1], unused), phi_pk(a[o + 2], a[o + 3], unused), phi_pk(a[o + 4], a[o + 5], unused), phi_pk(a[o + 6], a[o + 7], unused)};
  return __builtin_bit_cast(bf16x8, r);
}
DI void dma16(unsigned lds, unsigned voff, rsrc_t rs, unsigned soff) {
  asm volatile("s_mov_b32 m0, %0\n\ts_nop 0\n\tbuffer_load_dwordx4 %1, %2, %3 offen lds" ::"s"(lds), "v"(voff), "s"(rs), "s"(soff) : "memory");
}
DI void dma4(unsigned lds, unsigned voff, rsrc_t rs) {
  asm volatile("s_mov_b32 m0, %0\n\ts_nop 0\n\tbuffer_load_dword %1, %2, 0 offen sc1 lds" ::"s"(lds), "v"(voff), "s"(rs) : "memory");
}
DI u64 lds_tag(lptr p) {
  const u32x2 v = *(const LAS u32x2*)p;
  return (((u64)(unsigned)__builtin_amdgcn_readfirstlane(v[1]) << 32) | (unsigned)__builtin_amdgcn_readfirstlane(v[0])) + 1;
}
DI bf16x8 lds_frag(lptr p) { return *(const LAS bf16x8*)p; }
DI bf16x8 tr_frag(lptr p, int o0, int o1) {
  const s16x4 lo = __builtin_bit_cast(s16x4, __builtin_amdgcn_ds_read_tr16_b64_v4i16((LAS s16x4*)(p + o0)));
  const s16x4 hi = __builtin_bit_cast(s16x4, __builtin_amdgcn_ds_read_tr16_b64_v4i16((LAS s16x4*)(p + o1)));
  return __builtin_bit_cast(bf16x8, __builtin_shufflevector(lo, hi, 0, 1, 2, 3, 4, 5, 6, 7));
}
DI float sel(bool c, float a, float b) { asm("" : "+v"(a), "+v"(b)); return c ? a : b; }
#define ACQ 0
constexpr int LD_AUX = ACQ ? 0 : 16;
DI void acquire() { if (ACQ) __builtin_amdgcn_fence(__ATOMIC_ACQUIRE, "agent"); }
DI void flag_store(char* ws, unsigned off, u64 v) { __hip_atomic_store((gu64*)(ws + off), v, __ATOMIC_RELAXED, __HIP_MEMORY_SCOPE_AGENT); }
DI void poll_flags(char* ws, unsigned off0, unsigned stride, int n, u64 tag, int lane) {
  gu64* p = (gu64*)(ws + off0 + (unsigned)lane * stride);
#pragma unroll 1
  for (unsigned spins = 0; spins < SPIN_CAP; ++spins) {
    u64 v = tag;
    if (lane < n) v = __hip_atomic_load(p, __ATOMIC_RELAXED, __HIP_MEMORY_SCOPE_AGENT);
    if (__builtin_amdgcn_ballot_w64(v != tag) == 0) break;
    __builtin_amdgcn_s_sleep(1);
  }
  __builtin_amdgcn_fence(__ATOMIC_ACQUIRE, "wavefront");
  asm volatile("" ::: "memory");
  acquire();
}
DI void lds_wait(lptr p, unsigned want) {
#pragma unroll 1
  for (unsigned spins = 0; spins < SPIN_CAP; ++spins) {
    if ((unsigned)__builtin_amdgcn_readfirstlane(__hip_atomic_load((LAS unsigned*)p, __ATOMIC_RELAXED, __HIP_MEMORY_SCOPE_WORKGROUP)) == want) break;
    __builtin_amdgcn_s_sleep(2);
  }
  asm volatile("" ::: "memory");
}
DI float sum_lanes_mod4(float x) {
  x += as_f(__builtin_amdgcn_update_dpp(0u, as_u(x), 0x124, 0xf, 0xf, false));
  x += as_f(__builtin_amdgcn_update_dpp(0u, as_u(x), 0x128, 0xf, 0xf, false));
  auto a = __builtin_amdgcn_permlane16_swap(as_u(x), as_u(x), false, false);
  x = as_f(a[0]) + as_f(a[1]);
  auto b = __builtin_amdgcn_permlane32_swap(as_u(x), as_u(x), false, false);
  return as_f(b[0]) + as_f(b[1]);
}
DI float swap_max(float m) {
  auto r = __builtin_amdgcn_permlane32_swap(as_u(m), as_u(m), false, false);
  return __builtin_fmaxf(as_f(r[0]), as_f(r[1]));
}

DI void read_wo(bf16x8 (&wo)[32], lptr L, int kq, int lane) {
  const int h = lane >> 5, q4 = (lane & 15) >> 2, p4 = lane & 3, nsub = (lane >> 4) & 1;
  const lptr base = L + (kq + 1) * SLOT + (4 * h + q4) * 128 + 8 * (p4 & 1);
  const int sw = ((q4 >> 1) & 1) << 2;
  const lptr b0 = base + ((2 * nsub + (p4 >> 1)) ^ sw) * 16, b1 = base + ((4 + 2 * nsub + (p4 >> 1)) ^ sw) * 16;
#pragma unroll
  for (int i = 0; i < 16; ++i) {
    wo[2 * i] = tr_frag(b0, i * 2048, i * 2048 + 1024);
    wo[2 * i + 1] = tr_frag(b1, i * 2048, i * 2048 + 1024);
  }
}

template <int BR>
DI void phase1(char* smem_generic, const rsrc_t rsX, const rsrc_t rsW, const rsrc_t rsWoA, const rsrc_t rsWoB, const rsrc_t rsG, const rsrc_t rsWS, char* ws,
               const int rt, const int quad, const int wid, const int lane, const unsigned vX0, const unsigned vX1, const unsigned vW,
               const unsigned vWo, unsigned& vts, u64& tagO, bf16x8 (&wo)[32]) {
  const lptr L = (lptr)smem_generic;
  const unsigned lds0 = (unsigned)(uintptr_t)L;
  const int c = wid >> 2, rq = wid & 3, p = rq & 1, sp = rq >> 1;
  const int i32 = lane & 31, h = lane >> 5;
  const unsigned pieceX = lds0 + wid * 2048, pieceW = lds0 + wid * 1024;
  const unsigned rot = ROT_OF(quad, rt);

  auto piece = [&](int f, int i) LAM {
    const unsigned sl = (unsigned)slot_of(f) * SLOT;
    if (f < 8) {
      const unsigned kc = (f + rot) & 7;
      if (i == 0) dma16(pieceX + sl, vX0, rsX, kc * 128);
      else if (i == 1) dma16(pieceX + sl + 1024, vX1, rsX, kc * 128);
      else dma16(pieceW + sl + 16384 + (i - 2) * 8192, vW, rsW, kc * 196608 + (i - 1) * 1024);
    } else {
      dma16(pieceW + sl + i * 8192, vW, rsW, (((f - 8) * 4 + i + rot) & 7) * 196608);
    }
  };
  auto wo_piece = [&](int which, unsigned q) LAM { dma16(lds0 + (which ? 3 : 1) * SLOT + q * 1024, vWo, which ? rsWoB : rsWoA, q * 8192); };

  const int xs = h ^ ((i32 >> 1) & 7);
  const int q4 = (lane & 15) >> 2, p4 = lane & 3, nsub = (lane >> 4) & 1;
  const int wchunk = (4 * c + 2 * nsub + (p4 >> 1)) ^ (((q4 >> 1) & 1) << 2);
  lptr xb[3][4], wb[3];
#pragma unroll
  for (int t = 0; t < 4; ++t) {
    xb[0][t] = L + (32 * rq + i32) * 128 + ((xs ^ (2 * t)) << 4);
    xb[1][t] = xb[0][t] + 2 * SLOT;
    xb[2][t] = xb[0][t] + 4 * SLOT;
  }
  wb[0] = L + (8 * h + q4) * 128 + wchunk * 16 + 8 * (p4 & 1);
  wb[1] = wb[0] + 2 * SLOT;
  wb[2] = wb[0] + 4 * SLOT;
#pragma unroll
  for (int t = 0; t < 4; ++t) { asm("" : "+v"(xb[0][t])); asm("" : "+v"(xb[1][t])); asm("" : "+v"(xb[2][t])); }
  asm("" : "+v"(wb[0])); asm("" : "+v"(wb[1])); asm("" : "+v"(wb[2]));

  bf16x8 xf[32], wk[3], wv[3];
  f32x16 accK = {}, accV = {};
  constexpr int VLAG = BR == 0 ? 8 : 0;
  auto rdv = [&](int s, int buf) LAM {
    const int sl = slot_of(s >> 2), o = (sl & 1) * SLOT + 16384 + 8192 + (s & 3) * 2048;
    wv[buf] = tr_frag(wb[sl >> 1], o, o + 512);
  };
  auto rd1 = [&](int s) LAM {
    if (s < 32) {
      const int sl = slot_of(s >> 2), t = s & 3;
      xf[s] = lds_frag(sl < 4 ? xb[sl >> 1][t] + (sl & 1) * SLOT : xb[2][t]);
      const int o = (sl & 1) * SLOT + 16384 + t * 2048;
      wk[s % 3] = tr_frag(wb[sl >> 1], o, o + 512);
      if (s < 32 - VLAG) rdv(s, s % 3);
    } else {
      rdv(s - VLAG, s % 3);
    }
  };
  WAIT_BAR(4);
  STAMP(15);
  rd1(0); rd1(1);
  SBAR();
  unsigned kpk[8];
  float zs0 = 0.f, zs1 = 0.f;
#pragma unroll
  for (int s = 0; s < 32 + VLAG; ++s) {
    const int cc = s >> 2;
    if ((s & 3) == 2 && cc < 7) {
      if (s == 2) WAIT_BAR_NL(4); else WAIT_BAR_NL(8);
      if (cc == 6 && wid == 0) {
        dma16(lds0 + L_GW, (lane * 16 + rot * 128) & 1023, rsG, 0);
        const unsigned e0 = WS_EO + rt * 128;
        const unsigned e1 = BR == 0 ? WS_EKV + quad * 128 : WS_EH + (rt * 8 + quad) * 128;
        const unsigned e2 = BR == 0 ? e1 : WS_EH + ((rt > 0 ? rt - 1 : 0) * 8 + quad) * 128;
        const unsigned eo = (lane < 2 ? e0 : lane < 4 ? e1 : e2) + (lane & 1) * 4;
        if (lane < 6) dma4(lds0 + L_EP, eo, rsWS);
        if (lane < 3) *(LAS unsigned*)(L + L_CNT + lane * 4) = 0;
      }
    }
    if (s < 2) { piece(2, 2 * s); piece(2, 2 * s + 1); }
    else if (s < 6) { piece(3 + ((s - 2) >> 1), 2 * (s & 1)); piece(3 + ((s - 2) >> 1), 2 * (s & 1) + 1); }
    else if (((s - 2) >> 2) + 4 < 10) piece(((s - 2) >> 2) + 4, (s - 2) & 3);
    if (s + 2 < 32 + VLAG) rd1(s + 2);
    if (s < 32) accK = BR == 0 ? MFMA(xf[s], wk[s % 3], accK) : MFMA(wk[s % 3], xf[s], accK);
    if (s < 32 - VLAG) accV = MFMA(xf[s], wv[s % 3], accV);
    if (s >= 32) {
      accV = MFMA(xf[s - VLAG], wv[s % 3], accV);
      kpk[s - 32] = phi_pk1(accK[2 * (s - 32)], accK[2 * (s - 32) + 1], zs0, zs1);
    }
    SBAR();
  }
  STAMP(2);
  bf16x8 Kf[2], Vf[2];
  if (BR == 0) {
    Kf[0] = __builtin_bit_cast(bf16x8, (u32x4){kpk[0], kpk[1], kpk[2], kpk[3]});
    Kf[1] = __builtin_bit_cast(bf16x8, (u32x4){kpk[4], kpk[5], kpk[6], kpk[7]});
  } else { Kf[0] = pack8(accK, 0); Kf[1] = pack8(accK, 8); }
  Vf[0] = pack8(accV, 0); Vf[1] = pack8(accV, 8);
  if (BR == 0) {
    f32x16 kv = {};
    kv = MFMA(Kf[0], Vf[0], kv);
    kv = MFMA(Kf[1], Vf[1], kv);
    const lptr pp = L + L_PART + wid * 2176;
    {
      const float zh = zs0 + zs1;
      auto sw = __builtin_amdgcn_permlane32_swap(as_u(zh), as_u(zh), false, false);
      if (h == 0) *(LAS float*)(pp + 2048 + i32 * 4) = as_f(sw[0]) + as_f(sw[1]);
    }
    const bool lo = i32 < 16;
    f32x4 u0, u1;
#pragma unroll
    for (int j = 0; j < 4; ++j) { u0[j] = sel(lo, kv[j], kv[8 + j]); u1[j] = sel(lo, kv[4 + j], kv[12 + j]); }
    *(LAS f32x4*)(pp + lane * 16) = u0;
    *(LAS f32x4*)(pp + 1024 + lane * 16) = u1;
  } else {
    if (sp == 0) {
      const lptr ex = L + L_PART + (c * 2 + p) * 1024;
      if (i32 >= 24) {
        *(LAS bf16x8*)(ex + h * 128 + (i32 - 24) * 16) = Kf[0];
        *(LAS bf16x8*)(ex + 256 + h * 128 + (i32 - 24) * 16) = Kf[1];
      }
      const u32x4 v1 = __builtin_bit_cast(u32x4, Vf[1]);
      *(LAS u32x2*)(ex + 512 + lane * 8) = (u32x2){v1[2], v1[3]};
    }
  }
  WAIT_BAR(0);
  STAMP(3);
  if (BR == 0) {
    if (wid == 3) {
      __builtin_amdgcn_s_setprio(3);
      f32x4 pa[5][4];
#pragma unroll
      for (int it = 0; it < 5; ++it) {
        const int u = it < 4 ? it * 64 + lane : 256 + (lane & 15);
        const int cc = it < 4 ? (it >> 1) : ((lane >> 3) & 1);
        const int o = it < 4 ? (u & 127) * 16 : 2048 + (lane & 7) * 16;
        const lptr s = L + L_PART + cc * 4 * 2176 + o;
#pragma unroll
        for (int r = 0; r < 4; ++r) pa[it][r] = *(LAS f32x4*)(s + r * 2176);
      }
      SBAR();
#pragma unroll
      for (int it = 0; it < 5; ++it) {
        f32x4 a;
#pragma unroll
        for (int j = 0; j < 4; ++j) a[j] = opq(opq(pa[it][0][j] + pa[it][1][j]) + opq(pa[it][2][j] + pa[it][3][j]));
        const unsigned go = WS_KVP + (quad * 16 + rt) * KVP_BYTES + (it < 4 ? it * 64 + lane : 256 + lane) * 16;
        if (it < 4 || lane < 16) __builtin_amdgcn_raw_buffer_store_b128(__builtin_bit_cast(u32x4, a), rsWS, go, 0, 16);
      }
      __builtin_amdgcn_s_setprio(0);
    }
  } else {
    if (sp == 1 && rt < 15) {
      const unsigned ex = WS_HALO + ((rt * 8 + quad) * 4 + c * 2 + p) * 1024;
      if (i32 >= 24) {
        __builtin_amdgcn_raw_buffer_store_b128(__builtin_bit_cast(u32x4, Kf[0]), rsWS, ex + h * 128 + (i32 - 24) * 16, 0, 16);
        __builtin_amdgcn_raw_buffer_store_b128(__builtin_bit_cast(u32x4, Kf[1]), rsWS, ex + 256 + h * 128 + (i32 - 24) * 16, 0, 16);
      }
      const u32x4 v1 = __builtin_bit_cast(u32x4, Vf[1]);
      __builtin_amdgcn_raw_buffer_store_b64((u32x2){v1[2], v1[3]}, rsWS, ex + 512 + lane * 8, 0, 16);
    }
  }
  SBAR();
  tagO = lds_tag(L + L_EP);
  const u64 tagP = lds_tag(L + L_EP + 8), tagG = lds_tag(L + L_EP + 16);
  if (BR == 0 && c == 1) __builtin_amdgcn_s_setprio(1);
  f32x16 accQ = {};
  {
    bf16x8 wq[5];
    auto rd2 = [&](int s) LAM {
      const lptr w = s < 16 ? wb[1] : wb[2];
      const int o = (s < 16 ? SLOT : -16 * 2048) + s * 2048;
      wq[s % 5] = tr_frag(w, o, o + 512);
    };
    rd2(0); rd2(1); rd2(2); rd2(3);
    SBAR();
#pragma unroll
    for (int s = 0; s < 32; ++s) {
      if (s + 4 < 32) rd2(s + 4);
      accQ = MFMA(wq[s % 5], xf[s], accQ);
      if (s >= 12 && s < 20) wo_piece(0, wid * 8 + (s - 12));
      if (s == PUBS) {
        if (BR == 0 ? (wid == 3) : (sp == 1 && rt < 15)) {
          WAIT_VM(0);
          if (lane == 0) flag_store(ws, BR == 0 ? WS_FKV + (quad * 16 + rt) * 128 : WS_FH + ((rt * 8 + quad) * 4 + c * 2 + p) * 64, tagP);
          STAMP(14);
        }
      }
      SBAR();
    }
  }
  bf16x8 Qf[2];
  if (BR == 0) { Qf[0] = phi8(accQ, 0); Qf[1] = phi8(accQ, 8); }
  else { Qf[0] = pack8(accQ, 0); Qf[1] = pack8(accQ, 8); }
  STAMP(4);
  asm volatile("s_waitcnt vmcnt(0) lgkmcnt(0)" ::: "memory");
  if (lane == 0) __hip_atomic_fetch_add((LAS unsigned*)(L + L_CNT), 1u, __ATOMIC_RELAXED, __HIP_MEMORY_SCOPE_WORKGROUP);
  STAMP(16);
  auto gate = [&](auto pre) LAM {
    f32x16 ga = {};
    bf16x8 gf[4];
    const lptr gp = L + L_GW + h * 16;
#pragma unroll
    for (int t = 0; t < 3; ++t) gf[t] = lds_frag(gp + t * 32);
    SBAR();
#pragma unroll
    for (int t = 0; t < 32; ++t) {
      pre(t);
      if (t + 3 < 32) gf[(t + 3) % 4] = lds_frag(gp + (t + 3) * 32);
      ga = MFMA(gf[t % 4], xf[t], ga);
      SBAR();
    }
    const float g = __builtin_amdgcn_rcpf(1.f + __builtin_amdgcn_exp2f(-LOG2E * ga[0]));
    if (h == 0) *(LAS float*)(L + L_G + (p * 32 + i32) * 4) = g;
  };
  const unsigned ofrag = WS_O + ((((rt * 2 + sp) * 2 + p) * 64) + BR * 32 + quad * 4 + 2 * c) * 1024 + lane * 16;
  if (BR == 0) {
    if (wid < 2) { gate([&](int) LAM {}); STAMP(20); }
    if (c == 0) {
      lds_wait(L + L_CNT, 8);
      STAMP(21);
      read_wo(wo, L, wid >> 1, lane);
      STAMP(22);
      lds_wait(L + L_UIS, 4);
      STAMP(17);
#pragma unroll
      for (int i = 0; i < 16; ++i) wo_piece(1, wid * 16 + i);
      STAMP(18);
    }
    if (c == 1) {
      poll_flags(ws, WS_FKV + quad * 16 * 128, 128, 16, tagG, lane);
      STAMP(5);
      const unsigned u = rq * 64 + lane;
      f32x4 a[16];
#pragma unroll
      for (int r = 0; r < 16; ++r)
        a[r] = __builtin_bit_cast(f32x4, __builtin_amdgcn_raw_buffer_load_b128(rsWS, WS_KVP + quad * 16 * KVP_BYTES + u * 16, r * KVP_BYTES, LD_AUX));
      f32x4 zv = __builtin_bit_cast(f32x4, __builtin_amdgcn_raw_buffer_load_b128(rsWS, WS_KVP + (quad * 16 + (lane >> 2)) * KVP_BYTES + (256 + rq * 4 + (lane & 3)) * 16, 0, LD_AUX));
      SBAR();
      if (lane == 0) __hip_atomic_fetch_add((LAS unsigned*)(L + L_UIS), 1u, __ATOMIC_RELAXED, __HIP_MEMORY_SCOPE_WORKGROUP);
      SBAR();
#pragma unroll
      for (int s = 1; s < 16; s *= 2)
#pragma unroll
        for (int r = 0; r < 16; r += 2 * s) a[r] += a[r + s];
      *(LAS f32x4*)(L + L_TOT + u * 16) = a[0];
#pragma unroll
      for (int j = 0; j < 4; ++j) zv[j] = sum_lanes_mod4(zv[j]);
      if (lane < 4) *(LAS f32x4*)(L + L_TOT + (256 + rq * 4 + lane) * 16) = zv;
      STAMP(23);
    }
    LBAR();
    STAMP(6);
    const f32x4 u0 = *(LAS f32x4*)(L + L_TOT + (c * 128 + lane) * 16), u1 = *(LAS f32x4*)(L + L_TOT + (c * 128 + 64 + lane) * 16);
    const lptr zp = L + L_TOT + 4096 + c * 128 + h * 16;
    const f32x4 z0 = *(LAS f32x4*)zp, z1 = *(LAS f32x4*)(zp + 32), z2 = *(LAS f32x4*)(zp + 64), z3 = *(LAS f32x4*)(zp + 96);
    const bool lo = i32 < 16;
    const u32x4 pu = {pk(u0[0], u0[1]), pk(u0[2], u0[3]), pk(u1[0], u1[1]), pk(u1[2], u1[3])};
    const u32x4 pz0 = {pk(z0[0], z0[1]), pk(z0[2], z0[3]), pk(z1[0], z1[1]), pk(z1[2], z1[3])};
    const u32x4 pz1 = {pk(z2[0], z2[1]), pk(z2[2], z2[3]), pk(z3[0], z3[1]), pk(z3[2], z3[3])};
    u32x4 a0, a1;
#pragma unroll
    for (int j = 0; j < 4; ++j) { a0[j] = lo ? pu[j] : pz0[j]; a1[j] = lo ? pz1[j] : pu[j]; }
    const f32x16 zero = {};
    const f32x16 d0 = MFMA(__builtin_bit_cast(bf16x8, a0), Qf[0], zero);
    const f32x16 d1 = MFMA(__builtin_bit_cast(bf16x8, a1), Qf[1], zero);
    const float s0 = __builtin_amdgcn_rcpf(d0[8] + 1e-6f), s1 = __builtin_amdgcn_rcpf(d1[0] + 1e-6f);
    const u32x4 o0 = {pk(d0[0] * s0, d0[1] * s0), pk(d0[2] * s0, d0[3] * s0), pk(d0[4] * s0, d0[5] * s0), pk(d0[6] * s0, d0[7] * s0)};
    const u32x4 o1 = {pk(d1[8] * s1, d1[9] * s1), pk(d1[10] * s1, d1[11] * s1), pk(d1[12] * s1, d1[13] * s1), pk(d1[14] * s1, d1[15] * s1)};
    __builtin_amdgcn_raw_buffer_store_b128(o0, rsWS, ofrag, 0, 16);
    __builtin_amdgcn_raw_buffer_store_b128(o1, rsWS, ofrag + 1024, 0, 16);
    if (rt == 0 && wid == 4 && lane == 0) flag_store(ws, WS_EKV + quad * 128, tagG);
  } else {
    bf16x8 Kp[2];
    u32x2 vp;
    if (sp == 1) {
      const lptr ex = L + L_PART + (c * 2 + p) * 1024;
      Kp[0] = lds_frag(ex + h * 128 + (i32 & 7) * 16);
      Kp[1] = lds_frag(ex + 256 + h * 128 + (i32 & 7) * 16);
      vp = *(LAS u32x2*)(ex + 512 + lane * 8);
    } else if (rt > 0) {
      poll_flags(ws, WS_FH + (((rt - 1) * 8 + quad) * 4 + c * 2 + p) * 64, 64, 1, tagG, lane);
      const unsigned ex = WS_HALO + (((rt - 1) * 8 + quad) * 4 + c * 2 + p) * 1024;
      Kp[0] = __builtin_bit_cast(bf16x8, __builtin_amdgcn_raw_buffer_load_b128(rsWS, ex + h * 128 + (i32 & 7) * 16, 0, LD_AUX));
      Kp[1] = __builtin_bit_cast(bf16x8, __builtin_amdgcn_raw_buffer_load_b128(rsWS, ex + 256 + h * 128 + (i32 & 7) * 16, 0, LD_AUX));
      vp = __builtin_amdgcn_raw_buffer_load_b64(rsWS, ex + 512 + lane * 8, 0, LD_AUX);
      if (wid == 0 && lane == 0) flag_store(ws, WS_EH + ((rt - 1) * 8 + quad) * 128, tagG);
    } else {
      Kp[0] = Kf[0]; Kp[1] = Kf[1]; vp = (u32x2){0u, 0u};
    }
    STAMP(5);
    STAMP(6);
    const bool noprev = (rt == 0 && sp == 0);
    f32x16 bias;
#pragma unroll
    for (int r = 0; r < 16; ++r) bias[r] = ((unsigned)(i32 - ((r & 3) + 8 * (r >> 2) + 4 * h)) <= 7u) ? 0.f : -1e30f;
    float bprev[4];
#pragma unroll
    for (int j = 0; j < 4; ++j) bprev[j] = (!noprev && (24 + 4 * h + j >= i32 + 25)) ? 0.f : -1e30f;
    constexpr float C1 = 0.25f * LOG2E;
    const unsigned ONE2 = 0x3F803F80u;
    const f32x16 zero = {};
#pragma unroll
    for (int hh = 0; hh < 2; ++hh) {
      f32x16 sc = MFMA(Kf[hh], Qf[hh], bias);
      const f32x16 sv = MFMA(Kp[hh], Qf[hh], zero);
      float sp4[4];
#pragma unroll
      for (int j = 0; j < 4; ++j) sp4[j] = sv[12 + j] + bprev[j];
      float m = __builtin_fmaxf(__builtin_fmaxf(sp4[0], sp4[1]), __builtin_fmaxf(sp4[2], sp4[3]));
#pragma unroll
      for (int r = 0; r < 16; r += 2) m = __builtin_fmaxf(__builtin_fmaxf(m, sc[r]), sc[r + 1]);
      m = swap_max(m);
      const float nm = -m * C1;
#pragma unroll
      for (int r = 0; r < 16; ++r) sc[r] = __builtin_amdgcn_exp2f(__builtin_fmaf(sc[r], C1, nm));
#pragma unroll
      for (int j = 0; j < 4; ++j) sp4[j] = __builtin_amdgcn_exp2f(__builtin_fmaf(sp4[j], C1, nm));
      const bf16x8 P0 = pack8(sc, 0), P1 = pack8(sc, 8);
      const u32x4 ppv = {0u, 0u, pk(sp4[0], sp4[1]), pk(sp4[2], sp4[3])};
      const bool mine = (i32 < 16) == (hh == 0);
      const u32x4 v0 = __builtin_bit_cast(u32x4, Vf[0]), v1 = __builtin_bit_cast(u32x4, Vf[1]);
      u32x4 a0, a1, ap;
#pragma unroll
      for (int j = 0; j < 4; ++j) { a0[j] = mine ? v0[j] : ONE2; a1[j] = mine ? v1[j] : ONE2; }
      ap = (u32x4){0u, 0u, mine ? vp[0] : ONE2, mine ? vp[1] : ONE2};
      f32x16 o = MFMA(__builtin_bit_cast(bf16x8, a0), P0, zero);
      o = MFMA(__builtin_bit_cast(bf16x8, a1), P1, o);
      o = MFMA(__builtin_bit_cast(bf16x8, ap), __builtin_bit_cast(bf16x8, ppv), o);
      const int nb = hh == 0 ? 0 : 8, db = hh == 0 ? 8 : 0;
      const float s = __builtin_amdgcn_rcpf(o[db]);
      const u32x4 ov = {pk(o[nb] * s, o[nb + 1] * s), pk(o[nb + 2] * s, o[nb + 3] * s), pk(o[nb + 4] * s, o[nb + 5] * s), pk(o[nb + 6] * s, o[nb + 7] * s)};
      __builtin_amdgcn_raw_buffer_store_b128(ov, rsWS, ofrag + hh * 1024, 0, 16);
    }
  }
  STAMP(7);
  WAIT_VM(0);
  if (lane == 0) flag_store(ws, WS_FO + (((((rt * 2 + sp) * 2 + p) * 2 + BR) * 8 + quad) * 2 + c) * 64, tagO);
  if (BR == 0 && c == 0 && lane == 0) __hip_atomic_fetch_add((LAS unsigned*)(L + L_WOB), 1u, __ATOMIC_RELAXED, __HIP_MEMORY_SCOPE_WORKGROUP);
  STAMP(8);
  __builtin_amdgcn_s_setprio(0);
  if (BR == 1 && c == 1) {
    lds_wait(L + L_CNT, 8);
#pragma unroll
    for (int i = 0; i < 16; ++i) wo_piece(1, rq * 16 + i);
    STAMP(18);
  }
  if (BR == 1 && wid >= 6) gate([&](int) LAM {});
}

__global__ __launch_bounds__(512, 2) void gated_mix_kernel(const __hip_bfloat16* __restrict__ x, const __hip_bfloat16* __restrict__ wA,
                                                           const __hip_bfloat16* __restrict__ woA, const __hip_bfloat16* __restrict__ wB,
                                                           const __hip_bfloat16* __restrict__ woB, const __hip_bfloat16* __restrict__ gw,
                                                           __hip_bfloat16* __restrict__ out, char* ws) {
  __shared__ __attribute__((aligned(1024))) char smem[LDS_BYTES];
  const int tid = threadIdx.x, lane = tid & 63, wid = __builtin_amdgcn_readfirstlane(tid >> 6);
  const int b = blockIdx.x, xcd = b & 7, loc = b >> 3;
#define XMAP 0
  const int br = XMAP ? (xcd >> 2) : (xcd & 1), rt = (XMAP ? (xcd & 3) : (xcd >> 1)) * 4 + (loc >> 3), quad = loc & 7;
  unsigned vts = 0;
  STAMP(0);
  const rsrc_t rsX = __builtin_amdgcn_make_buffer_rsrc((void*)x, 0, 2048 * 512 * 2, 0x00020000);
  const rsrc_t rsW = __builtin_amdgcn_make_buffer_rsrc((void*)(br ? wB : wA), 0, 512 * 1536 * 2, 0x00020000);
  const rsrc_t rsWoA = __builtin_amdgcn_make_buffer_rsrc((void*)woA, 0, 512 * 512 * 2, 0x00020000);
  const rsrc_t rsWoB = __builtin_amdgcn_make_buffer_rsrc((void*)woB, 0, 512 * 512 * 2, 0x00020000);
  const rsrc_t rsG = __builtin_amdgcn_make_buffer_rsrc((void*)gw, 0, 1024, 0x00020000);
  const rsrc_t rsWS = __builtin_amdgcn_make_buffer_rsrc((void*)ws, 0, WS_USED, 0x00020000);
  const lptr L = (lptr)smem;
  const unsigned lds0 = (unsigned)(uintptr_t)L;

  const int l8 = lane >> 3, c8 = lane & 7;
  const int xrow = rt * 128 + 64 * (wid >> 2) + ((wid >> 1) & 1) + 2 * (16 * (wid & 1) + l8);
  const unsigned vX0 = xrow * 1024 + ((c8 ^ (lane >> 4)) << 4);
  const unsigned vX1 = (xrow + 16) * 1024 + ((c8 ^ (4 + (lane >> 4))) << 4);
  const int wsw = (c8 ^ (((lane >> 4) & 1) << 2)) << 4;
  const unsigned vW = (8 * wid + l8) * 3072 + quad * 128 + wsw;
  const unsigned vWo = l8 * 1024 + quad * 128 + wsw;

  asm volatile("s_nop 4" ::: "memory");
#pragma unroll
  for (int f = 0; f < 2; ++f) {
    const unsigned sl = lds0 + f * SLOT;
    const unsigned kc = (f + ROT_OF(quad, rt)) & 7;
    dma16(sl + wid * 2048, vX0, rsX, kc * 128);
    dma16(sl + wid * 2048 + 1024, vX1, rsX, kc * 128);
    dma16(sl + 16384 + wid * 1024, vW, rsW, kc * 196608 + 1024);
    dma16(sl + 24576 + wid * 1024, vW, rsW, kc * 196608 + 2048);
    if (f < 1) asm volatile("s_barrier" ::: "memory");
  }
  STAMP(1);
  u64 tagO;
  bf16x8 wo[32];
  if (br == 0) phase1<0>(smem, rsX, rsW, rsWoA, rsWoB, rsG, rsWS, ws, rt, quad, wid, lane, vX0, vX1, vW, vWo, vts, tagO, wo);
  else phase1<1>(smem, rsX, rsW, rsWoA, rsWoB, rsG, rsWS, ws, rt, quad, wid, lane, vX0, vX1, vW, vWo, vts, tagO, wo);

  const int pt = wid & 1, kq = (wid >> 2) * 2 + ((wid >> 1) & 1);
  const int i32 = lane & 31, h = lane >> 5;
  const unsigned fwait = WS_FO + (((((rt * 2 + br) * 2 + pt) * 2 + (kq >> 1)) * 8 + (kq & 1) * 4) * 2) * 64;
  const unsigned ob = WS_O + ((((rt * 2 + br) * 2 + pt) * 64) + kq * 16) * 1024 + lane * 16;
  bf16x8 of[16];
  auto load_o = [&]() LAM {
#pragma unroll
    for (int i = 0; i < 16; ++i) of[i] = __builtin_bit_cast(bf16x8, __builtin_amdgcn_raw_buffer_load_b128(rsWS, ob, i * 1024, LD_AUX));
  };
  bool ready = false;
  if (!(br == 0 && kq < 2)) {
    u64 v0 = tagO;
    if (lane < 8) v0 = __hip_atomic_load((gu64*)(ws + fwait + lane * 64), __ATOMIC_RELAXED, __HIP_MEMORY_SCOPE_AGENT);
    ready = __builtin_amdgcn_ballot_w64(v0 != tagO) == 0;
    WAIT_VM(0);
    if (br == 1 && kq >= 2 && lane == 0) __hip_atomic_fetch_add((LAS unsigned*)(L + L_WOB), 1u, __ATOMIC_RELAXED, __HIP_MEMORY_SCOPE_WORKGROUP);
    STAMP(26);
    if (ready) load_o();
    if (kq >= 2) lds_wait(L + L_WOB, 4); else lds_wait(L + L_CNT, 8);
    STAMP(19);
    read_wo(wo, L, kq, lane);
  }
  STAMP(9);
  if (!ready) {
    poll_flags(ws, fwait, 64, 8, tagO, lane);
    load_o();
  }
  STAMP(10);
  SBAR();
  f32x16 a0 = {}, a1 = {};
#pragma unroll
  for (int i = 0; i < 16; ++i) { a0 = MFMA(wo[2 * i], of[i], a0); a1 = MFMA(wo[2 * i + 1], of[i], a1); }
  STAMP(11);
  LBAR();
  STAMP(24);
  {
    const lptr pr = L + L_RED + (kq * 64 + pt * 32 + i32) * PROW + h * 16;
#pragma unroll
    for (int g4 = 0; g4 < 4; ++g4) {
      *(LAS f32x4*)(pr + g4 * 32) = (f32x4){a0[4 * g4], a0[4 * g4 + 1], a0[4 * g4 + 2], a0[4 * g4 + 3]};
      *(LAS f32x4*)(pr + 128 + g4 * 32) = (f32x4){a1[4 * g4], a1[4 * g4 + 1], a1[4 * g4 + 2], a1[4 * g4 + 3]};
    }
  }
  LBAR();
  STAMP(13);
  {
    const int pos = tid >> 3, cg = tid & 7;
    const lptr pr = L + L_RED + pos * PROW + cg * 32;
    const float g = *(LAS float*)(L + L_G + pos * 4), gb = 1.f - g;
    const f32x4 s0 = (*(LAS f32x4*)pr + *(LAS f32x4*)(pr + 64 * PROW)) * g + (*(LAS f32x4*)(pr + 128 * PROW) + *(LAS f32x4*)(pr + 192 * PROW)) * gb;
    const f32x4 s1 = (*(LAS f32x4*)(pr + 16) + *(LAS f32x4*)(pr + 64 * PROW + 16)) * g + (*(LAS f32x4*)(pr + 128 * PROW + 16) + *(LAS f32x4*)(pr + 192 * PROW + 16)) * gb;
    const int row = rt * 128 + 64 * br + 2 * (pos & 31) + (pos >> 5);
    const u32x4 ov = {pk(s0[0], s0[1]), pk(s0[2], s0[3]), pk(s1[0], s1[1]), pk(s1[2], s1[3])};
    __builtin_amdgcn_raw_buffer_store_b128(ov, __builtin_amdgcn_make_buffer_rsrc((void*)out, 0, 2048 * 1024, 0x00020000), row * 1024 + quad * 128 + cg * 16, 0, 0);
  }
  if (quad == 0 && br == 0 && tid == 0) flag_store(ws, WS_EO + rt * 128, tagO);
  STAMP(12);
  TSDUMP();
}

#if NOPK && defined(__HIP_DEVICE_COMPILE__)
#pragma clang attribute pop
#endif

extern "C" void kernel_launch(void* const* d_in, const int* in_sizes, int n_in, void* d_out, int out_size, void* d_ws, size_t ws_size,
                              hipStream_t stream) {
  gated_mix_kernel<<<256, 512, 0, stream>>>((const __hip_bfloat16*)d_in[0], (const __hip_bfloat16*)d_in[1], (const __hip_bfloat16*)d_in[2],
                                            (const __hip_bfloat16*)d_in[4], (const __hip_bfloat16*)d_in[5], (const __hip_bfloat16*)d_in[6],
                                            (__hip_bfloat16*)d_out, (char*)d_ws);
}
```

```cpp
#include <hip/hip_runtime.h>
#include <hip/hip_bf16.h>
#include <stdint.h>

typedef __bf16 bf16x2 __attribute__((ext_vector_type(2)));
typedef __bf16 bf16x8 __attribute__((ext_vector_type(8)));
typedef short s16x4 __attribute__((ext_vector_type(4)));
typedef float f32x2 __attribute__((ext_vector_type(2)));
typedef float f32x4 __attribute__((ext_vector_type(4)));
typedef float f32x16 __attribute__((ext_vector_type(16)));
typedef unsigned u32x2 __attribute__((ext_vector_type(2)));
typedef unsigned u32x4 __attribute__((ext_vector_type(4)));
typedef unsigned long long u64;
#define LAS __attribute__((address_space(3)))
#define GAS __attribute__((address_space(1)))
#define DI __device__ __forceinline__
typedef LAS char* lptr;
typedef GAS u64 gu64;
typedef __amdgpu_buffer_rsrc_t rsrc_t;

constexpr unsigned WS_EO = 0;
constexpr unsigned WS_EKV = 2048;
constexpr unsigned WS_EH = 4096;
constexpr unsigned WS_FKV = 32768;
constexpr unsigned WS_FH = 65536;
constexpr unsigned WS_FO = 131072;
constexpr unsigned WS_KVP = 262144 + 65536;
constexpr unsigned KVP_BYTES = 4352;
constexpr unsigned WS_HALO = 1048576;
constexpr unsigned WS_O = 2097152;
constexpr unsigned WS_STAMP = 8u << 20;
constexpr unsigned WS_USED = 16u << 20;

constexpr int SLOT = 32768;
constexpr int L_GW = 0;
constexpr int L_G = L_GW + 1024;
constexpr int L_EP = L_GW + 1536;
constexpr int L_CNT = L_GW + 1568;
constexpr int L_WOB = L_GW + 1572;
constexpr int L_UIS = L_GW + 1576;
constexpr int L_PART = L_GW + 2048;
constexpr int L_TOT = L_PART + 8 * 2176;
constexpr int L_RED = SLOT;
constexpr int LDS_BYTES = 163840;
constexpr int PROW = 272;
#define slot_of(f) ((f) < 8 ? (f) % 5 : (f) == 8 ? 3 : 4)

constexpr float LOG2E = 1.4426950408889634f;
constexpr unsigned SPIN_CAP = 1u << 16;

#define MFMA(a, b, c) __builtin_amdgcn_mfma_f32_32x32x16_bf16((a), (b), (c), 0, 0, 0)
#define WAIT_BAR(N) asm volatile("s_waitcnt vmcnt(" #N ") lgkmcnt(0)\n\ts_barrier" ::: "memory")
#define WAIT_BAR_NL(N) asm volatile("s_waitcnt vmcnt(" #N ")\n\ts_barrier" ::: "memory")
#define WAIT_VM(N) asm volatile("s_waitcnt vmcnt(" #N ")" ::: "memory")
#define ROTM 6
#define ROT_OF(q, r) ((unsigned)(ROTM == 0 ? 0 : ROTM == 1 ? (q) : ROTM == 2 ? (q) + 2 * (r) : ROTM == 3 ? 2 * (r) : ROTM == 4 ? (r) : ROTM == 5 ? (r) + (q) : (r) + 4 * ((q) & 1)))
#define PUBS 7
#define PFD 1
#define SBAR() __builtin_amdgcn_sched_barrier(0)
#define LBAR() asm volatile("s_waitcnt lgkmcnt(0)\n\ts_barrier" ::: "memory")

#define STAMP(i) do {} while (0)
#define TSDUMP() do {} while (0)

#define NOPK 0
#if NOPK && defined(__HIP_DEVICE_COMPILE__)
#define LAM __attribute__((target("no-packed-fp32-ops"), always_inline))
#else
#define LAM __attribute__((always_inline))
#endif
#if NOPK && defined(__HIP_DEVICE_COMPILE__)
#pragma clang attribute push(__attribute__((target("no-packed-fp32-ops"))), apply_to = function)
#endif
DI float as_f(unsigned u) { return __builtin_bit_cast(float, u); }
DI unsigned as_u(float f) { return __builtin_bit_cast(unsigned, f); }
DI unsigned pk(float a, float b) { return __builtin_bit_cast(unsigned, __builtin_convertvector((f32x2){a, b}, bf16x2)); }
DI bf16x8 pack8(const f32x16& a, int o) {
  u32x4 r = {pk(a[o], a[o + 1]), pk(a[o + 2], a[o + 3]), pk(a[o + 4], a[o + 5]), pk(a[o + 6], a[o + 7])};
  return __builtin_bit_cast(bf16x8, r);
}
DI unsigned phi_pk(float a, float b, f32x2& zsum) {
  const f32x2 t = {a, b};
  const f32x2 u = t * (f32x2){LOG2E, LOG2E}, w = t + (f32x2){1.f, 1.f};
  const float e0 = __builtin_amdgcn_fmed3f(__builtin_amdgcn_exp2f(u[0]), 0.f, 1.f), e1 = __builtin_amdgcn_fmed3f(__builtin_amdgcn_exp2f(u[1]), 0.f, 1.f);
  const f32x2 r = (f32x2){__builtin_fmaxf(w[0], e0), __builtin_fmaxf(w[1], e1)} + (f32x2){1e-6f, 1e-6f};
  zsum += r;
  return pk(r[0], r[1]);
}
DI float opq(float x) { asm("" : "+v"(x)); return x; }
DI unsigned phi_pk1(float a, float b, float& z0, float& z1) {
  const float e0 = __builtin_amdgcn_fmed3f(__builtin_amdgcn_exp2f(a * LOG2E), 0.f, 1.f), e1 = __builtin_amdgcn_fmed3f(__builtin_amdgcn_exp2f(b * LOG2E), 0.f, 1.f);
  const float r0 = opq(__builtin_fmaxf(a + 1.f, e0) + 1e-6f), r1 = __builtin_fmaxf(b + 1.f, e1) + 1e-6f;
  z0 = opq(z0 + r0); z1 += r1;
  return pk(r0, r1);
}
DI bf16x8 phi8(const f32x16& a, int o) {
  f32x2 unused = {};
  u32x4 r = {phi_pk(a[o], a[o + 1], unused), phi_pk(a[o + 2], a[o + 3], unused), phi_pk(a[o + 4], a[o + 5], unused), phi_pk(a[o + 6], a[o + 7], unused)};
  return __builtin_bit_cast(bf16x8, r);
}
DI void dma16(unsigned lds, unsigned voff, rsrc_t rs, unsigned soff) {
  asm volatile("s_mov_b32 m0, %0\n\ts_nop 0\n\tbuffer_load_dwordx4 %1, %2, %3 offen lds" ::"s"(lds), "v"(voff), "s"(rs), "s"(soff) : "memory");
}
DI void dma4(unsigned lds, unsigned voff, rsrc_t rs) {
  asm volatile("s_mov_b32 m0, %0\n\ts_nop 0\n\tbuffer_load_dword %1, %2, 0 offen sc1 lds" ::"s"(lds), "v"(voff), "s"(rs) : "memory");
}
DI u64 lds_tag(lptr p) {
  const u32x2 v = *(const LAS u32x2*)p;
  return (((u64)(unsigned)__builtin_amdgcn_readfirstlane(v[1]) << 32) | (unsigned)__builtin_amdgcn_readfirstlane(v[0])) + 1;
}
DI bf16x8 lds_frag(lptr p) { return *(const LAS bf16x8*)p; }
DI bf16x8 tr_frag(lptr p, int o0, int o1) {
  const s16x4 lo = __builtin_bit_cast(s16x4, __builtin_amdgcn_ds_read_tr16_b64_v4i16((LAS s16x4*)(p + o0)));
  const s16x4 hi = __builtin_bit_cast(s16x4, __builtin_amdgcn_ds_read_tr16_b64_v4i16((LAS s16x4*)(p + o1)));
  return __builtin_bit_cast(bf16x8, __builtin_shufflevector(lo, hi, 0, 1, 2, 3, 4, 5, 6, 7));
}
DI float sel(bool c, float a, float b) { asm("" : "+v"(a), "+v"(b)); return c ? a : b; }
#define ACQ 0
constexpr int LD_AUX = ACQ ? 0 : 16;
DI void acquire() { if (ACQ) __builtin_amdgcn_fence(__ATOMIC_ACQUIRE, "agent"); }
DI void flag_store(char* ws, unsigned off, u64 v) { __hip_atomic_store((gu64*)(ws + off), v, __ATOMIC_RELAXED, __HIP_MEMORY_SCOPE_AGENT); }
DI void poll_flags(char* ws, unsigned off0, unsigned stride, int n, u64 tag, int lane) {
  gu64* p = (gu64*)(ws + off0 + (unsigned)lane * stride);
#pragma unroll 1
  for (unsigned spins = 0; spins < SPIN_CAP; ++spins) {
    u64 v = tag;
    if (lane < n) v = __hip_atomic_load(p, __ATOMIC_RELAXED, __HIP_MEMORY_SCOPE_AGENT);
    if (__builtin_amdgcn_ballot_w64(v != tag) == 0) break;
    __builtin_amdgcn_s_sleep(1);
  }
  __builtin_amdgcn_fence(__ATOMIC_ACQUIRE, "wavefront");
  asm volatile("" ::: "memory");
  acquire();
}
DI void lds_wait(lptr p, unsigned want) {
#pragma unroll 1
  for (unsigned spins = 0; spins < SPIN_CAP; ++spins) {
    if ((unsigned)__builtin_amdgcn_readfirstlane(__hip_atomic_load((LAS unsigned*)p, __ATOMIC_RELAXED, __HIP_MEMORY_SCOPE_WORKGROUP)) == want) break;
    __builtin_amdgcn_s_sleep(2);
  }
  asm volatile("" ::: "memory");
}
DI float sum_lanes_mod4(float x) {
  x += as_f(__builtin_amdgcn_update_dpp(0u, as_u(x), 0x124, 0xf, 0xf, false));
  x += as_f(__builtin_amdgcn_update_dpp(0u, as_u(x), 0x128, 0xf, 0xf, false));
  auto a = __builtin_amdgcn_permlane16_swap(as_u(x), as_u(x), false, false);
  x = as_f(a[0]) + as_f(a[1]);
  auto b = __builtin_amdgcn_permlane32_swap(as_u(x), as_u(x), false, false);
  return as_f(b[0]) + as_f(b[1]);
}
DI float swap_max(float m) {
  auto r = __builtin_amdgcn_permlane32_swap(as_u(m), as_u(m), false, false);
  return __builtin_fmaxf(as_f(r[0]), as_f(r[1]));
}

DI void read_wo(bf16x8 (&wo)[32], lptr L, int kq, int lane) {
  const int h = lane >> 5, q4 = (lane & 15) >> 2, p4 = lane & 3, nsub = (lane >> 4) & 1;
  const lptr base = L + (kq + 1) * SLOT + (4 * h + q4) * 128 + 8 * (p4 & 1);
  const int sw = ((q4 >> 1) & 1) << 2;
  const lptr b0 = base + ((2 * nsub + (p4 >> 1)) ^ sw) * 16, b1 = base + ((4 + 2 * nsub + (p4 >> 1)) ^ sw) * 16;
#pragma unroll
  for (int i = 0; i < 16; ++i) {
    wo[2 * i] = tr_frag(b0, i * 2048, i * 2048 + 1024);
    wo[2 * i + 1] = tr_frag(b1, i * 2048, i * 2048 + 1024);
  }
}

template <int BR>
DI void phase1(char* smem_generic, const rsrc_t rsX, const rsrc_t rsW, const rsrc_t rsWoA, const rsrc_t rsWoB, const rsrc_t rsG, const rsrc_t rsWS, char* ws,
               const int rt, const int quad, const int wid, const int lane, const unsigned vX0, const unsigned vX1, const unsigned vW,
               const unsigned vWo, unsigned& vts, u64& tagO, bf16x8 (&wo)[32]) {
  const lptr L = (lptr)smem_generic;
  const unsigned lds0 = (unsigned)(uintptr_t)L;
  const int c = wid >> 2, rq = wid & 3, p = rq & 1, sp = rq >> 1;
  const int i32 = lane & 31, h = lane >> 5;
  const unsigned pieceX = lds0 + wid * 2048, pieceW = lds0 + wid * 1024;
  const unsigned rot = ROT_OF(quad, rt);

  auto piece = [&](int f, int i) LAM {
    const unsigned sl = (unsigned)slot_of(f) * SLOT;
    if (f < 8) {
      const unsigned kc = (f + rot) & 7;
      if (i == 0) dma16(pieceX + sl, vX0, rsX, kc * 128);
      else if (i == 1) dma16(pieceX + sl + 1024, vX1, rsX, kc * 128);
      else dma16(pieceW + sl + 16384 + (i - 2) * 8192, vW, rsW, kc * 196608 + (i - 1) * 1024);
    } else {
      dma16(pieceW + sl + i * 8192, vW, rsW, (((f - 8) * 4 + i + rot) & 7) * 196608);
    }
  };
  auto wo_piece = [&](int which, unsigned q) LAM { dma16(lds0 + (which ? 3 : 1) * SLOT + q * 1024, vWo, which ? rsWoB : rsWoA, q * 8192); };

  const int xs = h ^ ((i32 >> 1) & 7);
  const int q4 = (lane & 15) >> 2, p4 = lane & 3, nsub = (lane >> 4) & 1;
  const int wchunk = (4 * c + 2 * nsub + (p4 >> 1)) ^ (((q4 >> 1) & 1) << 2);
  lptr xb[3][4], wb[3];
#pragma unroll
  for (int t = 0; t < 4; ++t) {
    xb[0][t] = L + (32 * rq + i32) * 128 + ((xs ^ (2 * t)) << 4);
    xb[1][t] = xb[0][t] + 2 * SLOT;
    xb[2][t] = xb[0][t] + 4 * SLOT;
  }
  wb[0] = L + (8 * h + q4) * 128 + wchunk * 16 + 8 * (p4 & 1);
  wb[1] = wb[0] + 2 * SLOT;
  wb[2] = wb[0] + 4 * SLOT;
#pragma unroll
  for (int t = 0; t < 4; ++t) { asm("" : "+v"(xb[0][t])); asm("" : "+v"(xb[1][t])); asm("" : "+v"(xb[2][t])); }
  asm("" : "+v"(wb[0])); asm("" : "+v"(wb[1])); asm("" : "+v"(wb[2]));

  bf16x8 xf[32], wk[3], wv[3];
  f32x16 accK = {}, accV = {};
  constexpr int VLAG = BR == 0 ? 8 : 0;
  auto rdv = [&](int s, int buf) LAM {
    const int sl = slot_of(s >> 2), o = (sl & 1) * SLOT + 16384 + 8192 + (s & 3) * 2048;
    wv[buf] = tr_frag(wb[sl >> 1], o, o + 512);
  };
  auto rd1 = [&](int s) LAM {
    if (s < 32) {
      const int sl = slot_of(s >> 2), t = s & 3;
      xf[s] = lds_frag(sl < 4 ? xb[sl >> 1][t] + (sl & 1) * SLOT : xb[2][t]);
      const int o = (sl & 1) * SLOT + 16384 + t * 2048;
      wk[s % 3] = tr_frag(wb[sl >> 1], o, o + 512);
      if (s < 32 - VLAG) rdv(s, s % 3);
    } else {
      rdv(s - VLAG, s % 3);
    }
  };
  WAIT_BAR(7);
  STAMP(15);
  rd1(0); rd1(1);
  SBAR();
  unsigned kpk[8];
  float zs0 = 0.f, zs1 = 0.f;
#pragma unroll
  for (int s = 0; s < 32 + VLAG; ++s) {
    const int cc = s >> 2;
    if ((s & 3) == 2 && cc < 7) {
      if (s == 2) WAIT_BAR_NL(4); else WAIT_BAR_NL(8);
      if (cc == 6 && wid == 0) {
        dma16(lds0 + L_GW, (lane * 16 + rot * 128) & 1023, rsG, 0);
        const unsigned e0 = WS_EO + rt * 128;
        const unsigned e1 = BR == 0 ? WS_EKV + quad * 128 : WS_EH + (rt * 8 + quad) * 128;
        const unsigned e2 = BR == 0 ? e1 : WS_EH + ((rt > 0 ? rt - 1 : 0) * 8 + quad) * 128;
        const unsigned eo = (lane < 2 ? e0 : lane < 4 ? e1 : e2) + (lane & 1) * 4;
        if (lane < 6) dma4(lds0 + L_EP, eo, rsWS);
        if (lane < 3) *(LAS unsigned*)(L + L_CNT + lane * 4) = 0;
      }
    }
    if (s < 2) { piece(2, 2 * s); piece(2, 2 * s + 1); }
    else if (s < 6) { piece(3 + ((s - 2) >> 1), 2 * (s & 1)); piece(3 + ((s - 2) >> 1), 2 * (s & 1) + 1); }
    else if (((s - 2) >> 2) + 4 < 10) piece(((s - 2) >> 2) + 4, (s - 2) & 3);
    if (s + 2 < 32 + VLAG) rd1(s + 2);
    if (s < 32) accK = BR == 0 ? MFMA(xf[s], wk[s % 3], accK) : MFMA(wk[s % 3], xf[s], accK);
    if (s < 32 - VLAG) accV = MFMA(xf[s], wv[s % 3], accV);
    if (s >= 32) {
      accV = MFMA(xf[s - VLAG], wv[s % 3], accV);
      kpk[s - 32] = phi_pk1(accK[2 * (s - 32)], accK[2 * (s - 32) + 1], zs0, zs1);
    }
    SBAR();
  }
  STAMP(2);
  bf16x8 Kf[2], Vf[2];
  if (BR == 0) {
    Kf[0] = __builtin_bit_cast(bf16x8, (u32x4){kpk[0], kpk[1], kpk[2], kpk[3]});
    Kf[1] = __builtin_bit_cast(bf16x8, (u32x4){kpk[4], kpk[5], kpk[6], kpk[7]});
  } else { Kf[0] = pack8(accK, 0); Kf[1] = pack8(accK, 8); }
  Vf[0] = pack8(accV, 0); Vf[1] = pack8(accV, 8);
  if (BR == 0) {
    f32x16 kv = {};
    kv = MFMA(Kf[0], Vf[0], kv);
    kv = MFMA(Kf[1], Vf[1], kv);
    const lptr pp = L + L_PART + wid * 2176;
    {
      const float zh = zs0 + zs1;
      auto sw = __builtin_amdgcn_permlane32_swap(as_u(zh), as_u(zh), false, false);
      if (h == 0) *(LAS float*)(pp + 2048 + i32 * 4) = as_f(sw[0]) + as_f(sw[1]);
    }
    const bool lo = i32 < 16;
    f32x4 u0, u1;
#pragma unroll
    for (int j = 0; j < 4; ++j) { u0[j] = sel(lo, kv[j], kv[8 + j]); u1[j] = sel(lo, kv[4 + j], kv[12 + j]); }
    *(LAS f32x4*)(pp + lane * 16) = u0;
    *(LAS f32x4*)(pp + 1024 + lane * 16) = u1;
  } else {
    if (sp == 0) {
      const lptr ex = L + L_PART + (c * 2 + p) * 1024;
      if (i32 >= 24) {
        *(LAS bf16x8*)(ex + h * 128 + (i32 - 24) * 16) = Kf[0];
        *(LAS bf16x8*)(ex + 256 + h * 128 + (i32 - 24) * 16) = Kf[1];
      }
      const u32x4 v1 = __builtin_bit_cast(u32x4, Vf[1]);
      *(LAS u32x2*)(ex + 512 + lane * 8) = (u32x2){v1[2], v1[3]};
    }
  }
  WAIT_BAR(0);
  STAMP(3);
  if (BR == 0) {
    if (wid == 3) {
      __builtin_amdgcn_s_setprio(3);
      f32x4 pa[5][4];
#pragma unroll
      for (int it = 0; it < 5; ++it) {
        const int u = it < 4 ? it * 64 + lane : 256 + (lane & 15);
        const int cc = it < 4 ? (it >> 1) : ((lane >> 3) & 1);
        const int o = it < 4 ? (u & 127) * 16 : 2048 + (lane & 7) * 16;
        const lptr s = L + L_PART + cc * 4 * 2176 + o;
#pragma unroll
        for (int r = 0; r < 4; ++r) pa[it][r] = *(LAS f32x4*)(s + r * 2176);
      }
      SBAR();
#pragma unroll
      for (int it = 0; it < 5; ++it) {
        f32x4 a;
#pragma unroll
        for (int j = 0; j < 4; ++j) a[j] = opq(opq(pa[it][0][j] + pa[it][1][j]) + opq(pa[it][2][j] + pa[it][3][j]));
        const unsigned go = WS_KVP + (quad * 16 + rt) * KVP_BYTES + (it < 4 ? it * 64 + lane : 256 + lane) * 16;
        if (it < 4 || lane < 16) __builtin_amdgcn_raw_buffer_store_b128(__builtin_bit_cast(u32x4, a), rsWS, go, 0, 16);
      }
      __builtin_amdgcn_s_setprio(0);
    }
  } else {
    if (sp == 1 && rt < 15) {
      const unsigned ex = WS_HALO + ((rt * 8 + quad) * 4 + c * 2 + p) * 1024;
      if (i32 >= 24) {
        __builtin_amdgcn_raw_buffer_store_b128(__builtin_bit_cast(u32x4, Kf[0]), rsWS, ex + h * 128 + (i32 - 24) * 16, 0, 16);
        __builtin_amdgcn_raw_buffer_store_b128(__builtin_bit_cast(u32x4, Kf[1]), rsWS, ex + 256 + h * 128 + (i32 - 24) * 16, 0, 16);
      }
      const u32x4 v1 = __builtin_bit_cast(u32x4, Vf[1]);
      __builtin_amdgcn_raw_buffer_store_b64((u32x2){v1[2], v1[3]}, rsWS, ex + 512 + lane * 8, 0, 16);
    }
  }
  SBAR();
  tagO = lds_tag(L + L_EP);
  const u64 tagP = lds_tag(L + L_EP + 8), tagG = lds_tag(L + L_EP + 16);
  if (BR == 0 && c == 1) __builtin_amdgcn_s_setprio(1);
  f32x16 accQ = {};
  {
    bf16x8 wq[5];
    auto rd2 = [&](int s) LAM {
      const lptr w = s < 16 ? wb[1] : wb[2];
      const int o = (s < 16 ? SLOT : -16 * 2048) + s * 2048;
      wq[s % 5] = tr_frag(w, o, o + 512);
    };
    rd2(0); rd2(1); rd2(2); rd2(3);
    SBAR();
#pragma unroll
    for (int s = 0; s < 32; ++s) {
      if (s + 4 < 32) rd2(s + 4);
      accQ = MFMA(wq[s % 5], xf[s], accQ);
      if (s >= 12 && s < 20) wo_piece(0, wid * 8 + (s - 12));
      if (s == PUBS) {
        if (BR == 0 ? (wid == 3) : (sp == 1 && rt < 15)) {
          WAIT_VM(0);
          if (lane == 0) flag_store(ws, BR == 0 ? WS_FKV + (quad * 16 + rt) * 128 : WS_FH + ((rt * 8 + quad) * 4 + c * 2 + p) * 64, tagP);
          STAMP(14);
        }
      }
      SBAR();
    }
  }
  bf16x8 Qf[2];
  if (BR == 0) { Qf[0] = phi8(accQ, 0); Qf[1] = phi8(accQ, 8); }
  else { Qf[0] = pack8(accQ, 0); Qf[1] = pack8(accQ, 8); }
  STAMP(4);
  asm volatile("s_waitcnt vmcnt(0) lgkmcnt(0)" ::: "memory");
  if (lane == 0) __hip_atomic_fetch_add((LAS unsigned*)(L + L_CNT), 1u, __ATOMIC_RELAXED, __HIP_MEMORY_SCOPE_WORKGROUP);
  STAMP(16);
  auto gate = [&](auto pre) LAM {
    f32x16 ga = {};
    bf16x8 gf[4];
    const lptr gp = L + L_GW + h * 16;
#pragma unroll
    for (int t = 0; t < 3; ++t) gf[t] = lds_frag(gp + t * 32);
    SBAR();
#pragma unroll
    for (int t = 0; t < 32; ++t) {
      pre(t);
      if (t + 3 < 32) gf[(t + 3) % 4] = lds_frag(gp + (t + 3) * 32);
      ga = MFMA(gf[t % 4], xf[t], ga);
      SBAR();
    }
    const float g = __builtin_amdgcn_rcpf(1.f + __builtin_amdgcn_exp2f(-LOG2E * ga[0]));
    if (h == 0) *(LAS float*)(L + L_G + (p * 32 + i32) * 4) = g;
  };
  const unsigned ofrag = WS_O + ((((rt * 2 + sp) * 2 + p) * 64) + BR * 32 + quad * 4 + 2 * c) * 1024 + lane * 16;
  if (BR == 0) {
    if (wid < 2) { gate([&](int) LAM {}); STAMP(20); }
    if (c == 0) {
      lds_wait(L + L_CNT, 8);
      STAMP(21);
      read_wo(wo, L, wid >> 1, lane);
      STAMP(22);
      lds_wait(L + L_UIS, 4);
      STAMP(17);
#pragma unroll
      for (int i = 0; i < 16; ++i) wo_piece(1, wid * 16 + i);
      STAMP(18);
    }
    if (c == 1) {
      poll_flags(ws, WS_FKV + quad * 16 * 128, 128, 16, tagG, lane);
      STAMP(5);
      const unsigned u = rq * 64 + lane;
      f32x4 a[16];
#pragma unroll
      for (int r = 0; r < 16; ++r)
        a[r] = __builtin_bit_cast(f32x4, __builtin_amdgcn_raw_buffer_load_b128(rsWS, WS_KVP + quad * 16 * KVP_BYTES + u * 16, r * KVP_BYTES, LD_AUX));
      f32x4 zv = __builtin_bit_cast(f32x4, __builtin_amdgcn_raw_buffer_load_b128(rsWS, WS_KVP + (quad * 16 + (lane >> 2)) * KVP_BYTES + (256 + rq * 4 + (lane & 3)) * 16, 0, LD_AUX));
      SBAR();
      if (lane == 0) __hip_atomic_fetch_add((LAS unsigned*)(L + L_UIS), 1u, __ATOMIC_RELAXED, __HIP_MEMORY_SCOPE_WORKGROUP);
      SBAR();
#pragma unroll
      for (int s = 1; s < 16; s *= 2)
#pragma unroll
        for (int r = 0; r < 16; r += 2 * s) a[r] += a[r + s];
      *(LAS f32x4*)(L + L_TOT + u * 16) = a[0];
#pragma unroll
      for (int j = 0; j < 4; ++j) zv[j] = sum_lanes_mod4(zv[j]);
      if (lane < 4) *(LAS f32x4*)(L + L_TOT + (256 + rq * 4 + lane) * 16) = zv;
      STAMP(23);
    }
    LBAR();
    STAMP(6);
    const f32x4 u0 = *(LAS f32x4*)(L + L_TOT + (c * 128 + lane) * 16), u1 = *(LAS f32x4*)(L + L_TOT + (c * 128 + 64 + lane) * 16);
    const lptr zp = L + L_TOT + 4096 + c * 128 + h * 16;
    const f32x4 z0 = *(LAS f32x4*)zp, z1 = *(LAS f32x4*)(zp + 32), z2 = *(LAS f32x4*)(zp + 64), z3 = *(LAS f32x4*)(zp + 96);
    const bool lo = i32 < 16;
    const u32x4 pu = {pk(u0[0], u0[1]), pk(u0[2], u0[3]), pk(u1[0], u1[1]), pk(u1[2], u1[3])};
    const u32x4 pz0 = {pk(z0[0], z0[1]), pk(z0[2], z0[3]), pk(z1[0], z1[1]), pk(z1[2], z1[3])};
    const u32x4 pz1 = {pk(z2[0], z2[1]), pk(z2[2], z2[3]), pk(z3[0], z3[1]), pk(z3[2], z3[3])};
    u32x4 a0, a1;
#pragma unroll
    for (int j = 0; j < 4; ++j) { a0[j] = lo ? pu[j] : pz0[j]; a1[j] = lo ? pz1[j] : pu[j]; }
    const f32x16 zero = {};
    const f32x16 d0 = MFMA(__builtin_bit_cast(bf16x8, a0), Qf[0], zero);
    const f32x16 d1 = MFMA(__builtin_bit_cast(bf16x8, a1), Qf[1], zero);
    const float s0 = __builtin_amdgcn_rcpf(d0[8] + 1e-6f), s1 = __builtin_amdgcn_rcpf(d1[0] + 1e-6f);
    const u32x4 o0 = {pk(d0[0] * s0, d0[1] * s0), pk(d0[2] * s0, d0[3] * s0), pk(d0[4] * s0, d0[5] * s0), pk(d0[6] * s0, d0[7] * s0)};
    const u32x4 o1 = {pk(d1[8] * s1, d1[9] * s1), pk(d1[10] * s1, d1[11] * s1), pk(d1[12] * s1, d1[13] * s1), pk(d1[14] * s1, d1[15] * s1)};
    __builtin_amdgcn_raw_buffer_store_b128(o0, rsWS, ofrag, 0, 16);
    __builtin_amdgcn_raw_buffer_store_b128(o1, rsWS, ofrag + 1024, 0, 16);
    if (rt == 0 && wid == 4 && lane == 0) flag_store(ws, WS_EKV + quad * 128, tagG);
  } else {
    bf16x8 Kp[2];
    u32x2 vp;
    if (sp == 1) {
      const lptr ex = L + L_PART + (c * 2 + p) * 1024;
      Kp[0] = lds_frag(ex + h * 128 + (i32 & 7) * 16);
      Kp[1] = lds_frag(ex + 256 + h * 128 + (i32 & 7) * 16);
      vp = *(LAS u32x2*)(ex + 512 + lane * 8);
    } else if (rt > 0) {
      poll_flags(ws, WS_FH + (((rt - 1) * 8 + quad) * 4 + c * 2 + p) * 64, 64, 1, tagG, lane);
      const unsigned ex = WS_HALO + (((rt - 1) * 8 + quad) * 4 + c * 2 + p) * 1024;
      Kp[0] = __builtin_bit_cast(bf16x8, __builtin_amdgcn_raw_buffer_load_b128(rsWS, ex + h * 128 + (i32 & 7) * 16, 0, LD_AUX));
      Kp[1] = __builtin_bit_cast(bf16x8, __builtin_amdgcn_raw_buffer_load_b128(rsWS, ex + 256 + h * 128 + (i32 & 7) * 16, 0, LD_AUX));
      vp = __builtin_amdgcn_raw_buffer_load_b64(rsWS, ex + 512 + lane * 8, 0, LD_AUX);
      if (wid == 0 && lane == 0) flag_store(ws, WS_EH + ((rt - 1) * 8 + quad) * 128, tagG);
    } else {
      Kp[0] = Kf[0]; Kp[1] = Kf[1]; vp = (u32x2){0u, 0u};
    }
    STAMP(5);
    STAMP(6);
    const bool noprev = (rt == 0 && sp == 0);
    f32x16 bias;
#pragma unroll
    for (int r = 0; r < 16; ++r) bias[r] = ((unsigned)(i32 - ((r & 3) + 8 * (r >> 2) + 4 * h)) <= 7u) ? 0.f : -1e30f;
    float bprev[4];
#pragma unroll
    for (int j = 0; j < 4; ++j) bprev[j] = (!noprev && (24 + 4 * h + j >= i32 + 25)) ? 0.f : -1e30f;
    constexpr float C1 = 0.25f * LOG2E;
    const unsigned ONE2 = 0x3F803F80u;
    const f32x16 zero = {};
#pragma unroll
    for (int hh = 0; hh < 2; ++hh) {
      f32x16 sc = MFMA(Kf[hh], Qf[hh], bias);
      const f32x16 sv = MFMA(Kp[hh], Qf[hh], zero);
      float sp4[4];
#pragma unroll
      for (int j = 0; j < 4; ++j) sp4[j] = sv[12 + j] + bprev[j];
      float m = __builtin_fmaxf(__builtin_fmaxf(sp4[0], sp4[1]), __builtin_fmaxf(sp4[2], sp4[3]));
#pragma unroll
      for (int r = 0; r < 16; r += 2) m = __builtin_fmaxf(__builtin_fmaxf(m, sc[r]), sc[r + 1]);
      m = swap_max(m);
      const float nm = -m * C1;
#pragma unroll
      for (int r = 0; r < 16; ++r) sc[r] = __builtin_amdgcn_exp2f(__builtin_fmaf(sc[r], C1, nm));
#pragma unroll
      for (int j = 0; j < 4; ++j) sp4[j] = __builtin_amdgcn_exp2f(__builtin_fmaf(sp4[j], C1, nm));
      const bf16x8 P0 = pack8(sc, 0), P1 = pack8(sc, 8);
      const u32x4 ppv = {0u, 0u, pk(sp4[0], sp4[1]), pk(sp4[2], sp4[3])};
      const bool mine = (i32 < 16) == (hh == 0);
      const u32x4 v0 = __builtin_bit_cast(u32x4, Vf[0]), v1 = __builtin_bit_cast(u32x4, Vf[1]);
      u32x4 a0, a1, ap;
#pragma unroll
      for (int j = 0; j < 4; ++j) { a0[j] = mine ? v0[j] : ONE2; a1[j] = mine ? v1[j] : ONE2; }
      ap = (u32x4){0u, 0u, mine ? vp[0] : ONE2, mine ? vp[1] : ONE2};
      f32x16 o = MFMA(__builtin_bit_cast(bf16x8, a0), P0, zero);
      o = MFMA(__builtin_bit_cast(bf16x8, a1), P1, o);
      o = MFMA(__builtin_bit_cast(bf16x8, ap), __builtin_bit_cast(bf16x8, ppv), o);
      const int nb = hh == 0 ? 0 : 8, db = hh == 0 ? 8 : 0;
      const float s = __builtin_amdgcn_rcpf(o[db]);
      const u32x4 ov = {pk(o[nb] * s, o[nb + 1] * s), pk(o[nb + 2] * s, o[nb + 3] * s), pk(o[nb + 4] * s, o[nb + 5] * s), pk(o[nb + 6] * s, o[nb + 7] * s)};
      __builtin_amdgcn_raw_buffer_store_b128(ov, rsWS, ofrag + hh * 1024, 0, 16);
    }
  }
  STAMP(7);
  WAIT_VM(0);
  if (lane == 0) flag_store(ws, WS_FO + (((((rt * 2 + sp) * 2 + p) * 2 + BR) * 8 + quad) * 2 + c) * 64, tagO);
  if (BR == 0 && c == 0 && lane == 0) __hip_atomic_fetch_add((LAS unsigned*)(L + L_WOB), 1u, __ATOMIC_RELAXED, __HIP_MEMORY_SCOPE_WORKGROUP);
  STAMP(8);
  __builtin_amdgcn_s_setprio(0);
  if (BR == 1 && c == 1) {
    lds_wait(L + L_CNT, 8);
#pragma unroll
    for (int i = 0; i < 16; ++i) wo_piece(1, rq * 16 + i);
    STAMP(18);
  }
  if (BR == 1 && wid >= 6) gate([&](int) LAM {});
}

__global__ __launch_bounds__(512, 2) void gated_mix_kernel(const __hip_bfloat16* __restrict__ x, const __hip_bfloat16* __restrict__ wA,
                                                           const __hip_bfloat16* __restrict__ woA, const __hip_bfloat16* __restrict__ wB,
                                                           const __hip_bfloat16* __restrict__ woB, const __hip_bfloat16* __restrict__ gw,
                                                           __hip_bfloat16* __restrict__ out, char* ws) {
  __shared__ __attribute__((aligned(1024))) char smem[LDS_BYTES];
  const int tid = threadIdx.x, lane = tid & 63, wid = __builtin_amdgcn_readfirstlane(tid >> 6);
  const int b = blockIdx.x, xcd = b & 7, loc = b >> 3;
#define XMAP 1
  const int br = XMAP ? (xcd >> 2) : (xcd & 1), rt = (XMAP ? (xcd & 3) : (xcd >> 1)) * 4 + (loc >> 3), quad = loc & 7;
  unsigned vts = 0;
  STAMP(0);
  const rsrc_t rsX = __builtin_amdgcn_make_buffer_rsrc((void*)x, 0, 2048 * 512 * 2, 0x00020000);
  const rsrc_t rsW = __builtin_amdgcn_make_buffer_rsrc((void*)(br ? wB : wA), 0, 512 * 1536 * 2, 0x00020000);
  const rsrc_t rsWoA = __builtin_amdgcn_make_buffer_rsrc((void*)woA, 0, 512 * 512 * 2, 0x00020000);
  const rsrc_t rsWoB = __builtin_amdgcn_make_buffer_rsrc((void*)woB, 0, 512 * 512 * 2, 0x00020000);
  const rsrc_t rsG = __builtin_amdgcn_make_buffer_rsrc((void*)gw, 0, 1024, 0x00020000);
  const rsrc_t rsWS = __builtin_amdgcn_make_buffer_rsrc((void*)ws, 0, WS_USED, 0x00020000);
  const lptr L = (lptr)smem;
  const unsigned lds0 = (unsigned)(uintptr_t)L;

  const int l8 = lane >> 3, c8 = lane & 7;
  const int xrow = rt * 128 + 64 * (wid >> 2) + ((wid >> 1) & 1) + 2 * (16 * (wid & 1) + l8);
  const unsigned vX0 = xrow * 1024 + ((c8 ^ (lane >> 4)) << 4);
  const unsigned vX1 = (xrow + 16) * 1024 + ((c8 ^ (4 + (lane >> 4))) << 4);
  const int wsw = (c8 ^ (((lane >> 4) & 1) << 2)) << 4;
  const unsigned vW = (8 * wid + l8) * 3072 + quad * 128 + wsw;
  const unsigned vWo = l8 * 1024 + quad * 128 + wsw;

  asm volatile("s_nop 4" ::: "memory");
#pragma unroll
  for (int f = 0; f < 2; ++f) {
    const unsigned sl = lds0 + f * SLOT;
    const unsigned kc = (f + ROT_OF(quad, rt)) & 7;
    dma16(sl + wid * 2048, vX0, rsX, kc * 128);
    dma16(sl + wid * 2048 + 1024, vX1, rsX, kc * 128);
    dma16(sl + 16384 + wid * 1024, vW, rsW, kc * 196608 + 1024);
    dma16(sl + 24576 + wid * 1024, vW, rsW, kc * 196608 + 2048);
    if (f < 1) asm volatile("s_barrier" ::: "memory");
  }
#pragma unroll
  for (int j = 0; j < 3; ++j) {
    const unsigned P = b * 24 + wid * 3 + j;
    const unsigned dst = lds0 + 4 * SLOT + (wid * 3 + j) * 1024;
    if (P < 2048) dma16(dst, lane * 16, rsX, P * 1024);
    else if (P < 3584) dma16(dst, lane * 16, __builtin_amdgcn_make_buffer_rsrc((void*)wA, 0, 512 * 1536 * 2, 0x00020000), (P - 2048) * 1024);
    else if (P < 5120) dma16(dst, lane * 16, __builtin_amdgcn_make_buffer_rsrc((void*)wB, 0, 512 * 1536 * 2, 0x00020000), (P - 3584) * 1024);
    else if (P < 5632) dma16(dst, lane * 16, rsWoA, (P - 5120) * 1024);
    else dma16(dst, lane * 16, rsWoB, (P - 5632) * 1024);
  }
  STAMP(1);
  u64 tagO;
  bf16x8 wo[32];
  if (br == 0) phase1<0>(smem, rsX, rsW, rsWoA, rsWoB, rsG, rsWS, ws, rt, quad, wid, lane, vX0, vX1, vW, vWo, vts, tagO, wo);
  else phase1<1>(smem, rsX, rsW, rsWoA, rsWoB, rsG, rsWS, ws, rt, quad, wid, lane, vX0, vX1, vW, vWo, vts, tagO, wo);

  const int pt = wid & 1, kq = (wid >> 2) * 2 + ((wid >> 1) & 1);
  const int i32 = lane & 31, h = lane >> 5;
  const unsigned fwait = WS_FO + (((((rt * 2 + br) * 2 + pt) * 2 + (kq >> 1)) * 8 + (kq & 1) * 4) * 2) * 64;
  const unsigned ob = WS_O + ((((rt * 2 + br) * 2 + pt) * 64) + kq * 16) * 1024 + lane * 16;
  bf16x8 of[16];
  auto load_o = [&]() LAM {
#pragma unroll
    for (int i = 0; i < 16; ++i) of[i] = __builtin_bit_cast(bf16x8, __builtin_amdgcn_raw_buffer_load_b128(rsWS, ob, i * 1024, LD_AUX));
  };
  bool ready = false;
  if (!(br == 0 && kq < 2)) {
    u64 v0 = tagO;
    if (lane < 8) v0 = __hip_atomic_load((gu64*)(ws + fwait + lane * 64), __ATOMIC_RELAXED, __HIP_MEMORY_SCOPE_AGENT);
    ready = __builtin_amdgcn_ballot_w64(v0 != tagO) == 0;
    WAIT_VM(0);
    if (br == 1 && kq >= 2 && lane == 0) __hip_atomic_fetch_add((LAS unsigned*)(L + L_WOB), 1u, __ATOMIC_RELAXED, __HIP_MEMORY_SCOPE_WORKGROUP);
    STAMP(26);
    if (ready) load_o();
    if (kq >= 2) lds_wait(L + L_WOB, 4); else lds_wait(L + L_CNT, 8);
    STAMP(19);
    read_wo(wo, L, kq, lane);
  }
  STAMP(9);
  if (!ready) {
    poll_flags(ws, fwait, 64, 8, tagO, lane);
    load_o();
  }
  STAMP(10);
  SBAR();
  f32x16 a0 = {}, a1 = {};
#pragma unroll
  for (int i = 0; i < 16; ++i) { a0 = MFMA(wo[2 * i], of[i], a0); a1 = MFMA(wo[2 * i + 1], of[i], a1); }
  STAMP(11);
  LBAR();
  STAMP(24);
  {
    const lptr pr = L + L_RED + (kq * 64 + pt * 32 + i32) * PROW + h * 16;
#pragma unroll
    for (int g4 = 0; g4 < 4; ++g4) {
      *(LAS f32x4*)(pr + g4 * 32) = (f32x4){a0[4 * g4], a0[4 * g4 + 1], a0[4 * g4 + 2], a0[4 * g4 + 3]};
      *(LAS f32x4*)(pr + 128 + g4 * 32) = (f32x4){a1[4 * g4], a1[4 * g4 + 1], a1[4 * g4 + 2], a1[4 * g4 + 3]};
    }
  }
  LBAR();
  STAMP(13);
  {
    const int pos = tid >> 3, cg = tid & 7;
    const lptr pr = L + L_RED + pos * PROW + cg * 32;
    const float g = *(LAS float*)(L + L_G + pos * 4), gb = 1.f - g;
    const f32x4 s0 = (*(LAS f32x4*)pr + *(LAS f32x4*)(pr + 64 * PROW)) * g + (*(LAS f32x4*)(pr + 128 * PROW) + *(LAS f32x4*)(pr + 192 * PROW)) * gb;
    const f32x4 s1 = (*(LAS f32x4*)(pr + 16) + *(LAS f32x4*)(pr + 64 * PROW + 16)) * g + (*(LAS f32x4*)(pr + 128 * PROW + 16) + *(LAS f32x4*)(pr + 192 * PROW + 16)) * gb;
    const int row = rt * 128 + 64 * br + 2 * (pos & 31) + (pos >> 5);
    const u32x4 ov = {pk(s0[0], s0[1]), pk(s0[2], s0[3]), pk(s1[0], s1[1]), pk(s1[2], s1[3])};
    __builtin_amdgcn_raw_buffer_store_b128(ov, __builtin_amdgcn_make_buffer_rsrc((void*)out, 0, 2048 * 1024, 0x00020000), row * 1024 + quad * 128 + cg * 16, 0, 0);
  }
  if (quad == 0 && br == 0 && tid == 0) flag_store(ws, WS_EO + rt * 128, tagO);
  STAMP(12);
  TSDUMP();
}

#if NOPK && defined(__HIP_DEVICE_COMPILE__)
#pragma clang attribute pop
#endif

extern "C" void kernel_launch(void* const* d_in, const int* in_sizes, int n_in, void* d_out, int out_size, void* d_ws, size_t ws_size,
                              hipStream_t stream) {
  gated_mix_kernel<<<256, 512, 0, stream>>>((const __hip_bfloat16*)d_in[0], (const __hip_bfloat16*)d_in[1], (const __hip_bfloat16*)d_in[2],
                                            (const __hip_bfloat16*)d_in[4], (const __hip_bfloat16*)d_in[5], (const __hip_bfloat16*)d_in[6],
                                            (__hip_bfloat16*)d_out, (char*)d_ws);
}
```

```cpp
#include <hip/hip_runtime.h>
#include <hip/hip_bf16.h>
#include <stdint.h>

typedef __bf16 bf16x2 __attribute__((ext_vector_type(2)));
typedef __bf16 bf16x8 __attribute__((ext_vector_type(8)));
typedef short s16x4 __attribute__((ext_vector_type(4)));
typedef float f32x2 __attribute__((ext_vector_type(2)));
typedef float f32x4 __attribute__((ext_vector_type(4)));
typedef float f32x16 __attribute__((ext_vector_type(16)));
typedef unsigned u32x2 __attribute__((ext_vector_type(2)));
typedef unsigned u32x4 __attribute__((ext_vector_type(4)));
typedef unsigned long long u64;
#define LAS __attribute__((address_space(3)))
#define GAS __attribute__((address_space(1)))
#define DI __device__ __forceinline__
typedef LAS char* lptr;
typedef GAS u64 gu64;
typedef __amdgpu_buffer_rsrc_t rsrc_t;

constexpr unsigned WS_EO = 0;
constexpr unsigned WS_EKV = 2048;
constexpr unsigned WS_EH = 4096;
constexpr unsigned WS_FKV = 32768;
constexpr unsigned WS_FH = 65536;
constexpr unsigned WS_FO = 131072;
constexpr unsigned WS_KVP = 262144 + 65536;
constexpr unsigned KVP_BYTES = 4352;
constexpr unsigned WS_HALO = 1048576;
constexpr unsigned WS_O = 2097152;
constexpr unsigned WS_STAMP = 8u << 20;
constexpr unsigned WS_USED = 16u << 20;

constexpr int SLOT = 32768;
constexpr int L_GW = 0;
constexpr int L_G = L_GW + 1024;
constexpr int L_EP = L_GW + 1536;
constexpr int L_CNT = L_GW + 1568;
constexpr int L_WOB = L_GW + 1572;
constexpr int L_UIS = L_GW + 1576;
constexpr int L_PART = L_GW + 2048;
constexpr int L_TOT = L_PART + 8 * 2176;
constexpr int L_SINK = L_TOT + 4352;
constexpr int L_RED = SLOT;
constexpr int LDS_BYTES = 163840;
constexpr int PROW = 272;
#define slot_of(f) ((f) < 8 ? (f) % 5 : (f) == 8 ? 3 : 4)

constexpr float LOG2E = 1.4426950408889634f;
constexpr unsigned SPIN_CAP = 1u << 16;

#define MFMA(a, b, c) __builtin_amdgcn_mfma_f32_32x32x16_bf16((a), (b), (c), 0, 0, 0)
#define WAIT_BAR(N) asm volatile("s_waitcnt vmcnt(" #N ") lgkmcnt(0)\n\ts_barrier" ::: "memory")
#define WAIT_BAR_NL(N) asm volatile("s_waitcnt vmcnt(" #N ")\n\ts_barrier" ::: "memory")
#define WAIT_VM(N) asm volatile("s_waitcnt vmcnt(" #N ")" ::: "memory")
#define ROTM 6
#define ROT_OF(q, r) ((unsigned)(ROTM == 0 ? 0 : ROTM == 1 ? (q) : ROTM == 2 ? (q) + 2 * (r) : ROTM == 3 ? 2 * (r) : ROTM == 4 ? (r) : ROTM == 5 ? (r) + (q) : (r) + 4 * ((q) & 1)))
#define PUBS 3
#define PFW 1
#define DEEP 1
#define SBAR() __builtin_amdgcn_sched_barrier(0)
#define LBAR() asm volatile("s_waitcnt lgkmcnt(0)\n\ts_barrier" ::: "memory")

#define STAMP(i) do {} while (0)
#define TSDUMP() do {} while (0)

#define NOPK 0
#if NOPK && defined(__HIP_DEVICE_COMPILE__)
#define LAM __attribute__((target("no-packed-fp32-ops"), always_inline))
#else
#define LAM __attribute__((always_inline))
#endif
#if NOPK && defined(__HIP_DEVICE_COMPILE__)
#pragma clang attribute push(__attribute__((target("no-packed-fp32-ops"))), apply_to = function)
#endif
DI float as_f(unsigned u) { return __builtin_bit_cast(float, u); }
DI unsigned as_u(float f) { return __builtin_bit_cast(unsigned, f); }
DI unsigned pk(float a, float b) { return __builtin_bit_cast(unsigned, __builtin_convertvector((f32x2){a, b}, bf16x2)); }
DI bf16x8 pack8(const f32x16& a, int o) {
  u32x4 r = {pk(a[o], a[o + 1]), pk(a[o + 2], a[o + 3]), pk(a[o + 4], a[o + 5]), pk(a[o + 6], a[o + 7])};
  return __builtin_bit_cast(bf16x8, r);
}
DI unsigned phi_pk(float a, float b, f32x2& zsum) {
  const f32x2 t = {a, b};
  const f32x2 u = t * (f32x2){LOG2E, LOG2E}, w = t + (f32x2){1.f, 1.f};
  const float e0 = __builtin_amdgcn_fmed3f(__builtin_amdgcn_exp2f(u[0]), 0.f, 1.f), e1 = __builtin_amdgcn_fmed3f(__builtin_amdgcn_exp2f(u[1]), 0.f, 1.f);
  const f32x2 r = (f32x2){__builtin_fmaxf(w[0], e0), __builtin_fmaxf(w[1], e1)} + (f32x2){1e-6f, 1e-6f};
  zsum += r;
  return pk(r[0], r[1]);
}
DI float opq(float x) { asm("" : "+v"(x)); return x; }
DI unsigned phi_pk1(float a, float b, float& z0, float& z1) {
  const float e0 = __builtin_amdgcn_fmed3f(__builtin_amdgcn_exp2f(a * LOG2E), 0.f, 1.f), e1 = __builtin_amdgcn_fmed3f(__builtin_amdgcn_exp2f(b * LOG2E), 0.f, 1.f);
  const float r0 = opq(__builtin_fmaxf(a + 1.f, e0) + 1e-6f), r1 = __builtin_fmaxf(b + 1.f, e1) + 1e-6f;
  z0 = opq(z0 + r0); z1 += r1;
  return pk(r0, r1);
}
DI bf16x8 phi8(const f32x16& a, int o) {
  f32x2 unused = {};
  u32x4 r = {phi_pk(a[o], a[o + 1], unused), phi_pk(a[o + 2], a[o + 3], unused), phi_pk(a[o + 4], a[o + 5], unused), phi_pk(a[o + 6], a[o + 7], unused)};
  return __builtin_bit_cast(bf16x8, r);
}
DI void dma16(unsigned lds, unsigned voff, rsrc_t rs, unsigned soff) {
  asm volatile("s_mov_b32 m0, %0\n\ts_nop 0\n\tbuffer_load_dwordx4 %1, %2, %3 offen lds" ::"s"(lds), "v"(voff), "s"(rs), "s"(soff) : "memory");
}
DI void dma4(unsigned lds, unsigned voff, rsrc_t rs) {
  asm volatile("s_mov_b32 m0, %0\n\ts_nop 0\n\tbuffer_load_dword %1, %2, 0 offen sc1 lds" ::"s"(lds), "v"(voff), "s"(rs) : "memory");
}
DI u64 lds_tag(lptr p) {
  const u32x2 v = *(const LAS u32x2*)p;
  return (((u64)(unsigned)__builtin_amdgcn_readfirstlane(v[1]) << 32) | (unsigned)__builtin_amdgcn_readfirstlane(v[0])) + 1;
}
DI bf16x8 lds_frag(lptr p) { return *(const LAS bf16x8*)p; }
DI bf16x8 tr_frag(lptr p, int o0, int o1) {
  const s16x4 lo = __builtin_bit_cast(s16x4, __builtin_amdgcn_ds_read_tr16_b64_v4i16((LAS s16x4*)(p + o0)));
  const s16x4 hi = __builtin_bit_cast(s16x4, __builtin_amdgcn_ds_read_tr16_b64_v4i16((LAS s16x4*)(p + o1)));
  return __builtin_bit_cast(bf16x8, __builtin_shufflevector(lo, hi, 0, 1, 2, 3, 4, 5, 6, 7));
}
DI float sel(bool c, float a, float b) { asm("" : "+v"(a), "+v"(b)); return c ? a : b; }
#define ACQ 0
constexpr int LD_AUX = ACQ ? 0 : 16;
DI void acquire() { if (ACQ) __builtin_amdgcn_fence(__ATOMIC_ACQUIRE, "agent"); }
DI void flag_store(char* ws, unsigned off, u64 v) { __hip_atomic_store((gu64*)(ws + off), v, __ATOMIC_RELAXED, __HIP_MEMORY_SCOPE_AGENT); }
DI void poll_flags(char* ws, unsigned off0, unsigned stride, int n, u64 tag, int lane) {
  gu64* p = (gu64*)(ws + off0 + (unsigned)lane * stride);
#pragma unroll 1
  for (unsigned spins = 0; spins < SPIN_CAP; ++spins) {
    u64 v = tag;
    if (lane < n) v = __hip_atomic_load(p, __ATOMIC_RELAXED, __HIP_MEMORY_SCOPE_AGENT);
    if (__builtin_amdgcn_ballot_w64(v != tag) == 0) break;
    __builtin_amdgcn_s_sleep(1);
  }
  __builtin_amdgcn_fence(__ATOMIC_ACQUIRE, "wavefront");
  asm volatile("" ::: "memory");
  acquire();
}
DI void lds_wait(lptr p, unsigned want) {
#pragma unroll 1
  for (unsigned spins = 0; spins < SPIN_CAP; ++spins) {
    if ((unsigned)__builtin_amdgcn_readfirstlane(__hip_atomic_load((LAS unsigned*)p, __ATOMIC_RELAXED, __HIP_MEMORY_SCOPE_WORKGROUP)) == want) break;
    __builtin_amdgcn_s_sleep(2);
  }
  asm volatile("" ::: "memory");
}
DI float sum_lanes_mod4(float x) {
  x += as_f(__builtin_amdgcn_update_dpp(0u, as_u(x), 0x124, 0xf, 0xf, false));
  x += as_f(__builtin_amdgcn_update_dpp(0u, as_u(x), 0x128, 0xf, 0xf, false));
  auto a = __builtin_amdgcn_permlane16_swap(as_u(x), as_u(x), false, false);
  x = as_f(a[0]) + as_f(a[1]);
  auto b = __builtin_amdgcn_permlane32_swap(as_u(x), as_u(x), false, false);
  return as_f(b[0]) + as_f(b[1]);
}
DI float swap_max(float m) {
  auto r = __builtin_amdgcn_permlane32_swap(as_u(m), as_u(m), false, false);
  return __builtin_fmaxf(as_f(r[0]), as_f(r[1]));
}

DI void read_wo(bf16x8 (&wo)[32], lptr L, int kq, int lane) {
  const int h = lane >> 5, q4 = (lane & 15) >> 2, p4 = lane & 3, nsub = (lane >> 4) & 1;
  const lptr base = L + (kq + 1) * SLOT + (4 * h + q4) * 128 + 8 * (p4 & 1);
  const int sw = ((q4 >> 1) & 1) << 2;
  const lptr b0 = base + ((2 * nsub + (p4 >> 1)) ^ sw) * 16, b1 = base + ((4 + 2 * nsub + (p4 >> 1)) ^ sw) * 16;
#pragma unroll
  for (int i = 0; i < 16; ++i) {
    wo[2 * i] = tr_frag(b0, i * 2048, i * 2048 + 1024);
    wo[2 * i + 1] = tr_frag(b1, i * 2048, i * 2048 + 1024);
  }
}

template <int BR>
DI void phase1(char* smem_generic, const rsrc_t rsX, const rsrc_t rsW, const rsrc_t rsWoA, const rsrc_t rsWoB, const rsrc_t rsG, const rsrc_t rsWS, char* ws,
               const int rt, const int quad, const int loc, const int wid, const int lane, const unsigned vX0, const unsigned vX1, const unsigned vW,
               const unsigned vWo, unsigned& vts, u64& tagO, bf16x8 (&wo)[32]) {
  const lptr L = (lptr)smem_generic;
  const unsigned lds0 = (unsigned)(uintptr_t)L;
  const int c = wid >> 2, rq = wid & 3, p = rq & 1, sp = rq >> 1;
  const int i32 = lane & 31, h = lane >> 5;
  const unsigned pieceX = lds0 + wid * 2048, pieceW = lds0 + wid * 1024;
  const unsigned rot = ROT_OF(quad, rt);

  auto piece = [&](int f, int i) LAM {
    const unsigned sl = (unsigned)slot_of(f) * SLOT;
    if (f < 8) {
      const unsigned kc = (f + rot) & 7;
      if (i == 0) dma16(pieceX + sl, vX0, rsX, kc * 128);
      else if (i == 1) dma16(pieceX + sl + 1024, vX1, rsX, kc * 128);
      else dma16(pieceW + sl + 16384 + (i - 2) * 8192, vW, rsW, kc * 196608 + (i - 1) * 1024);
    } else {
      dma16(pieceW + sl + i * 8192, vW, rsW, (((f - 8) * 4 + i + rot) & 7) * 196608);
    }
  };
  auto wo_piece = [&](int which, unsigned q) LAM { dma16(lds0 + (which ? 3 : 1) * SLOT + q * 1024, vWo, which ? rsWoB : rsWoA, q * 8192); };

  const int xs = h ^ ((i32 >> 1) & 7);
  const int q4 = (lane & 15) >> 2, p4 = lane & 3, nsub = (lane >> 4) & 1;
  const int wchunk = (4 * c + 2 * nsub + (p4 >> 1)) ^ (((q4 >> 1) & 1) << 2);
  lptr xb[3][4], wb[3];
#pragma unroll
  for (int t = 0; t < 4; ++t) {
    xb[0][t] = L + (32 * rq + i32) * 128 + ((xs ^ (2 * t)) << 4);
    xb[1][t] = xb[0][t] + 2 * SLOT;
    xb[2][t] = xb[0][t] + 4 * SLOT;
  }
  wb[0] = L + (8 * h + q4) * 128 + wchunk * 16 + 8 * (p4 & 1);
  wb[1] = wb[0] + 2 * SLOT;
  wb[2] = wb[0] + 4 * SLOT;
#pragma unroll
  for (int t = 0; t < 4; ++t) { asm("" : "+v"(xb[0][t])); asm("" : "+v"(xb[1][t])); asm("" : "+v"(xb[2][t])); }
  asm("" : "+v"(wb[0])); asm("" : "+v"(wb[1])); asm("" : "+v"(wb[2]));

  bf16x8 xf[32], wk[3], wv[3];
  f32x16 accK = {}, accV = {};
  constexpr int VLAG = BR == 0 ? 8 : 0;
  auto rdv = [&](int s, int buf) LAM {
    const int sl = slot_of(s >> 2), o = (sl & 1) * SLOT + 16384 + 8192 + (s & 3) * 2048;
    wv[buf] = tr_frag(wb[sl >> 1], o, o + 512);
  };
  auto rd1 = [&](int s) LAM {
    if (s < 32) {
      const int sl = slot_of(s >> 2), t = s & 3;
      xf[s] = lds_frag(sl < 4 ? xb[sl >> 1][t] + (sl & 1) * SLOT : xb[2][t]);
      const int o = (sl & 1) * SLOT + 16384 + t * 2048;
      wk[s % 3] = tr_frag(wb[sl >> 1], o, o + 512);
      if (s < 32 - VLAG) rdv(s, s % 3);
    } else {
      rdv(s - VLAG, s % 3);
    }
  };
  WAIT_BAR(4);
  STAMP(15);
  rd1(0); rd1(1);
  SBAR();
  unsigned kpk[8];
  float zs0 = 0.f, zs1 = 0.f;
#pragma unroll
  for (int s = 0; s < 32 + VLAG; ++s) {
    const int cc = s >> 2;
    if ((s & 3) == 2 && cc < 7) {
      if (s == 2) WAIT_BAR(4); else if (s == 26) WAIT_BAR(8); else WAIT_BAR(12);
      if (cc == 6 && wid == 0) {
        dma16(lds0 + L_GW, (lane * 16 + rot * 128) & 1023, rsG, 0);
        const unsigned e0 = WS_EO + rt * 128;
        const unsigned e1 = BR == 0 ? WS_EKV + quad * 128 : WS_EH + (rt * 8 + quad) * 128;
        const unsigned e2 = BR == 0 ? e1 : WS_EH + ((rt > 0 ? rt - 1 : 0) * 8 + quad) * 128;
        const unsigned eo = (lane < 2 ? e0 : lane < 4 ? e1 : e2) + (lane & 1) * 4;
        if (lane < 6) dma4(lds0 + L_EP, eo, rsWS);
        if (lane < 3) *(LAS unsigned*)(L + L_CNT + lane * 4) = 0;
      }
    }
    if (s >= 27 && s < 31) {
      const unsigned P = loc * 32 + wid * 4 + (s - 27);
      dma16(lds0 + L_SINK, lane * 16, P < 512 ? rsWoA : rsWoB, (P & 511) * 1024);
    }
    if (s < 2) { piece(2, 2 * s); piece(2, 2 * s + 1); }
    else if (s < 6) {
#pragma unroll
      for (int j = 3 * (s - 2); j < 3 * (s - 2) + 3; ++j) piece(3 + (j >> 2), j & 3);
    }
    else if (((s - 2) >> 2) + 5 < 10) piece(((s - 2) >> 2) + 5, (s - 2) & 3);
    if (s + 2 < 32 + VLAG) rd1(s + 2);
    if (s < 32) accK = BR == 0 ? MFMA(xf[s], wk[s % 3], accK) : MFMA(wk[s % 3], xf[s], accK);
    if (s < 32 - VLAG) accV = MFMA(xf[s], wv[s % 3], accV);
    if (s >= 32) {
      accV = MFMA(xf[s - VLAG], wv[s % 3], accV);
      kpk[s - 32] = phi_pk1(accK[2 * (s - 32)], accK[2 * (s - 32) + 1], zs0, zs1);
    }
    SBAR();
  }
  STAMP(2);
  bf16x8 Kf[2], Vf[2];
  if (BR == 0) {
    Kf[0] = __builtin_bit_cast(bf16x8, (u32x4){kpk[0], kpk[1], kpk[2], kpk[3]});
    Kf[1] = __builtin_bit_cast(bf16x8, (u32x4){kpk[4], kpk[5], kpk[6], kpk[7]});
  } else { Kf[0] = pack8(accK, 0); Kf[1] = pack8(accK, 8); }
  Vf[0] = pack8(accV, 0); Vf[1] = pack8(accV, 8);
  if (BR == 0) {
    f32x16 kv = {};
    kv = MFMA(Kf[0], Vf[0], kv);
    kv = MFMA(Kf[1], Vf[1], kv);
    const lptr pp = L + L_PART + wid * 2176;
    {
      const float zh = zs0 + zs1;
      auto sw = __builtin_amdgcn_permlane32_swap(as_u(zh), as_u(zh), false, false);
      if (h == 0) *(LAS float*)(pp + 2048 + i32 * 4) = as_f(sw[0]) + as_f(sw[1]);
    }
    const bool lo = i32 < 16;
    f32x4 u0, u1;
#pragma unroll
    for (int j = 0; j < 4; ++j) { u0[j] = sel(lo, kv[j], kv[8 + j]); u1[j] = sel(lo, kv[4 + j], kv[12 + j]); }
    *(LAS f32x4*)(pp + lane * 16) = u0;
    *(LAS f32x4*)(pp + 1024 + lane * 16) = u1;
  } else {
    if (sp == 0) {
      const lptr ex = L + L_PART + (c * 2 + p) * 1024;
      if (i32 >= 24) {
        *(LAS bf16x8*)(ex + h * 128 + (i32 - 24) * 16) = Kf[0];
        *(LAS bf16x8*)(ex + 256 + h * 128 + (i32 - 24) * 16) = Kf[1];
      }
      const u32x4 v1 = __builtin_bit_cast(u32x4, Vf[1]);
      *(LAS u32x2*)(ex + 512 + lane * 8) = (u32x2){v1[2], v1[3]};
    }
  }
  WAIT_BAR(4);
  STAMP(3);
  if (BR == 0) {
    if (wid == 3) {
      __builtin_amdgcn_s_setprio(3);
      f32x4 pa[5][4];
#pragma unroll
      for (int it = 0; it < 5; ++it) {
        const int u = it < 4 ? it * 64 + lane : 256 + (lane & 15);
        const int cc = it < 4 ? (it >> 1) : ((lane >> 3) & 1);
        const int o = it < 4 ? (u & 127) * 16 : 2048 + (lane & 7) * 16;
        const lptr s = L + L_PART + cc * 4 * 2176 + o;
#pragma unroll
        for (int r = 0; r < 4; ++r) pa[it][r] = *(LAS f32x4*)(s + r * 2176);
      }
      SBAR();
#pragma unroll
      for (int it = 0; it < 5; ++it) {
        f32x4 a;
#pragma unroll
        for (int j = 0; j < 4; ++j) a[j] = opq(opq(pa[it][0][j] + pa[it][1][j]) + opq(pa[it][2][j] + pa[it][3][j]));
        const unsigned go = WS_KVP + (quad * 16 + rt) * KVP_BYTES + (it < 4 ? it * 64 + lane : 256 + lane) * 16;
        if (it < 4 || lane < 16) __builtin_amdgcn_raw_buffer_store_b128(__builtin_bit_cast(u32x4, a), rsWS, go, 0, 16);
      }
      __builtin_amdgcn_s_setprio(0);
    }
  } else {
    if (sp == 1 && rt < 15) {
      const unsigned ex = WS_HALO + ((rt * 8 + quad) * 4 + c * 2 + p) * 1024;
      if (i32 >= 24) {
        __builtin_amdgcn_raw_buffer_store_b128(__builtin_bit_cast(u32x4, Kf[0]), rsWS, ex + h * 128 + (i32 - 24) * 16, 0, 16);
        __builtin_amdgcn_raw_buffer_store_b128(__builtin_bit_cast(u32x4, Kf[1]), rsWS, ex + 256 + h * 128 + (i32 - 24) * 16, 0, 16);
      }
      const u32x4 v1 = __builtin_bit_cast(u32x4, Vf[1]);
      __builtin_amdgcn_raw_buffer_store_b64((u32x2){v1[2], v1[3]}, rsWS, ex + 512 + lane * 8, 0, 16);
    }
  }
  SBAR();
  tagO = lds_tag(L + L_EP);
  const u64 tagP = lds_tag(L + L_EP + 8), tagG = lds_tag(L + L_EP + 16);
  if (BR == 0 && c == 1) __builtin_amdgcn_s_setprio(1);
  f32x16 accQ = {};
  {
    bf16x8 wq[5];
    auto rd2 = [&](int s) LAM {
      const lptr w = s < 16 ? wb[1] : wb[2];
      const int o = (s < 16 ? SLOT : -16 * 2048) + s * 2048;
      wq[s % 5] = tr_frag(w, o, o + 512);
    };
    rd2(0); rd2(1); rd2(2); rd2(3);
    SBAR();
#pragma unroll
    for (int s = 0; s < 32; ++s) {
      if (s + 4 < 32) rd2(s + 4);
      accQ = MFMA(wq[s % 5], xf[s], accQ);
      if (s >= 12 && s < 20) wo_piece(0, wid * 8 + (s - 12));
      if (s == PUBS) {
        if (BR == 0 ? (wid == 3) : (sp == 1 && rt < 15)) {
          WAIT_VM(0);
          if (lane == 0) flag_store(ws, BR == 0 ? WS_FKV + (quad * 16 + rt) * 128 : WS_FH + ((rt * 8 + quad) * 4 + c * 2 + p) * 64, tagP);
          STAMP(14);
        }
      }
      SBAR();
    }
  }
  bf16x8 Qf[2];
  if (BR == 0) { Qf[0] = phi8(accQ, 0); Qf[1] = phi8(accQ, 8); }
  else { Qf[0] = pack8(accQ, 0); Qf[1] = pack8(accQ, 8); }
  STAMP(4);
  asm volatile("s_waitcnt vmcnt(0) lgkmcnt(0)" ::: "memory");
  if (lane == 0) __hip_atomic_fetch_add((LAS unsigned*)(L + L_CNT), 1u, __ATOMIC_RELAXED, __HIP_MEMORY_SCOPE_WORKGROUP);
  STAMP(16);
  auto gate = [&](auto pre) LAM {
    f32x16 ga = {};
    bf16x8 gf[4];
    const lptr gp = L + L_GW + h * 16;
#pragma unroll
    for (int t = 0; t < 3; ++t) gf[t] = lds_frag(gp + t * 32);
    SBAR();
#pragma unroll
    for (int t = 0; t < 32; ++t) {
      pre(t);
      if (t + 3 < 32) gf[(t + 3) % 4] = lds_frag(gp + (t + 3) * 32);
      ga = MFMA(gf[t % 4], xf[t], ga);
      SBAR();
    }
    const float g = __builtin_amdgcn_rcpf(1.f + __builtin_amdgcn_exp2f(-LOG2E * ga[0]));
    if (h == 0) *(LAS float*)(L + L_G + (p * 32 + i32) * 4) = g;
  };
  const unsigned ofrag = WS_O + ((((rt * 2 + sp) * 2 + p) * 64) + BR * 32 + quad * 4 + 2 * c) * 1024 + lane * 16;
  if (BR == 0) {
    if (wid < 2) { gate([&](int) LAM {}); STAMP(20); }
    if (c == 0) {
      lds_wait(L + L_CNT, 8);
      STAMP(21);
      read_wo(wo, L, wid >> 1, lane);
      STAMP(22);
    }
    if (c == 1) {
      poll_flags(ws, WS_FKV + quad * 16 * 128, 128, 16, tagG, lane);
      STAMP(5);
      const unsigned u = rq * 64 + lane;
      f32x4 a[16];
#pragma unroll
      for (int r = 0; r < 16; ++r)
        a[r] = __builtin_bit_cast(f32x4, __builtin_amdgcn_raw_buffer_load_b128(rsWS, WS_KVP + quad * 16 * KVP_BYTES + u * 16, r * KVP_BYTES, LD_AUX));
      f32x4 zv = __builtin_bit_cast(f32x4, __builtin_amdgcn_raw_buffer_load_b128(rsWS, WS_KVP + (quad * 16 + (lane >> 2)) * KVP_BYTES + (256 + rq * 4 + (lane & 3)) * 16, 0, LD_AUX));
      SBAR();
#pragma unroll
      for (int s = 1; s < 16; s *= 2)
#pragma unroll
        for (int r = 0; r < 16; r += 2 * s) a[r] += a[r + s];
      *(LAS f32x4*)(L + L_TOT + u * 16) = a[0];
#pragma unroll
      for (int j = 0; j < 4; ++j) zv[j] = sum_lanes_mod4(zv[j]);
      if (lane < 4) *(LAS f32x4*)(L + L_TOT + (256 + rq * 4 + lane) * 16) = zv;
      STAMP(23);
    }
    LBAR();
    STAMP(6);
    const f32x4 u0 = *(LAS f32x4*)(L + L_TOT + (c * 128 + lane) * 16), u1 = *(LAS f32x4*)(L + L_TOT + (c * 128 + 64 + lane) * 16);
    const lptr zp = L + L_TOT + 4096 + c * 128 + h * 16;
    const f32x4 z0 = *(LAS f32x4*)zp, z1 = *(LAS f32x4*)(zp + 32), z2 = *(LAS f32x4*)(zp + 64), z3 = *(LAS f32x4*)(zp + 96);
    const bool lo = i32 < 16;
    const u32x4 pu = {pk(u0[0], u0[1]), pk(u0[2], u0[3]), pk(u1[0], u1[1]), pk(u1[2], u1[3])};
    const u32x4 pz0 = {pk(z0[0], z0[1]), pk(z0[2], z0[3]), pk(z1[0], z1[1]), pk(z1[2], z1[3])};
    const u32x4 pz1 = {pk(z2[0], z2[1]), pk(z2[2], z2[3]), pk(z3[0], z3[1]), pk(z3[2], z3[3])};
    u32x4 a0, a1;
#pragma unroll
    for (int j = 0; j < 4; ++j) { a0[j] = lo ? pu[j] : pz0[j]; a1[j] = lo ? pz1[j] : pu[j]; }
    const f32x16 zero = {};
    const f32x16 d0 = MFMA(__builtin_bit_cast(bf16x8, a0), Qf[0], zero);
    const f32x16 d1 = MFMA(__builtin_bit_cast(bf16x8, a1), Qf[1], zero);
    const float s0 = __builtin_amdgcn_rcpf(d0[8] + 1e-6f), s1 = __builtin_amdgcn_rcpf(d1[0] + 1e-6f);
    const u32x4 o0 = {pk(d0[0] * s0, d0[1] * s0), pk(d0[2] * s0, d0[3] * s0), pk(d0[4] * s0, d0[5] * s0), pk(d0[6] * s0, d0[7] * s0)};
    const u32x4 o1 = {pk(d1[8] * s1, d1[9] * s1), pk(d1[10] * s1, d1[11] * s1), pk(d1[12] * s1, d1[13] * s1), pk(d1[14] * s1, d1[15] * s1)};
    __builtin_amdgcn_raw_buffer_store_b128(o0, rsWS, ofrag, 0, 16);
    __builtin_amdgcn_raw_buffer_store_b128(o1, rsWS, ofrag + 1024, 0, 16);
    if (rt == 0 && wid == 4 && lane == 0) flag_store(ws, WS_EKV + quad * 128, tagG);
  } else {
    bf16x8 Kp[2];
    u32x2 vp;
    if (sp == 1) {
      const lptr ex = L + L_PART + (c * 2 + p) * 1024;
      Kp[0] = lds_frag(ex + h * 128 + (i32 & 7) * 16);
      Kp[1] = lds_frag(ex + 256 + h * 128 + (i32 & 7) * 16);
      vp = *(LAS u32x2*)(ex + 512 + lane * 8);
    } else if (rt > 0) {
      poll_flags(ws, WS_FH + (((rt - 1) * 8 + quad) * 4 + c * 2 + p) * 64, 64, 1, tagG, lane);
      const unsigned ex = WS_HALO + (((rt - 1) * 8 + quad) * 4 + c * 2 + p) * 1024;
      Kp[0] = __builtin_bit_cast(bf16x8, __builtin_amdgcn_raw_buffer_load_b128(rsWS, ex + h * 128 + (i32 & 7) * 16, 0, LD_AUX));
      Kp[1] = __builtin_bit_cast(bf16x8, __builtin_amdgcn_raw_buffer_load_b128(rsWS, ex + 256 + h * 128 + (i32 & 7) * 16, 0, LD_AUX));
      vp = __builtin_amdgcn_raw_buffer_load_b64(rsWS, ex + 512 + lane * 8, 0, LD_AUX);
      if (wid == 0 && lane == 0) flag_store(ws, WS_EH + ((rt - 1) * 8 + quad) * 128, tagG);
    } else {
      Kp[0] = Kf[0]; Kp[1] = Kf[1]; vp = (u32x2){0u, 0u};
    }
    STAMP(5);
    STAMP(6);
    const bool noprev = (rt == 0 && sp == 0);
    f32x16 bias;
#pragma unroll
    for (int r = 0; r < 16; ++r) bias[r] = ((unsigned)(i32 - ((r & 3) + 8 * (r >> 2) + 4 * h)) <= 7u) ? 0.f : -1e30f;
    float bprev[4];
#pragma unroll
    for (int j = 0; j < 4; ++j) bprev[j] = (!noprev && (24 + 4 * h + j >= i32 + 25)) ? 0.f : -1e30f;
    constexpr float C1 = 0.25f * LOG2E;
    const unsigned ONE2 = 0x3F803F80u;
    const f32x16 zero = {};
#pragma unroll
    for (int hh = 0; hh < 2; ++hh) {
      f32x16 sc = MFMA(Kf[hh], Qf[hh], bias);
      const f32x16 sv = MFMA(Kp[hh], Qf[hh], zero);
      float sp4[4];
#pragma unroll
      for (int j = 0; j < 4; ++j) sp4[j] = sv[12 + j] + bprev[j];
      float m = __builtin_fmaxf(__builtin_fmaxf(sp4[0], sp4[1]), __builtin_fmaxf(sp4[2], sp4[3]));
#pragma unroll
      for (int r = 0; r < 16; r += 2) m = __builtin_fmaxf(__builtin_fmaxf(m, sc[r]), sc[r + 1]);
      m = swap_max(m);
      const float nm = -m * C1;
#pragma unroll
      for (int r = 0; r < 16; ++r) sc[r] = __builtin_amdgcn_exp2f(__builtin_fmaf(sc[r], C1, nm));
#pragma unroll
      for (int j = 0; j < 4; ++j) sp4[j] = __builtin_amdgcn_exp2f(__builtin_fmaf(sp4[j], C1, nm));
      const bf16x8 P0 = pack8(sc, 0), P1 = pack8(sc, 8);
      const u32x4 ppv = {0u, 0u, pk(sp4[0], sp4[1]), pk(sp4[2], sp4[3])};
      const bool mine = (i32 < 16) == (hh == 0);
      const u32x4 v0 = __builtin_bit_cast(u32x4, Vf[0]), v1 = __builtin_bit_cast(u32x4, Vf[1]);
      u32x4 a0, a1, ap;
#pragma unroll
      for (int j = 0; j < 4; ++j) { a0[j] = mine ? v0[j] : ONE2; a1[j] = mine ? v1[j] : ONE2; }
      ap = (u32x4){0u, 0u, mine ? vp[0] : ONE2, mine ? vp[1] : ONE2};
      f32x16 o = MFMA(__builtin_bit_cast(bf16x8, a0), P0, zero);
      o = MFMA(__builtin_bit_cast(bf16x8, a1), P1, o);
      o = MFMA(__builtin_bit_cast(bf16x8, ap), __builtin_bit_cast(bf16x8, ppv), o);
      const int nb = hh == 0 ? 0 : 8, db = hh == 0 ? 8 : 0;
      const float s = __builtin_amdgcn_rcpf(o[db]);
      const u32x4 ov = {pk(o[nb] * s, o[nb + 1] * s), pk(o[nb + 2] * s, o[nb + 3] * s), pk(o[nb + 4] * s, o[nb + 5] * s), pk(o[nb + 6] * s, o[nb + 7] * s)};
      __builtin_amdgcn_raw_buffer_store_b128(ov, rsWS, ofrag + hh * 1024, 0, 16);
    }
  }
  STAMP(7);
  WAIT_VM(0);
  if (lane == 0) flag_store(ws, WS_FO + (((((rt * 2 + sp) * 2 + p) * 2 + BR) * 8 + quad) * 2 + c) * 64, tagO);
  STAMP(8);
  __builtin_amdgcn_s_setprio(0);
  if (c == BR) {
    if (BR == 1) lds_wait(L + L_CNT, 8);
#pragma unroll
    for (int i = 0; i < 16; ++i) wo_piece(1, rq * 16 + i);
    STAMP(18);
  }
  if (BR == 1 && wid >= 6) gate([&](int) LAM {});
}

__global__ __launch_bounds__(512, 2) void gated_mix_kernel(const __hip_bfloat16* __restrict__ x, const __hip_bfloat16* __restrict__ wA,
                                                           const __hip_bfloat16* __restrict__ woA, const __hip_bfloat16* __restrict__ wB,
                                                           const __hip_bfloat16* __restrict__ woB, const __hip_bfloat16* __restrict__ gw,
                                                           __hip_bfloat16* __restrict__ out, char* ws) {
  __shared__ __attribute__((aligned(1024))) char smem[LDS_BYTES];
  const int tid = threadIdx.x, lane = tid & 63, wid = __builtin_amdgcn_readfirstlane(tid >> 6);
  const int b = blockIdx.x, xcd = b & 7, loc = b >> 3;
#define XMAP 0
  const int br = XMAP ? (xcd >> 2) : (xcd & 1), rt = (XMAP ? (xcd & 3) : (xcd >> 1)) * 4 + (loc >> 3), quad = loc & 7;
  unsigned vts = 0;
  STAMP(0);
  const rsrc_t rsX = __builtin_amdgcn_make_buffer_rsrc((void*)x, 0, 2048 * 512 * 2, 0x00020000);
  const rsrc_t rsW = __builtin_amdgcn_make_buffer_rsrc((void*)(br ? wB : wA), 0, 512 * 1536 * 2, 0x00020000);
  const rsrc_t rsWoA = __builtin_amdgcn_make_buffer_rsrc((void*)woA, 0, 512 * 512 * 2, 0x00020000);
  const rsrc_t rsWoB = __builtin_amdgcn_make_buffer_rsrc((void*)woB, 0, 512 * 512 * 2, 0x00020000);
  const rsrc_t rsG = __builtin_amdgcn_make_buffer_rsrc((void*)gw, 0, 1024, 0x00020000);
  const rsrc_t rsWS = __builtin_amdgcn_make_buffer_rsrc((void*)ws, 0, WS_USED, 0x00020000);
  const lptr L = (lptr)smem;
  const unsigned lds0 = (unsigned)(uintptr_t)L;

  const int l8 = lane >> 3, c8 = lane & 7;
  const int xrow = rt * 128 + 64 * (wid >> 2) + ((wid >> 1) & 1) + 2 * (16 * (wid & 1) + l8);
  const unsigned vX0 = xrow * 1024 + ((c8 ^ (lane >> 4)) << 4);
  const unsigned vX1 = (xrow + 16) * 1024 + ((c8 ^ (4 + (lane >> 4))) << 4);
  const int wsw = (c8 ^ (((lane >> 4) & 1) << 2)) << 4;
  const unsigned vW = (8 * wid + l8) * 3072 + quad * 128 + wsw;
  const unsigned vWo = l8 * 1024 + quad * 128 + wsw;

  asm volatile("s_nop 4" ::: "memory");
#pragma unroll
  for (int f = 0; f < 2; ++f) {
    const unsigned sl = lds0 + f * SLOT;
    const unsigned kc = (f + ROT_OF(quad, rt)) & 7;
    dma16(sl + wid * 2048, vX0, rsX, kc * 128);
    dma16(sl + wid * 2048 + 1024, vX1, rsX, kc * 128);
    dma16(sl + 16384 + wid * 1024, vW, rsW, kc * 196608 + 1024);
    dma16(sl + 24576 + wid * 1024, vW, rsW, kc * 196608 + 2048);
    if (f < 1) asm volatile("s_barrier" ::: "memory");
  }
  STAMP(1);
  u64 tagO;
  bf16x8 wo[32];
  if (br == 0) phase1<0>(smem, rsX, rsW, rsWoA, rsWoB, rsG, rsWS, ws, rt, quad, loc, wid, lane, vX0, vX1, vW, vWo, vts, tagO, wo);
  else phase1<1>(smem, rsX, rsW, rsWoA, rsWoB, rsG, rsWS, ws, rt, quad, loc, wid, lane, vX0, vX1, vW, vWo, vts, tagO, wo);

  const int pt = wid & 1, kq = (wid >> 2) * 2 + ((wid >> 1) & 1);
  const int i32 = lane & 31, h = lane >> 5;
  const unsigned fwait = WS_FO + (((((rt * 2 + br) * 2 + pt) * 2 + (kq >> 1)) * 8 + (kq & 1) * 4) * 2) * 64;
  const unsigned ob = WS_O + ((((rt * 2 + br) * 2 + pt) * 64) + kq * 16) * 1024 + lane * 16;
  bf16x8 of[16];
  auto load_o = [&]() LAM {
#pragma unroll
    for (int i = 0; i < 16; ++i) of[i] = __builtin_bit_cast(bf16x8, __builtin_amdgcn_raw_buffer_load_b128(rsWS, ob, i * 1024, LD_AUX));
  };
  bool ready = false;
  {
    u64 v0 = tagO;
    if (lane < 8) v0 = __hip_atomic_load((gu64*)(ws + fwait + lane * 64), __ATOMIC_RELAXED, __HIP_MEMORY_SCOPE_AGENT);
    ready = __builtin_amdgcn_ballot_w64(v0 != tagO) == 0;
    WAIT_VM(0);
    if ((wid >> 2) == br && lane == 0) __hip_atomic_fetch_add((LAS unsigned*)(L + L_WOB), 1u, __ATOMIC_RELAXED, __HIP_MEMORY_SCOPE_WORKGROUP);
  }
  STAMP(26);
  if (ready) load_o();
  if (!(br == 0 && kq < 2)) {
    if (kq >= 2) lds_wait(L + L_WOB, 4); else lds_wait(L + L_CNT, 8);
    STAMP(19);
    read_wo(wo, L, kq, lane);
  }
  STAMP(9);
  if (!ready) {
    poll_flags(ws, fwait, 64, 8, tagO, lane);
    load_o();
  }
  STAMP(10);
  SBAR();
  f32x16 a0 = {}, a1 = {};
#pragma unroll
  for (int i = 0; i < 16; ++i) { a0 = MFMA(wo[2 * i], of[i], a0); a1 = MFMA(wo[2 * i + 1], of[i], a1); }
  STAMP(11);
  LBAR();
  STAMP(24);
  {
    const lptr pr = L + L_RED + (kq * 64 + pt * 32 + i32) * PROW + h * 16;
#pragma unroll
    for (int g4 = 0; g4 < 4; ++g4) {
      *(LAS f32x4*)(pr + g4 * 32) = (f32x4){a0[4 * g4], a0[4 * g4 + 1], a0[4 * g4 + 2], a0[4 * g4 + 3]};
      *(LAS f32x4*)(pr + 128 + g4 * 32) = (f32x4){a1[4 * g4], a1[4 * g4 + 1], a1[4 * g4 + 2], a1[4 * g4 + 3]};
    }
  }
  LBAR();
  STAMP(13);
  {
    const int pos = tid >> 3, cg = tid & 7;
    const lptr pr = L + L_RED + pos * PROW + cg * 32;
    const float g = *(LAS float*)(L + L_G + pos * 4), gb = 1.f - g;
    const f32x4 s0 = (*(LAS f32x4*)pr + *(LAS f32x4*)(pr + 64 * PROW)) * g + (*(LAS f32x4*)(pr + 128 * PROW) + *(LAS f32x4*)(pr + 192 * PROW)) * gb;
    const f32x4 s1 = (*(LAS f32x4*)(pr + 16) + *(LAS f32x4*)(pr + 64 * PROW + 16)) * g + (*(LAS f32x4*)(pr + 128 * PROW + 16) + *(LAS f32x4*)(pr + 192 * PROW + 16)) * gb;
    const int row = rt * 128 + 64 * br + 2 * (pos & 31) + (pos >> 5);
    const u32x4 ov = {pk(s0[0], s0[1]), pk(s0[2], s0[3]), pk(s1[0], s1[1]), pk(s1[2], s1[3])};
    __builtin_amdgcn_raw_buffer_store_b128(ov, __builtin_amdgcn_make_buffer_rsrc((void*)out, 0, 2048 * 1024, 0x00020000), row * 1024 + quad * 128 + cg * 16, 0, 0);
  }
  if (quad == 0 && br == 0 && tid == 0) flag_store(ws, WS_EO + rt * 128, tagO);
  STAMP(12);
  TSDUMP();
}

#if NOPK && defined(__HIP_DEVICE_COMPILE__)
#pragma clang attribute pop
#endif

extern "C" void kernel_launch(void* const* d_in, const int* in_sizes, int n_in, void* d_out, int out_size, void* d_ws, size_t ws_size,
                              hipStream_t stream) {
  gated_mix_kernel<<<256, 512, 0, stream>>>((const __hip_bfloat16*)d_in[0], (const __hip_bfloat16*)d_in[1], (const __hip_bfloat16*)d_in[2],
                                            (const __hip_bfloat16*)d_in[4], (const __hip_bfloat16*)d_in[5], (const __hip_bfloat16*)d_in[6],
                                            (__hip_bfloat16*)d_out, (char*)d_ws);
}
```
